```python
import torch

def _softmax(x, axis=-1):
    return torch.softmax(x, dim=axis)
def _gelu(x):
    return torch.nn.functional.gelu(x, approximate="tanh")
def _silu(x):
    return torch.nn.functional.silu(x)
def _sigmoid(x):
    return torch.sigmoid(x)
def _layernorm(x, axis=-1, eps=1e-5):
    m = x.mean(dim=axis, keepdim=True)
    v = x.var(dim=axis, keepdim=True, unbiased=False)
    return (x - m) / torch.sqrt(v + eps)
def _scatter_add(tgt, idx, src, axis=0):
    return tgt.clone().index_add_(axis, idx, src)
def _segment_sum(src, seg, n):
    out = torch.zeros((n,) + src.shape[1:], dtype=src.dtype)
    return out.index_add_(0, seg, src)
def _pad(x, pw):
    flat = tuple(v for pair in reversed(pw) for v in pair)
    return torch.nn.functional.pad(x, flat)

kernel_name = 'gated_mix__softmax_full_mha__dense_activation_silu__prompt'

def setup_inputs():

    _w = (lambda a: (lambda t: t.to(torch.bfloat16) if t.is_floating_point() else t)(torch.as_tensor(a)))
    def _A_setup():
        B, S, D = 1, 2048, 448
        return {
            "x": _w((lambda *s: torch.randn(*s, dtype=torch.bfloat16) * 0.05)(B, S, D)),
            "wqkv": _w((lambda *s: torch.randn(*s, dtype=torch.bfloat16) * 0.8)(D, 3 * D)),
            "wo": _w((lambda *s: torch.randn(*s, dtype=torch.bfloat16) * 0.05)(D, D)),
        }
    _A_in = _A_setup()

    def _B_setup():
        B, S, D, F = 1, 2048, 448, 1792
        return {
            "x": _w((lambda *s: torch.randn(*s, dtype=torch.bfloat16) * 0.05)(B, S, D)),
            "w1": _w((lambda *s: torch.randn(*s, dtype=torch.bfloat16) * 3.2)(D, F) * (D ** -0.5)),
            "w2": _w((lambda *s: torch.randn(*s, dtype=torch.bfloat16) * 0.8)(F, D) * (F ** -0.5)),
        }
    _B_in = _B_setup()

    return {
        "A_x": _A_in["x"],
        "A_wqkv": _A_in["wqkv"],
        "A_wo": _A_in["wo"],
        "B_x": _B_in["x"],
        "B_w1": _B_in["w1"],
        "B_w2": _B_in["w2"],
        "gate_w": _w((lambda *s: torch.randn(*s, dtype=torch.bfloat16) * 0.05)(448) * 16.0),
    }

def reference(A_x, A_wqkv, A_wo, B_x, B_w1, B_w2, gate_w):
    def _A_ref(x, wqkv, wo):
        B, S, D = x.shape
        H = 16; Dh = D // H
        qkv = (lambda x, *d: x.permute(*d))((x @ wqkv).reshape(B, S, 3, H, Dh), 2, 0, 3, 1, 4)
        q, k, v = qkv[0], qkv[1], qkv[2]
        scale = Dh ** -0.5
        scores = torch.einsum("bhqd,bhkd->bhqk", q, k) * scale
        p = _softmax(scores, axis=-1)
        o = torch.einsum("bhqk,bhkd->bhqd", p, v)
        return (lambda x, *d: x.permute(*d))(o, 0, 2, 1, 3).reshape(B, S, D) @ wo

    def _B_ref(x, w1, w2):
        h = torch.einsum("bsd,df->bsf", x, w1)
        h = _silu(h)
        out = torch.einsum("bsf,fd->bsd", h, w2)
        return out

    x = A_x
    g = _sigmoid((x * gate_w).sum(-1, keepdims=True))
    a = _A_ref(x, A_wqkv, A_wo)
    b = _B_ref(x, B_w1, B_w2)
    return g * a + (1 - g) * b
```

```cpp
#include <hip/hip_runtime.h>
#include <hip/hip_bf16.h>
#include <stdint.h>

typedef short bf16x8 __attribute__((ext_vector_type(8)));
typedef short s16x4 __attribute__((ext_vector_type(4)));
typedef float f32x16 __attribute__((ext_vector_type(16)));
typedef float f32x4 __attribute__((ext_vector_type(4)));
typedef unsigned u32x4 __attribute__((ext_vector_type(4)));
typedef unsigned u32x2 __attribute__((ext_vector_type(2)));
#define LAS __attribute__((address_space(3)))
#define DI __device__ __forceinline__
#define MFMA(a, b, c) __builtin_amdgcn_mfma_f32_32x32x16_bf16((a), (b), (c), 0, 0, 0)
#define SBAR() __builtin_amdgcn_sched_barrier(0)

constexpr int SEQ = 2048, DM = 448, NH = 16, DH = 28, FF = 1792;
constexpr unsigned QF_OFF = 0;
constexpr unsigned KV_OFF = QF_OFF + 16 * 64 * 2048;
constexpr unsigned HF_OFF = KV_OFF + 16 * 64 * 4096;
constexpr unsigned W2F_OFF = HF_OFF + 64 * 56 * 2048;
constexpr unsigned WOF_OFF = W2F_OFF + 14 * 56 * 2048;
constexpr unsigned OF_OFF = WOF_OFF + 14 * 16 * 2048;
constexpr unsigned KN_OFF = OF_OFF + 64 * 16 * 2048;
constexpr unsigned G_OFF = KN_OFF + 16 * 64 * 4;
constexpr unsigned B_OFF = G_OFF + 2048 * 4;
constexpr unsigned WS_END = B_OFF + 224 * 4 * 4096;

constexpr int LDS_BYTES = 3 * 45056;
constexpr float QSCALE = 0.18898223650461362f * 1.4426950408889634f;
constexpr float LOG2E = 1.4426950408889634f;
constexpr float SAFE_BOUND = 60.0f;

struct Params {
  const unsigned short* x;
  const unsigned short* wqkv;
  const unsigned short* wo;
  const unsigned short* w1;
  const unsigned short* w2;
  const unsigned short* gw;
  unsigned short* out;
  unsigned char* ws;
};

DI unsigned cvtpk(float lo, float hi) {
  typedef __bf16 bf2 __attribute__((ext_vector_type(2)));
  typedef float f2 __attribute__((ext_vector_type(2)));
  f2 v = {lo, hi};
  bf2 r = __builtin_convertvector(v, bf2);
  return __builtin_bit_cast(unsigned, r);
}
DI bf16x8 pack8(const f32x16& x, int s) {
  u32x4 p;
  p[0] = cvtpk(x[8 * s + 0], x[8 * s + 1]);
  p[1] = cvtpk(x[8 * s + 2], x[8 * s + 3]);
  p[2] = cvtpk(x[8 * s + 4], x[8 * s + 5]);
  p[3] = cvtpk(x[8 * s + 6], x[8 * s + 7]);
  return __builtin_bit_cast(bf16x8, p);
}
DI float swap_half(float v) {
  auto r = __builtin_amdgcn_permlane32_swap(__float_as_uint(v), __float_as_uint(v), false, false);
  return (threadIdx.x & 32) ? __uint_as_float(r[0]) : __uint_as_float(r[1]);
}
template <int CTRL> DI float dpp_mov(float v) {
  return __uint_as_float(__builtin_amdgcn_update_dpp(__float_as_uint(v), __float_as_uint(v), CTRL, 0xf, 0xf, false));
}
DI float row16_max(float v) {
  v = fmaxf(v, dpp_mov<0x128>(v));
  v = fmaxf(v, dpp_mov<0x124>(v));
  v = fmaxf(v, dpp_mov<0x122>(v));
  v = fmaxf(v, dpp_mov<0x121>(v));
  return v;
}
DI float rdlane(float v, int l) { return __uint_as_float(__builtin_amdgcn_readlane(__float_as_uint(v), l)); }
DI s16x4 tr_read(const LAS unsigned char* p) {
  typedef short v4i16_t __attribute__((ext_vector_type(4)));
  return __builtin_bit_cast(s16x4, __builtin_amdgcn_ds_read_tr16_b64_v4i16((LAS v4i16_t*)p));
}
DI bf16x8 tr_frag(const LAS unsigned char* p, int row_stride4) {
  s16x4 lo = tr_read(p), hi = tr_read(p + row_stride4);
  return __builtin_shufflevector(lo, hi, 0, 1, 2, 3, 4, 5, 6, 7);
}
DI __amdgpu_buffer_rsrc_t mk_rsrc(const void* p, unsigned bytes) {
  return __builtin_amdgcn_make_buffer_rsrc((void*)p, (short)0, (int)bytes, 0x00020000);
}
DI void glds16(__amdgpu_buffer_rsrc_t rs, unsigned dst, unsigned voff, unsigned soff) {
  asm volatile("s_mov_b32 m0, %0\n\ts_nop 0\n\tbuffer_load_dwordx4 %1, %2, %3 offen lds" ::"s"(dst), "v"(voff), "s"(rs), "s"(soff) : "memory");
}
DI u32x4 ld16(__amdgpu_buffer_rsrc_t rs, unsigned voff, unsigned soff) { return __builtin_amdgcn_raw_buffer_load_b128(rs, voff, soff, 0); }
DI void st16(__amdgpu_buffer_rsrc_t rs, u32x4 v, unsigned voff, unsigned soff) { __builtin_amdgcn_raw_buffer_store_b128(v, rs, voff + soff, 0, 0); }
DI bf16x8 as_frag(u32x4 v) { return __builtin_bit_cast(bf16x8, v); }
DI u32x4 as_u4(bf16x8 v) { return __builtin_bit_cast(u32x4, v); }
template <int N> DI void wait_bar() { asm volatile("s_waitcnt vmcnt(%0) lgkmcnt(0)\n\ts_barrier" ::"n"(N) : "memory"); }

constexpr int SLOT1 = 45056;
template <int ROLE>
DI void phase1_role(const Params& P, LAS unsigned char* lds, const int w, const int rb, const int j) {
  const int tid = threadIdx.x, lane = tid & 63, r = lane & 31, h = lane >> 5;
  const int wt = w & 3;
  const __amdgpu_buffer_rsrc_t rsX = mk_rsrc(P.x, SEQ * DM * 2);
  const __amdgpu_buffer_rsrc_t rsW = ROLE == 0 ? mk_rsrc(P.wqkv, DM * 3 * DM * 2) : mk_rsrc(P.w1, DM * FF * 2);
  const __amdgpu_buffer_rsrc_t rsWs = mk_rsrc(P.ws, WS_END);
  const unsigned lds0 = (unsigned)(uintptr_t)lds;
  constexpr int NW = ROLE == 0 ? 3 : ROLE == 1 ? 4 : 0;

  unsigned xsrc[2], wsrc[4];
#pragma unroll
  for (int i = 0; i < 2; ++i) {
    const int row = 8 * (2 * w + i) + (lane >> 3), ch = (lane & 7) ^ ((row >> 1) & 7);
    xsrc[i] = (unsigned)((rb * 128 + row) * (DM * 2) + ch * 16);
  }
  if (ROLE == 0) {
#pragma unroll
    for (int i = 0; i < 3; ++i) {
      const int p = 64 * (3 * wt + i) + lane, kr = p / 12, ch = p % 12;
      wsrc[i] = (unsigned)(kr * (3 * DM * 2) + (ch >> 2) * (DM * 2) + j * (DH * 2) + (ch & 3) * 16);
    }
  } else if (ROLE == 1) {
#pragma unroll
    for (int i = 0; i < 4; ++i) {
      const int row = 4 * (4 * wt + i) + (lane >> 4), pos = lane & 15, seg = (pos >> 2) ^ (row & 3);
      wsrc[i] = (unsigned)(row * (FF * 2) + j * 256 + seg * 64 + (pos & 3) * 16);
    }
  }
  auto issue = [&](int c) {
    const unsigned slot = lds0 + (c % 3) * SLOT1;
    glds16(rsX, slot + (2 * w) * 1024, xsrc[0], c * 128);
    glds16(rsX, slot + (2 * w + 1) * 1024, xsrc[1], c * 128);
#pragma unroll
    for (int i = 0; i < NW; ++i)
      glds16(rsW, slot + (ROLE == 0 ? 16384 : 28672) + (NW * wt + i) * 1024, wsrc[i], c * 64 * (ROLE == 0 ? 3 * DM * 2 : FF * 2));
  };
  issue(0);
  issue(1);
  issue(2);

  unsigned rp[16];
  const int gw = blockIdx.x * 4 + wt;
  if (ROLE == 0) {
    const bool isw2 = gw < 784;
    const int id = isw2 ? gw : gw - 784;
    const int nt = isw2 ? id / 56 : (id >> 4), row0 = isw2 ? 32 * (id % 56) : DH * (id & 15);
    const __amdgpu_buffer_rsrc_t rsR = isw2 ? mk_rsrc(P.w2, FF * DM * 2) : mk_rsrc(P.wo, gw < 1008 ? DM * DM * 2 : 0);
    const unsigned vo = (unsigned)((4 * h * DM + r) * 2), so = (unsigned)((row0 * DM + 32 * nt) * 2);
#pragma unroll
    for (int e = 0; e < 16; ++e) rp[e] = __builtin_amdgcn_raw_buffer_load_b16(rsR, vo + (8 * (e >> 2) + (e & 3)) * DM * 2, so, 0);
  }
  if (ROLE == 2) {
    if (wt == 0 && lane < 56) {
      const u32x4 g = *(const u32x4*)(P.gw + lane * 8);
      *(LAS u32x4*)(lds + 28672 + lane * 16) = g;
    }
  }

  unsigned xoff[4];
#pragma unroll
  for (int ks = 0; ks < 4; ++ks) xoff[ks] = (unsigned)(wt * 4096 + r * 128 + (((2 * ks + h) ^ ((r >> 1) & 7)) << 4));
  const int blk = (lane >> 4) & 1, q4 = (lane & 15) >> 2, p4 = lane & 3;
  const unsigned wqoff = (unsigned)(16384 + (8 * h + q4) * 192 + blk * 32 + p4 * 8);
  unsigned w1off[4];
#pragma unroll
  for (int ct = 0; ct < 4; ++ct) w1off[ct] = (unsigned)(28672 + (8 * h + q4) * 256 + ((ct ^ q4) * 64) + blk * 32 + p4 * 8);

  f32x16 acc[4];
#pragma unroll
  for (int i = 0; i < 4; ++i) acc[i] = f32x16{};

#pragma unroll
  for (int c = 0; c < 7; ++c) {
    constexpr int PER = 2 + NW, EX = ROLE == 0 ? 16 : 0;
    if (c == 0) wait_bar<2 * PER + EX>();
    else if (c == 1) wait_bar<PER + EX>();
    else if (c == 2) wait_bar<PER + EX>();
    else if (c < 6) wait_bar<PER>();
    else wait_bar<0>();
    if (c >= 1 && c + 2 < 7) issue(c + 2);
    const LAS unsigned char* slot = lds + (c % 3) * SLOT1;
    if (ROLE == 0) {
#pragma unroll
      for (int ks = 0; ks < 4; ++ks) {
        const bf16x8 xf = *(const LAS bf16x8*)(slot + xoff[ks]);
        const bf16x8 wq = tr_frag(slot + wqoff + ks * 16 * 192, 4 * 192);
        const bf16x8 wk = tr_frag(slot + wqoff + ks * 16 * 192 + 64, 4 * 192);
        const bf16x8 wv = tr_frag(slot + wqoff + ks * 16 * 192 + 128, 4 * 192);
        acc[0] = MFMA(wq, xf, acc[0]);
        acc[1] = MFMA(wk, xf, acc[1]);
        acc[2] = MFMA(xf, wv, acc[2]);
      }
    } else if (ROLE == 1) {
#pragma unroll
      for (int ks = 0; ks < 4; ++ks) {
        const bf16x8 xf = *(const LAS bf16x8*)(slot + xoff[ks]);
#pragma unroll
        for (int ct = 0; ct < 4; ++ct) {
          const bf16x8 wf = tr_frag(slot + w1off[ct] + ks * 16 * 256, 4 * 256);
          acc[ct] = MFMA(wf, xf, acc[ct]);
        }
      }
    } else if (ROLE == 2) {
#pragma unroll
      for (int ks = 0; ks < 4; ++ks) {
        const bf16x8 xf = *(const LAS bf16x8*)(slot + xoff[ks]);
        const bf16x8 gf = *(const LAS bf16x8*)(lds + 28672 + (c * 64 + ks * 16 + 8 * h) * 2);
        acc[0] = MFMA(gf, xf, acc[0]);
      }
    }
  }

  const int rt = rb * 4 + wt;
  if (ROLE == 0) {
    f32x16 q = acc[0], k = acc[1], v = acc[2];
    float qs = 0.f, ks2 = 0.f;
#pragma unroll
    for (int i = 0; i < 16; ++i) {
      q[i] *= QSCALE;
      if (i >= 12) { q[i] = h ? 0.f : q[i]; k[i] = h ? 0.f : k[i]; }
      qs = fmaf(q[i], q[i], qs);
      ks2 = fmaf(k[i], k[i], ks2);
    }
    qs += swap_half(qs);
    ks2 += swap_half(ks2);
    if (h) { q[12] = __builtin_sqrtf(qs); k[12] = 1.f; k[13] = 1.f; k[14] = 1.f; }
    ks2 = row16_max(ks2);
    const float kmax2 = fmaxf(rdlane(ks2, 0), rdlane(ks2, 16));
#pragma unroll
    for (int i = 0; i < 16; ++i) v[i] = (r < DH) ? v[i] : (r == DH) ? 1.f : 0.f;
    const unsigned lo = lane * 16;
    st16(rsWs, as_u4(pack8(k, 0)), lo, KV_OFF + (j * 64 + rt) * 4096);
    st16(rsWs, as_u4(pack8(k, 1)), lo + 1024, KV_OFF + (j * 64 + rt) * 4096);
    st16(rsWs, as_u4(pack8(v, 0)), lo + 2048, KV_OFF + (j * 64 + rt) * 4096);
    st16(rsWs, as_u4(pack8(v, 1)), lo + 3072, KV_OFF + (j * 64 + rt) * 4096);
    st16(rsWs, as_u4(pack8(q, 0)), lo, QF_OFF + (j * 64 + rt) * 2048);
    st16(rsWs, as_u4(pack8(q, 1)), lo + 1024, QF_OFF + (j * 64 + rt) * 2048);
    if (lane == 0) *(float*)(P.ws + KN_OFF + (j * 64 + rt) * 4) = kmax2;
    if (gw < 1008) {
#pragma unroll
      for (int e = 0; e < 16; ++e) asm volatile("" : "+v"(rp[e]));
      if (gw >= 784 && h) { rp[12] = 0; rp[13] = 0; rp[14] = 0; rp[15] = 0; }
      u32x4 a, c2;
#pragma unroll
      for (int i = 0; i < 4; ++i) {
        a[i] = (rp[2 * i] & 0xffffu) | (rp[2 * i + 1] << 16);
        c2[i] = (rp[8 + 2 * i] & 0xffffu) | (rp[8 + 2 * i + 1] << 16);
      }
      st16(rsWs, a, lo, W2F_OFF + gw * 2048);
      st16(rsWs, c2, lo + 1024, W2F_OFF + gw * 2048);
    }
  } else if (ROLE == 1) {
#pragma unroll
    for (int ct = 0; ct < 4; ++ct) {
      f32x16 z = acc[ct];
#pragma unroll
      for (int i = 0; i < 16; ++i) z[i] = z[i] * __builtin_amdgcn_rcpf(1.f + __builtin_amdgcn_exp2f(-LOG2E * z[i]));
      const unsigned so = HF_OFF + (rt * 56 + j * 4 + ct) * 2048;
      st16(rsWs, as_u4(pack8(z, 0)), lane * 16, so);
      st16(rsWs, as_u4(pack8(z, 1)), lane * 16 + 1024, so);
    }
  } else if (ROLE == 2) {
    const float g = __builtin_amdgcn_rcpf(1.f + __builtin_amdgcn_exp2f(-LOG2E * acc[0][0]));
    if (h == 0) *(float*)(P.ws + G_OFF + (rt * 32 + r) * 4) = g;
  }
}

DI void phase1(const Params& P, LAS unsigned char* lds) {
  const int w = __builtin_amdgcn_readfirstlane(threadIdx.x >> 6);
  const int b = blockIdx.x, xcd = b & 7, idx = b >> 3;
  const int rb = (xcd & 1) * 8 + (idx & 7), j = (xcd >> 1) * 4 + (idx >> 3);
  if (w < 4) phase1_role<0>(P, lds, w, rb, j);
  else if (j < 14) phase1_role<1>(P, lds, w, rb, j);
  else if (j == 14) phase1_role<2>(P, lds, w, rb, j);
  else phase1_role<3>(P, lds, w, rb, j);
}

DI void phase2(const Params& P, LAS unsigned char* lds) {
  const int tid = threadIdx.x, lane = tid & 63, h = lane >> 5;
  const int w = __builtin_amdgcn_readfirstlane(tid >> 6);
  const int b = blockIdx.x, xcd = b & 7, idx = b >> 3;
  const __amdgpu_buffer_rsrc_t rsWs = mk_rsrc(P.ws, WS_END);
  const unsigned lo = lane * 16;
  const int wt = w & 3;
  f32x16 acc[4];
#pragma unroll
  for (int i = 0; i < 4; ++i) acc[i] = f32x16{};
  const int head = xcd * 2 + (idx & 1), qb = idx >> 1;
  const bool has_tile = idx < 28;
  const int mb = 4 * xcd + (idx & 3), nb = idx >> 2;

  if (w < 4) {
    u32x4 q[4][2];
#pragma unroll
    for (int t = 0; t < 4; ++t) {
      q[t][0] = ld16(rsWs, lo, QF_OFF + (head * 64 + qb * 4 + t) * 2048);
      q[t][1] = ld16(rsWs, lo + 1024, QF_OFF + (head * 64 + qb * 4 + t) * 2048);
    }
    const unsigned kvbase = KV_OFF + (head * 64 + wt * 16) * 4096;
    u32x4 kv[4][4];
#pragma unroll
    for (int i = 0; i < 3; ++i)
#pragma unroll
      for (int e = 0; e < 4; ++e) kv[i][e] = ld16(rsWs, lo + e * 1024, kvbase + i * 4096);
    float kn = *(const float*)(P.ws + KN_OFF + (head * 64 + lane) * 4);
    kn = row16_max(kn);
    const float kmax = __builtin_sqrtf(fmaxf(fmaxf(rdlane(kn, 0), rdlane(kn, 16)), fmaxf(rdlane(kn, 32), rdlane(kn, 48))));
    float m[4];
    bool unsafe = false;
#pragma unroll
    for (int t = 0; t < 4; ++t) {
      m[t] = __uint_as_float(q[t][1][2] << 16) * kmax;
      unsafe |= h && !(m[t] <= SAFE_BOUND);
    }
    if (__builtin_expect(__any(unsafe), 0)) {
      float mx[4];
#pragma unroll
      for (int t = 0; t < 4; ++t) { mx[t] = -INFINITY; if (h) { q[t][1][2] = 0; q[t][1][3] = 0; } }
      for (int kt = 0; kt < 64; ++kt) {
        const u32x4 k0 = ld16(rsWs, lo, KV_OFF + (head * 64 + kt) * 4096), k1 = ld16(rsWs, lo + 1024, KV_OFF + (head * 64 + kt) * 4096);
#pragma unroll
        for (int t = 0; t < 4; ++t) {
          f32x16 s = MFMA(as_frag(k0), as_frag(q[t][0]), f32x16{});
          s = MFMA(as_frag(k1), as_frag(q[t][1]), s);
#pragma unroll
          for (int i = 0; i < 16; ++i) mx[t] = fmaxf(mx[t], s[i]);
        }
      }
#pragma unroll
      for (int t = 0; t < 4; ++t) m[t] = fmaxf(mx[t], swap_half(mx[t]));
    }
#pragma unroll
    for (int t = 0; t < 4; ++t) {
      const float nm = -m[t];
      const unsigned hi = __float_as_uint(nm) & 0xffff0000u;
      const float r1 = nm - __uint_as_float(hi);
      const unsigned mid = __float_as_uint(r1) & 0xffff0000u;
      const float r2 = r1 - __uint_as_float(mid);
      if (h) { q[t][1][2] = (hi >> 16) | mid; q[t][1][3] = __float_as_uint(r2) >> 16; }
    }
#pragma unroll
    for (int kt = 0; kt < 16; ++kt) {
      if (kt + 3 < 16) {
#pragma unroll
        for (int e = 0; e < 4; ++e) kv[(kt + 3) & 3][e] = ld16(rsWs, lo + e * 1024, kvbase + (kt + 3) * 4096);
      }
#pragma unroll
      for (int t = 0; t < 4; ++t) {
        f32x16 s = MFMA(as_frag(kv[kt & 3][0]), as_frag(q[t][0]), f32x16{});
        s = MFMA(as_frag(kv[kt & 3][1]), as_frag(q[t][1]), s);
#pragma unroll
        for (int i = 0; i < 16; ++i) s[i] = __builtin_amdgcn_exp2f(s[i]);
        acc[t] = MFMA(as_frag(kv[kt & 3][2]), pack8(s, 0), acc[t]);
        acc[t] = MFMA(as_frag(kv[kt & 3][3]), pack8(s, 1), acc[t]);
      }
    }
  } else if (has_tile) {
    u32x4 hb[3][4], wb[3][4];
    const unsigned hbase = HF_OFF + (mb * 2 * 56 + wt * 14) * 2048, wbase = W2F_OFF + (nb * 2 * 56 + wt * 14) * 2048;
    auto ldf = [&](int f) {
#pragma unroll
      for (int e = 0; e < 4; ++e) {
        hb[f % 3][e] = ld16(rsWs, lo + (e & 1) * 1024, hbase + ((e >> 1) * 56 + f) * 2048);
        wb[f % 3][e] = ld16(rsWs, lo + (e & 1) * 1024, wbase + ((e >> 1) * 56 + f) * 2048);
      }
    };
    ldf(0);
    ldf(1);
#pragma unroll
    for (int f = 0; f < 14; ++f) {
      if (f + 2 < 14) ldf(f + 2);
#pragma unroll
      for (int s = 0; s < 2; ++s)
#pragma unroll
        for (int nt = 0; nt < 2; ++nt)
#pragma unroll
          for (int mt = 0; mt < 2; ++mt)
            acc[nt * 2 + mt] = MFMA(as_frag(wb[f % 3][nt * 2 + s]), as_frag(hb[f % 3][mt * 2 + s]), acc[nt * 2 + mt]);
    }
  }
  LAS unsigned char* red = lds + (w >> 2) * 65536;
#pragma unroll
  for (int t = 0; t < 4; ++t) {
    if (t != wt) {
#pragma unroll
      for (int g = 0; g < 4; ++g) {
        f32x4 v = {acc[t][4 * g], acc[t][4 * g + 1], acc[t][4 * g + 2], acc[t][4 * g + 3]};
        *(LAS f32x4*)(red + ((wt * 4 + t) * 4 + g) * 1024 + lo) = v;
      }
    }
  }
  __syncthreads();
  f32x16 o = wt == 0 ? acc[0] : wt == 1 ? acc[1] : wt == 2 ? acc[2] : acc[3];
#pragma unroll
  for (int u = 1; u < 4; ++u) {
    const int src = (wt + u) & 3;
#pragma unroll
    for (int g = 0; g < 4; ++g) {
      const f32x4 v = *(const LAS f32x4*)(red + ((src * 4 + wt) * 4 + g) * 1024 + lo);
      o[4 * g] += v[0]; o[4 * g + 1] += v[1]; o[4 * g + 2] += v[2]; o[4 * g + 3] += v[3];
    }
  }
  if (w < 4) {
    const float lsw = swap_half(o[12]);
    const float inv = __builtin_amdgcn_rcpf(h ? o[12] : lsw);
#pragma unroll
    for (int i = 0; i < 16; ++i) o[i] = (i >= 12 && h) ? 0.f : o[i] * inv;
    const unsigned so = OF_OFF + ((qb * 4 + wt) * 16 + head) * 2048;
    st16(rsWs, as_u4(pack8(o, 0)), lo, so);
    st16(rsWs, as_u4(pack8(o, 1)), lo + 1024, so);
  } else if (has_tile) {
    const unsigned so = B_OFF + ((mb * 7 + nb) * 4 + wt) * 4096;
#pragma unroll
    for (int g = 0; g < 4; ++g) {
      f32x4 v = {o[4 * g], o[4 * g + 1], o[4 * g + 2], o[4 * g + 3]};
      st16(rsWs, __builtin_bit_cast(u32x4, v), lo + g * 1024, so);
    }
  }
}

DI void phase3(const Params& P, LAS unsigned char* lds) {
  const int tid = threadIdx.x, lane = tid & 63, r = lane & 31, h = lane >> 5;
  const int w = __builtin_amdgcn_readfirstlane(tid >> 6);
  const int b = blockIdx.x, xcd = b & 7, idx = b >> 3;
  if (idx >= 28) return;
  const int mb = 4 * xcd + (idx & 3), nb = idx >> 2;
  const __amdgpu_buffer_rsrc_t rsWs = mk_rsrc(P.ws, WS_END);
  const unsigned lo = lane * 16;
  const int mt = w & 1, kq = w >> 1;
  u32x4 of[8], wf[2][8];
#pragma unroll
  for (int e = 0; e < 8; ++e) {
    of[e] = ld16(rsWs, lo, OF_OFF + ((mb * 2 + mt) * 16 + kq * 4) * 2048 + e * 1024);
    wf[0][e] = ld16(rsWs, lo, WOF_OFF + ((nb * 2) * 16 + kq * 4) * 2048 + e * 1024);
    wf[1][e] = ld16(rsWs, lo, WOF_OFF + ((nb * 2 + 1) * 16 + kq * 4) * 2048 + e * 1024);
  }
  f32x16 bt;
  float g = 0.f;
  const int own = w - 4;
  if (w >= 4) {
#pragma unroll
    for (int q = 0; q < 4; ++q) {
      const f32x4 v = __builtin_bit_cast(f32x4, ld16(rsWs, lo + q * 1024, B_OFF + ((mb * 7 + nb) * 4 + own) * 4096));
      bt[4 * q] = v[0]; bt[4 * q + 1] = v[1]; bt[4 * q + 2] = v[2]; bt[4 * q + 3] = v[3];
    }
    g = *(const float*)(P.ws + G_OFF + ((mb * 2 + mt) * 32 + r) * 4);
  }
  f32x16 acc[2];
  acc[0] = f32x16{};
  acc[1] = f32x16{};
#pragma unroll
  for (int e = 0; e < 8; ++e) {
    acc[0] = MFMA(as_frag(wf[0][e]), as_frag(of[e]), acc[0]);
    acc[1] = MFMA(as_frag(wf[1][e]), as_frag(of[e]), acc[1]);
  }
#pragma unroll
  for (int nt = 0; nt < 2; ++nt) {
    if (own != nt * 2 + mt) {
#pragma unroll
      for (int q = 0; q < 4; ++q) {
        f32x4 v = {acc[nt][4 * q], acc[nt][4 * q + 1], acc[nt][4 * q + 2], acc[nt][4 * q + 3]};
        *(LAS f32x4*)(lds + (((nt * 2 + mt) * 4 + kq) * 4 + q) * 1024 + lo) = v;
      }
    }
  }
  __syncthreads();
  if (w >= 4) {
    const int nt = own >> 1;
    f32x16 a = nt ? acc[1] : acc[0];
#pragma unroll
    for (int u = 1; u < 4; ++u) {
      const int src = (kq + u) & 3;
#pragma unroll
      for (int q = 0; q < 4; ++q) {
        const f32x4 v = *(const LAS f32x4*)(lds + ((own * 4 + src) * 4 + q) * 1024 + lo);
        a[4 * q] += v[0]; a[4 * q + 1] += v[1]; a[4 * q + 2] += v[2]; a[4 * q + 3] += v[3];
      }
    }
    const float og = 1.f - g;
    unsigned short* dst = P.out + (size_t)((mb * 2 + mt) * 32 + r) * DM + (nb * 2 + nt) * 32 + 4 * h;
#pragma unroll
    for (int q = 0; q < 4; ++q) {
      u32x2 v;
      v[0] = cvtpk(g * a[4 * q] + og * bt[4 * q], g * a[4 * q + 1] + og * bt[4 * q + 1]);
      v[1] = cvtpk(g * a[4 * q + 2] + og * bt[4 * q + 2], g * a[4 * q + 3] + og * bt[4 * q + 3]);
      *(u32x2*)(dst + 8 * q) = v;
    }
  }
}

extern __shared__ __attribute__((aligned(16))) unsigned char g_lds[];

__global__ __launch_bounds__(512, 2) void k_phase1(Params P) { phase1(P, (LAS unsigned char*)g_lds); }
__global__ __launch_bounds__(512, 2) void k_phase2(Params P) { phase2(P, (LAS unsigned char*)g_lds); }
__global__ __launch_bounds__(512, 2) void k_phase3(Params P) { phase3(P, (LAS unsigned char*)g_lds); }

extern "C" void kernel_launch(void* const* d_in, const int* in_sizes, int n_in, void* d_out, int out_size, void* d_ws,
                              size_t ws_size, hipStream_t stream) {
  Params P;
  P.x = (const unsigned short*)d_in[0];
  P.wqkv = (const unsigned short*)d_in[1];
  P.wo = (const unsigned short*)d_in[2];
  P.w1 = (const unsigned short*)d_in[4];
  P.w2 = (const unsigned short*)d_in[5];
  P.gw = (const unsigned short*)d_in[6];
  P.out = (unsigned short*)d_out;
  P.ws = (unsigned char*)d_ws;
  (void)hipFuncSetAttribute((const void*)k_phase1, hipFuncAttributeMaxDynamicSharedMemorySize, LDS_BYTES);
  (void)hipFuncSetAttribute((const void*)k_phase2, hipFuncAttributeMaxDynamicSharedMemorySize, 131072);
  (void)hipFuncSetAttribute((const void*)k_phase3, hipFuncAttributeMaxDynamicSharedMemorySize, 65536);
  k_phase1<<<256, 512, LDS_BYTES, stream>>>(P);
  k_phase2<<<256, 512, 131072, stream>>>(P);
  k_phase3<<<256, 512, 65536, stream>>>(P);
}
```

```cpp
#include <hip/hip_runtime.h>
#include <hip/hip_bf16.h>
#include <stdint.h>

typedef short bf16x8 __attribute__((ext_vector_type(8)));
typedef short s16x4 __attribute__((ext_vector_type(4)));
typedef float f32x16 __attribute__((ext_vector_type(16)));
typedef float f32x4 __attribute__((ext_vector_type(4)));
typedef unsigned u32x4 __attribute__((ext_vector_type(4)));
typedef unsigned u32x2 __attribute__((ext_vector_type(2)));
#define LAS __attribute__((address_space(3)))
#define DI __device__ __forceinline__
#define MFMA(a, b, c) __builtin_amdgcn_mfma_f32_32x32x16_bf16((a), (b), (c), 0, 0, 0)
#define SBAR() __builtin_amdgcn_sched_barrier(0)

constexpr int SEQ = 2048, DM = 448, NH = 16, DH = 28, FF = 1792;
constexpr unsigned FL_OFF = 0;
constexpr unsigned SET_BASE = 1u << 20, SET_STRIDE = 24u << 20;
constexpr unsigned GQ_OFF = 0;
constexpr unsigned GH_OFF = GQ_OFF + 256 * 128;
constexpr unsigned GO_OFF = GH_OFF + 256 * 128;
constexpr unsigned GW_OFF = GO_OFF + 1024 * 128;
constexpr unsigned GG_OFF = GW_OFF + 256 * 8;
constexpr unsigned QF_OFF = 262144;
constexpr unsigned KV_OFF = QF_OFF + 16 * 64 * 2048;
constexpr unsigned HF_OFF = KV_OFF + 16 * 64 * 4096;
constexpr unsigned WOF_OFF = HF_OFF + 64 * 56 * 2048;
constexpr unsigned OF_OFF = WOF_OFF + 14 * 16 * 2048;
constexpr unsigned WS_END = OF_OFF + 64 * 16 * 2048;
static_assert(GG_OFF + 2048 * 8 <= QF_OFF && WS_END <= SET_STRIDE, "workspace map");

constexpr int L_X = 0;
constexpr int L_WQ = 114688;
constexpr int L_W1 = 114688;
constexpr int L_CTRL = 163776;
constexpr int L_W2 = 0;
constexpr int L_CMB = 98304;
constexpr int L_FIN = 147456;
constexpr int LDS_BYTES = 163840;

constexpr float QSCALE = 0.18898223650461362f * 1.4426950408889634f;
constexpr float LOG2E = 1.4426950408889634f;
constexpr float SAFE_BOUND = 60.0f;
constexpr unsigned SPIN_CAP = 1u << 18;
#define ROT1(rb, j) (((rb) + 2 * (j)) % 7)
#define POLL_A 4

struct Params {
  const unsigned short* x;
  const unsigned short* wqkv;
  const unsigned short* wo;
  const unsigned short* w1;
  const unsigned short* w2;
  const unsigned short* gw;
  unsigned short* out;
  unsigned char* ws;
};
typedef unsigned long long u64;
typedef __attribute__((address_space(1))) u64 gu64;

DI unsigned cvtpk(float lo, float hi) {
  typedef __bf16 bf2 __attribute__((ext_vector_type(2)));
  typedef float f2 __attribute__((ext_vector_type(2)));
  f2 v = {lo, hi};
  bf2 r = __builtin_convertvector(v, bf2);
  return __builtin_bit_cast(unsigned, r);
}
DI bf16x8 pack8(const f32x16& x, int s) {
  u32x4 p;
  p[0] = cvtpk(x[8 * s + 0], x[8 * s + 1]);
  p[1] = cvtpk(x[8 * s + 2], x[8 * s + 3]);
  p[2] = cvtpk(x[8 * s + 4], x[8 * s + 5]);
  p[3] = cvtpk(x[8 * s + 6], x[8 * s + 7]);
  return __builtin_bit_cast(bf16x8, p);
}
DI float swap_half(float v) {
  auto r = __builtin_amdgcn_permlane32_swap(__float_as_uint(v), __float_as_uint(v), false, false);
  return (threadIdx.x & 32) ? __uint_as_float(r[0]) : __uint_as_float(r[1]);
}
template <int CTRL> DI float dpp_mov(float v) {
  return __uint_as_float(__builtin_amdgcn_update_dpp(__float_as_uint(v), __float_as_uint(v), CTRL, 0xf, 0xf, false));
}
DI float row16_max(float v) {
  v = fmaxf(v, dpp_mov<0x128>(v));
  v = fmaxf(v, dpp_mov<0x124>(v));
  v = fmaxf(v, dpp_mov<0x122>(v));
  v = fmaxf(v, dpp_mov<0x121>(v));
  return v;
}
DI float rdlane(float v, int l) { return __uint_as_float(__builtin_amdgcn_readlane(__float_as_uint(v), l)); }
DI s16x4 tr_read(const LAS unsigned char* p) {
  typedef short v4i16_t __attribute__((ext_vector_type(4)));
  return __builtin_bit_cast(s16x4, __builtin_amdgcn_ds_read_tr16_b64_v4i16((LAS v4i16_t*)p));
}
DI bf16x8 tr_frag(const LAS unsigned char* p, int row_stride4) {
  s16x4 lo = tr_read(p), hi = tr_read(p + row_stride4);
  return __builtin_shufflevector(lo, hi, 0, 1, 2, 3, 4, 5, 6, 7);
}
DI __amdgpu_buffer_rsrc_t mk_rsrc(const void* p, unsigned bytes) {
  return __builtin_amdgcn_make_buffer_rsrc((void*)p, (short)0, (int)bytes, 0x00020000);
}
DI void glds16(__amdgpu_buffer_rsrc_t rs, unsigned dst, unsigned voff, unsigned soff) {
  asm volatile("s_mov_b32 m0, %0\n\ts_nop 0\n\tbuffer_load_dwordx4 %1, %2, %3 offen lds" ::"s"(dst), "v"(voff), "s"(rs), "s"(soff) : "memory");
}
DI u32x4 ld16(__amdgpu_buffer_rsrc_t rs, unsigned voff, unsigned soff) { return __builtin_amdgcn_raw_buffer_load_b128(rs, voff, soff, 0); }
DI u32x4 ld16c(__amdgpu_buffer_rsrc_t rs, unsigned voff, unsigned soff) { return __builtin_amdgcn_raw_buffer_load_b128(rs, voff, soff, 16); }
DI void st16(__amdgpu_buffer_rsrc_t rs, u32x4 v, unsigned voff, unsigned soff) { __builtin_amdgcn_raw_buffer_store_b128(v, rs, voff + soff, 0, 16); }
DI bf16x8 as_frag(u32x4 v) { return __builtin_bit_cast(bf16x8, v); }
DI u32x4 as_u4(bf16x8 v) { return __builtin_bit_cast(u32x4, v); }
template <int N> DI void wait_bar() { asm volatile("s_waitcnt vmcnt(%0) lgkmcnt(0)\n\ts_barrier" ::"n"(N) : "memory"); }

template <int V> struct IC { static constexpr int value = V; };
template <int I, int N, class Fn> DI void static_for(Fn&& fn) {
  if constexpr (I < N) { fn(IC<I>{}); static_for<I + 1, N>(fn); }
}
DI void drain() { asm volatile("s_waitcnt vmcnt(0)" ::: "memory"); }
template <int N> DI void wait_vm() { asm volatile("s_waitcnt vmcnt(%0)" ::"n"(N) : "memory"); }
DI void put_granule(unsigned char* ws, unsigned off, u64 tag, unsigned payload) {
  __hip_atomic_store((gu64*)(ws + off), (tag << 16) | (u64)(payload & 0xffffu), __ATOMIC_RELAXED, __HIP_MEMORY_SCOPE_AGENT);
}
template <int SLEEP> DI unsigned poll_granule(unsigned char* ws, unsigned off, u64 tag) {
  u64 v = ~(tag << 16);
  for (unsigned spins = 0;; ++spins) {
    if ((v >> 16) != tag) v = __hip_atomic_load((gu64*)(ws + off), __ATOMIC_RELAXED, __HIP_MEMORY_SCOPE_AGENT);
    if (__all((v >> 16) == tag) || spins > SPIN_CAP) break;
    __builtin_amdgcn_s_sleep(SLEEP);
  }
  __builtin_amdgcn_fence(__ATOMIC_ACQUIRE, "wavefront");
  return (unsigned)v & 0xffffu;
}
DI unsigned wait_lds_word(LAS unsigned char* p, unsigned want) {
  unsigned v = 0;
#pragma nounroll
  for (unsigned spins = 0; spins < (1u << 24); ++spins) {
    v = __builtin_amdgcn_readfirstlane(__hip_atomic_load((LAS unsigned*)p, __ATOMIC_RELAXED, __HIP_MEMORY_SCOPE_WORKGROUP));
    if (v >= want) break;
    __builtin_amdgcn_s_sleep(1);
  }
  asm volatile("" ::: "memory");
  return v;
}
template <int N> DI u64 wait_bar_epoch(const unsigned char* p) {
  u64 ep;
  asm volatile("s_load_dwordx2 %0, %1, 0x0\n\ts_waitcnt vmcnt(%2) lgkmcnt(0)\n\ts_barrier" : "=&s"(ep) : "s"(p), "n"(N) : "memory");
  return ep;
}
DI void bar_lds() { asm volatile("s_waitcnt lgkmcnt(0)\n\ts_barrier" ::: "memory"); }
DI void st_tile(LAS unsigned char* p, const f32x16& a) {
#pragma unroll
  for (int g = 0; g < 4; ++g) *(LAS f32x4*)(p + g * 1024) = f32x4{a[4 * g], a[4 * g + 1], a[4 * g + 2], a[4 * g + 3]};
}
DI void add_tile(f32x16& a, const LAS unsigned char* p) {
#pragma unroll
  for (int g = 0; g < 4; ++g) {
    const f32x4 v = *(const LAS f32x4*)(p + g * 1024);
    a[4 * g] += v[0]; a[4 * g + 1] += v[1]; a[4 * g + 2] += v[2]; a[4 * g + 3] += v[3];
  }
}

#define STAMP_DECL do {} while (0)
#define STAMP(id) do {} while (0)
#define STAMP_FLUSH(P, ph) do {} while (0)

DI void role_a(const Params& P, LAS unsigned char* lds, const int wt) {
  u64 ep = 0;
  const int lane = threadIdx.x & 63, r = lane & 31, h = lane >> 5;
  const int b = blockIdx.x, xcd = b & 7, idx = b >> 3;
  const unsigned lo = lane * 16;
  STAMP_DECL;
  STAMP(0);
  __builtin_amdgcn_s_setprio(2);
  f32x16 acc[4];
  const int rb = (xcd & 1) * 8 + (idx & 7), j = (xcd >> 1) * 4 + (idx >> 3);
  {
    unsigned xoff[4];
#pragma unroll
    for (int ks = 0; ks < 4; ++ks) xoff[ks] = (unsigned)(L_X + wt * 4096 + r * 128 + (((2 * ks + h) ^ ((r >> 1) & 7)) << 4));
    const unsigned wqoff = (unsigned)(L_WQ + (8 * h + ((lane & 15) >> 2)) * 192 + ((lane >> 4) & 1) * 32 + (lane & 3) * 8);
#pragma unroll
    for (int i = 0; i < 3; ++i) acc[i] = f32x16{};
    auto kloop = [&](auto nvc) {
      constexpr int NV = decltype(nvc)::value;
      bf16x8 xf[4], wf[4][3];
      auto rd = [&](int c, int ks) {
        xf[ks] = *(const LAS bf16x8*)(lds + xoff[ks] + c * 16384);
#pragma unroll
        for (int ct = 0; ct < 3; ++ct) wf[ks][ct] = tr_frag(lds + wqoff + (c & 3) * 12288 + ks * 16 * 192 + ct * 56, 4 * 192);
      };
      auto mm = [&](int ks) {
#pragma unroll
        for (int ct = 0; ct < 3; ++ct) acc[ct] = ct >= 3 - NV ? MFMA(xf[ks], wf[ks][ct], acc[ct]) : MFMA(wf[ks][ct], xf[ks], acc[ct]);
      };
#pragma unroll
      for (int c = 0; c < 7; ++c) {
        if (c == 0) ep = wait_bar_epoch<63>(P.ws + FL_OFF);
        else bar_lds();
        if (c == 0) STAMP(1);
        rd(c, 0);
        SBAR();
        if (c > 0) mm(2);
        SBAR();
        rd(c, 1);
        SBAR();
        if (c > 0) mm(3);
        SBAR();
        rd(c, 2);
        SBAR();
        mm(0);
        SBAR();
        rd(c, 3);
        SBAR();
        mm(1);
        SBAR();
      }
      bar_lds();
      mm(2);
      mm(3);
    };
    if (j < 10) kloop(IC<0>{});
    else if (j == 10) kloop(IC<1>{});
    else kloop(IC<3>{});
  }
  STAMP(2);
  const u64 tag = (ep + 1) & 0xffffffffffffull;
  unsigned char* const wsb = P.ws + SET_BASE + ((unsigned)tag & 3u) * SET_STRIDE;
  const __amdgpu_buffer_rsrc_t rsWs = mk_rsrc(wsb, WS_END);
  {
    const int rt = rb * 4 + wt;
    float kmax2 = 0.f;
#pragma unroll
    for (int ct = 0; ct < 3; ++ct) {
      const int u = 3 * j + ct, part = u >> 4, hd = u & 15;
      f32x16 t = acc[ct];
      if (part == 2) {
        const unsigned padv = r == DH ? 0x3f803f80u : 0u;
        u32x4 v0 = as_u4(pack8(t, 0)), v1 = as_u4(pack8(t, 1));
#pragma unroll
        for (int i = 0; i < 4; ++i) { v0[i] = r < DH ? v0[i] : padv; v1[i] = r < DH ? v1[i] : padv; }
        st16(rsWs, v0, lo + 2048, KV_OFF + (hd * 64 + rt) * 4096);
        st16(rsWs, v1, lo + 3072, KV_OFF + (hd * 64 + rt) * 4096);
      } else {
        const float sc = part == 0 ? QSCALE : 1.f;
        float na = 0.f, nb = 0.f;
#pragma unroll
        for (int i = 0; i < 16; ++i) {
          t[i] *= sc;
          if (i < 12) na = fmaf(t[i], t[i], na);
          else nb = fmaf(t[i], t[i], nb);
        }
        const unsigned so = part == 0 ? QF_OFF + (hd * 64 + rt) * 2048 : KV_OFF + (hd * 64 + rt) * 4096;
        st16(rsWs, as_u4(pack8(t, 0)), lo, so);
        na += h ? 0.f : nb;
        na += swap_half(na);
        u32x4 t1 = as_u4(pack8(t, 1));
        t1[2] = h ? (part == 0 ? cvtpk(__builtin_amdgcn_sqrtf(na), 0.f) : 0x3f803f80u) : t1[2];
        t1[3] = h ? (part == 0 ? 0u : 0x00003f80u) : t1[3];
        st16(rsWs, t1, lo + 1024, so);
        kmax2 = part == 1 ? fmaxf(kmax2, na) : kmax2;
      }
    }
    kmax2 = row16_max(kmax2);
    kmax2 = fmaxf(rdlane(kmax2, 0), rdlane(kmax2, 16));
    if (lane == 0) __hip_atomic_fetch_max((LAS unsigned*)(lds + L_CTRL + 12), __float_as_uint(kmax2), __ATOMIC_RELAXED, __HIP_MEMORY_SCOPE_WORKGROUP);
    drain();
    if (lane == 0) {
      if (__hip_atomic_fetch_add((LAS unsigned*)(lds + L_CTRL + 8), 1u, __ATOMIC_RELAXED, __HIP_MEMORY_SCOPE_WORKGROUP) == 3u) {
        const unsigned km = __hip_atomic_load((LAS unsigned*)(lds + L_CTRL + 12), __ATOMIC_RELAXED, __HIP_MEMORY_SCOPE_WORKGROUP);
        put_granule(wsb, GQ_OFF + (j * 16 + rb) * 128, tag, cvtpk(__uint_as_float(km), 0.f));
      }
    }
    STAMP(3);
  }
  const int head = xcd * 2 + (idx & 1), qb = idx >> 1;
  {
    float kmax;
    if (wt == 0) {
      const int grp = lane < 16 ? (16 + head) / 3 : lane < 32 ? (32 + head) / 3 : head / 3;
      float kn = __uint_as_float(poll_granule<POLL_A>(wsb, GQ_OFF + (grp * 16 + (h ? qb : (lane & 15))) * 128, tag) << 16);
      kn = row16_max(kn);
      kmax = __builtin_amdgcn_sqrtf(rdlane(kn, 0));
      if (lane == 0) *(volatile LAS unsigned*)(lds + L_CTRL + 4) = __float_as_uint(kmax) | 1u;
    } else {
      kmax = __uint_as_float(wait_lds_word(lds + L_CTRL + 4, 1));
    }
    kmax = __uint_as_float(__float_as_uint(kmax) | 1u);
    STAMP(4);
    u32x4 q[4][2];
    const unsigned kvbase = KV_OFF + (head * 64 + wt * 16) * 4096;
    auto kvo = [&](int kt) { return kvbase + (unsigned)((kt + qb) & 15) * 4096; };
    u32x4 kv[4][4];
    q[0][0] = ld16c(rsWs, lo, QF_OFF + (head * 64 + qb * 4) * 2048);
    q[0][1] = ld16c(rsWs, lo + 1024, QF_OFF + (head * 64 + qb * 4) * 2048);
#pragma unroll
    for (int e = 0; e < 4; ++e) kv[0][e] = ld16c(rsWs, lo + e * 1024, kvo(0));
#pragma unroll
    for (int t = 1; t < 4; ++t) {
      q[t][0] = ld16c(rsWs, lo, QF_OFF + (head * 64 + qb * 4 + t) * 2048);
      q[t][1] = ld16c(rsWs, lo + 1024, QF_OFF + (head * 64 + qb * 4 + t) * 2048);
    }
#pragma unroll
    for (int i = 1; i < 3; ++i)
#pragma unroll
      for (int e = 0; e < 4; ++e) kv[i][e] = ld16c(rsWs, lo + e * 1024, kvo(i));
    SBAR();
#pragma unroll
    for (int i = 0; i < 4; ++i) acc[i] = f32x16{};
    float m[4];
    bool unsafe = false;
#pragma unroll
    for (int t = 0; t < 4; ++t) {
      m[t] = __uint_as_float(q[t][1][2] << 16) * kmax;
      unsafe |= h && !(m[t] <= SAFE_BOUND);
    }
    if (__builtin_expect(__any(unsafe), 0)) {
      float mx[4];
#pragma unroll
      for (int t = 0; t < 4; ++t) { mx[t] = -INFINITY; if (h) { q[t][1][2] = 0; q[t][1][3] = 0; } }
      for (int kt = 0; kt < 64; ++kt) {
        const u32x4 k0 = ld16c(rsWs, lo, KV_OFF + (head * 64 + kt) * 4096), k1 = ld16c(rsWs, lo + 1024, KV_OFF + (head * 64 + kt) * 4096);
#pragma unroll
        for (int t = 0; t < 4; ++t) {
          f32x16 s = MFMA(as_frag(k0), as_frag(q[t][0]), f32x16{});
          s = MFMA(as_frag(k1), as_frag(q[t][1]), s);
#pragma unroll
          for (int i = 0; i < 16; ++i) mx[t] = fmaxf(mx[t], s[i]);
        }
      }
#pragma unroll
      for (int t = 0; t < 4; ++t) m[t] = fmaxf(mx[t], swap_half(mx[t]));
    }
#pragma unroll
    for (int t = 0; t < 4; ++t) {
      const float nm = -m[t];
      const unsigned hi = __float_as_uint(nm) & 0xffff0000u;
      const float r1 = nm - __uint_as_float(hi);
      const unsigned mid = __float_as_uint(r1) & 0xffff0000u;
      const float r2 = r1 - __uint_as_float(mid);
      if (h) { q[t][1][2] = (hi >> 16) | mid; q[t][1][3] = __float_as_uint(r2) >> 16; }
    }
    STAMP(5);
    __builtin_amdgcn_s_setprio(0);
    f32x16 sc = MFMA(as_frag(kv[0][0]), as_frag(q[0][0]), f32x16{});
    sc = MFMA(as_frag(kv[0][1]), as_frag(q[0][1]), sc);
    u32x4 p0, p1 = {0, 0, 0, 0};
    float e0 = 0.f, e1 = 0.f, e2 = 0.f, e3 = 0.f;
#define EXP4(i) do { e0 = __builtin_amdgcn_exp2f(sc[i]); e1 = __builtin_amdgcn_exp2f(sc[(i) + 1]); e2 = __builtin_amdgcn_exp2f(sc[(i) + 2]); e3 = __builtin_amdgcn_exp2f(sc[(i) + 3]); \
    asm volatile("" : "+v"(e0), "+v"(e1), "+v"(e2), "+v"(e3)); SBAR(); } while (0)
#define CVT2(P, i) do { P[i] = cvtpk(e0, e1); P[(i) + 1] = cvtpk(e2, e3); asm volatile("" : "+v"(P)); SBAR(); } while (0)
#pragma unroll
    for (int i = 0; i < 64; ++i) {
      const int kt = i >> 2, t = i & 3, kn_ = (i + 1) >> 2, tn = (i + 1) & 3, kp = (i - 1) >> 2, tp = (i - 1) & 3;
      f32x16 sn;
      SBAR();
      if (i < 63) sn = MFMA(as_frag(kv[kn_ & 3][0]), as_frag(q[tn][0]), f32x16{});
      SBAR();
      if (i > 0) CVT2(p1, 2);
      EXP4(0);
      if (i > 0) acc[tp] = MFMA(as_frag(kv[kp & 3][3]), as_frag(p1), acc[tp]);
      SBAR();
      if (kt + 3 < 16) kv[(kt + 3) & 3][t] = ld16c(rsWs, lo + t * 1024, kvo(kt + 3));
      SBAR();
      CVT2(p0, 0);
      EXP4(4);
      if (i < 63) sn = MFMA(as_frag(kv[kn_ & 3][1]), as_frag(q[tn][1]), sn);
      SBAR();
      CVT2(p0, 2);
      EXP4(8);
      acc[t] = MFMA(as_frag(kv[kt & 3][2]), as_frag(p0), acc[t]);
      SBAR();
      CVT2(p1, 0);
      EXP4(12);
      if (i < 63) sc = sn;
    }
    CVT2(p1, 2);
    acc[3] = MFMA(as_frag(kv[3][3]), as_frag(p1), acc[3]);
#undef EXP4
#undef CVT2
    STAMP(6);
    __builtin_amdgcn_s_setprio(2);
    (void)wait_lds_word(lds + L_CTRL, 4);
#pragma unroll
    for (int u = 1; u < 4; ++u) {
      const int t = (wt + u) & 3;
      const f32x16 a = t == 0 ? acc[0] : t == 1 ? acc[1] : t == 2 ? acc[2] : acc[3];
      st_tile(lds + L_CMB + (wt * 3 + u - 1) * 4096 + lo, a);
    }
    bar_lds();
    f32x16 o = wt == 0 ? acc[0] : wt == 1 ? acc[1] : wt == 2 ? acc[2] : acc[3];
#pragma unroll
    for (int u = 1; u < 4; ++u) add_tile(o, lds + L_CMB + (((wt - u) & 3) * 3 + u - 1) * 4096 + lo);
    const float lsw = swap_half(o[12]);
    const float inv = __builtin_amdgcn_rcpf(h ? o[12] : lsw);
#pragma unroll
    for (int i = 0; i < 16; ++i) o[i] *= inv;
    u32x4 o1 = as_u4(pack8(o, 1));
    o1[2] = h ? 0u : o1[2];
    o1[3] = h ? 0u : o1[3];
    const unsigned so = OF_OFF + ((qb * 4 + wt) * 16 + head) * 2048;
    st16(rsWs, as_u4(pack8(o, 0)), lo, so);
    st16(rsWs, o1, lo + 1024, so);
    drain();
    if (lane == 0) put_granule(wsb, GO_OFF + ((qb * 4 + wt) * 16 + head) * 128, tag, 0);
    STAMP(7);
  }
  if (idx < 28) {
    const int mb = 4 * xcd + (idx & 3), nb = idx >> 2;
    const int mt = wt & 1, kh = wt >> 1;
    (void)poll_granule<4>(wsb, GW_OFF + ((nb * 2 + ((lane >> 3) & 1)) * 16 + kh * 8 + (lane & 7)) * 8, tag);
    u32x4 wf[2][16], of[16];
#pragma unroll
    for (int e = 0; e < 16; ++e) {
      wf[0][e] = ld16c(rsWs, lo, WOF_OFF + ((nb * 2) * 16 + kh * 8) * 2048 + e * 1024);
      wf[1][e] = ld16c(rsWs, lo, WOF_OFF + ((nb * 2 + 1) * 16 + kh * 8) * 2048 + e * 1024);
    }
    SBAR();
    const unsigned pay = poll_granule<POLL_A>(wsb, h ? GO_OFF + ((mb * 2 + mt) * 16 + kh * 8 + (lane & 7)) * 128 : GG_OFF + ((mb * 2 + mt) * 32 + r) * 8, tag);
    STAMP(8);
#pragma unroll
    for (int e = 0; e < 16; ++e) of[e] = ld16c(rsWs, lo, OF_OFF + ((mb * 2 + mt) * 16 + kh * 8) * 2048 + e * 1024);
    SBAR();
    const float gl = __uint_as_float(pay << 16), gs = swap_half(gl);
    const float g = h ? gs : gl;
    acc[0] = f32x16{};
    acc[1] = f32x16{};
#pragma unroll
    for (int e = 0; e < 16; ++e) {
      acc[0] = MFMA(as_frag(wf[0][e]), as_frag(of[e]), acc[0]);
      acc[1] = MFMA(as_frag(wf[1][e]), as_frag(of[e]), acc[1]);
    }
    st_tile(lds + L_FIN + wt * 4096 + lo, kh ? acc[0] : acc[1]);
    STAMP(9);
    bar_lds();
    f32x16 a = kh ? acc[1] : acc[0], bt = f32x16{};
    add_tile(a, lds + L_FIN + (wt ^ 2) * 4096 + lo);
#pragma unroll
    for (int f = 0; f < 4; ++f) add_tile(bt, lds + L_W2 + f * 24576 + (kh * 2 + mt) * 4096 + lo);
    const float og = 1.f - g;
    unsigned short* dst = P.out + (size_t)((mb * 2 + mt) * 32 + r) * DM + (nb * 2 + kh) * 32 + 4 * h;
#pragma unroll
    for (int q = 0; q < 4; ++q) {
      u32x2 v;
      v[0] = cvtpk(g * a[4 * q] + og * bt[4 * q], g * a[4 * q + 1] + og * bt[4 * q + 1]);
      v[1] = cvtpk(g * a[4 * q + 2] + og * bt[4 * q + 2], g * a[4 * q + 3] + og * bt[4 * q + 3]);
      *(u32x2*)(dst + 8 * q) = v;
    }
  }
  STAMP(10);
  STAMP_FLUSH(P, 0);
}

constexpr int W2S = 6;
constexpr int HPRE = 9;
#define NPRE 5
constexpr int ffn2_ops(int f) { return (f + W2S < 14 ? 4 : 0) + (f + HPRE < 14 ? 4 : 0); }
constexpr int ffn2_younger(int f, int upto) {
  int n = (f - W2S + HPRE < 14) ? 4 : 0;
  for (int i = f - W2S + 1; i < upto; ++i) n += ffn2_ops(i);
  return n;
}

template <int KIND>
DI void role_f(const Params& P, LAS unsigned char* lds, const int wt) {
  u64 ep;
  const int lane = threadIdx.x & 63, r = lane & 31, h = lane >> 5;
  const int b = blockIdx.x, xcd = b & 7, idx = b >> 3;
  const int rb = (xcd & 1) * 8 + (idx & 7), j = (xcd >> 1) * 4 + (idx >> 3);
  const unsigned lo = lane * 16, lds0 = (unsigned)(uintptr_t)lds;
  STAMP_DECL;
  STAMP(0);
  __builtin_amdgcn_s_setprio(1);
  f32x16 acc[4];
  u32x4 wl[28];
  {
    const __amdgpu_buffer_rsrc_t rsX = mk_rsrc(P.x, SEQ * DM * 2);
    const __amdgpu_buffer_rsrc_t rsW = mk_rsrc(P.wqkv, DM * 3 * DM * 2);
    const __amdgpu_buffer_rsrc_t rsL = KIND == 0 ? mk_rsrc(P.w1, DM * FF * 2) : mk_rsrc(P.gw, DM * 2);
    unsigned xsrc[2], wsrc[3];
#pragma unroll
    for (int i = 0; i < 2; ++i) {
      const int row = 32 * wt + 8 * i + (lane >> 3), ch = (lane & 7) ^ ((row >> 1) & 7);
      xsrc[i] = (unsigned)((rb * 128 + row) * (DM * 2) + ch * 16);
    }
#pragma unroll
    for (int i = 0; i < 3; ++i) {
      const int p = 64 * (3 * wt + i) + lane, kr = p / 12, ch = p % 12;
      wsrc[i] = (unsigned)(kr * (3 * DM * 2) + j * 168 + (ch < 11 ? ch : 10) * 16);
    }
    const unsigned lsrc = KIND == 0 ? (unsigned)((lane >> 2) * (FF * 2) + (j * 4 + wt) * 64 + (lane & 3) * 16) : (unsigned)(h * 16);
    const int rot = ROT1(rb, j);
    auto kc = [&](int c) { const int v = c + rot; return v >= 7 ? v - 7 : v; };
    auto X = [&](int c) {
#pragma unroll
      for (int i = 0; i < 4; ++i) glds16(rsX, lds0 + L_X + c * 16384 + (4 * wt + i) * 1024, xsrc[i & 1], kc(c) * 128 + (i >> 1) * 16 * DM * 2);
    };
    auto W = [&](int c) {
#pragma unroll
      for (int i = 0; i < 3; ++i) glds16(rsW, lds0 + L_WQ + (c & 3) * 12288 + (3 * wt + i) * 1024, wsrc[i], kc(c) * 64 * 3 * DM * 2);
    };
    auto L = [&](int u0, int u1) {
      if (KIND != 2) {
#pragma unroll
        for (int u = u0; u < u1; ++u) wl[u] = ld16(rsL, lsrc, (kc(u >> 2) * 4 + (u & 3)) * (KIND == 0 ? 16 * FF * 2 : 32));
      }
      SBAR();
    };
    X(0); W(0); X(1); W(1); X(2); W(2);
    wait_bar<14>();
    STAMP(1);
    X(3); W(3); X(4);
    wait_bar<18>();
    W(4); X(5);
    wait_bar<18>();
    W(5); X(6);
    wait_bar<18>();
    W(6);
    wait_bar<14>();
    if (wt == 0 && lane < 8) *(LAS unsigned*)(lds + L_CTRL + lane * 4) = 0u;
    ep = wait_bar_epoch<7>(P.ws + FL_OFF);
    constexpr int LE = KIND == 2 ? 0 : 4;
    L(0, 4);
    wait_bar<LE>();
    L(4, 12);
    bar_lds();
    L(12, 28);
  }
  STAMP(2);
  const u64 tag = (ep + 1) & 0xffffffffffffull;
  unsigned char* const wsb = P.ws + SET_BASE + ((unsigned)tag & 3u) * SET_STRIDE;
  const __amdgpu_buffer_rsrc_t rsWs = mk_rsrc(wsb, WS_END);
  unsigned xoff[4];
#pragma unroll
  for (int ks = 0; ks < 4; ++ks) xoff[ks] = (unsigned)(L_X + r * 128 + (((2 * ks + h) ^ ((r >> 1) & 7)) << 4));
  if (KIND == 0) {
    LAS unsigned char* ring = lds + L_W1 + wt * 8192;
    const unsigned troff = (unsigned)((8 * h + ((lane & 15) >> 2)) * 64 + ((lane >> 4) & 1) * 32 + (lane & 3) * 8);
#pragma unroll
    for (int i = 0; i < 4; ++i) acc[i] = f32x16{};
    bf16x8 xf[2][4], wf[2];
    auto wr = [&](int u) { *(LAS u32x4*)(ring + (u & 7) * 1024 + lo) = wl[u]; };
    auto rd = [&](int u) {
      wf[u & 1] = tr_frag(ring + (u & 7) * 1024 + troff, 4 * 64);
#pragma unroll
      for (int t = 0; t < 4; ++t) xf[u & 1][t] = *(const LAS bf16x8*)(lds + xoff[u & 3] + (u >> 2) * 16384 + t * 4096);
    };
    wr(0); wr(1); wr(2);
    rd(0);
    SBAR();
#pragma unroll
    for (int u = 0; u < 28; ++u) {
      if (u + 3 < 28) wr(u + 3);
      if (u + 1 < 28) rd(u + 1);
      SBAR();
#pragma unroll
      for (int t = 0; t < 4; ++t) acc[t] = MFMA(wf[u & 1], xf[u & 1][t], acc[t]);
      SBAR();
    }
    if (lane == 0) __hip_atomic_fetch_add((LAS unsigned*)(lds + L_CTRL), 1u, __ATOMIC_RELAXED, __HIP_MEMORY_SCOPE_WORKGROUP);
    STAMP(3);
#pragma unroll
    for (int t = 0; t < 4; ++t) {
      f32x16 z = acc[t];
#pragma unroll
      for (int i = 0; i < 16; ++i) z[i] = z[i] * __builtin_amdgcn_rcpf(1.f + __builtin_amdgcn_exp2f(-LOG2E * z[i]));
      const unsigned so = HF_OFF + ((rb * 4 + t) * 56 + j * 4 + wt) * 2048;
      st16(rsWs, as_u4(pack8(z, 0)), lo, so);
      st16(rsWs, as_u4(pack8(z, 1)), lo + 1024, so);
    }
    drain();
    if (lane == 0) {
      if (__hip_atomic_fetch_add((LAS unsigned*)(lds + L_CTRL + 16), 1u, __ATOMIC_RELAXED, __HIP_MEMORY_SCOPE_WORKGROUP) == 3u)
        put_granule(wsb, GH_OFF + (rb * 16 + j) * 128, tag, 0);
    }
  } else {
    if (KIND == 1) {
      acc[0] = f32x16{};
#pragma unroll
      for (int u = 0; u < 28; ++u) {
        const bf16x8 xf = *(const LAS bf16x8*)(lds + xoff[u & 3] + (u >> 2) * 16384 + wt * 4096);
        acc[0] = MFMA(as_frag(wl[u]), xf, acc[0]);
      }
      const float g = __builtin_amdgcn_rcpf(1.f + __builtin_amdgcn_exp2f(-LOG2E * acc[0][0]));
      if (h == 0) put_granule(wsb, GG_OFF + ((rb * 4 + wt) * 32 + r) * 8, tag, cvtpk(g, 0.f));
    }
    if (lane == 0) __hip_atomic_fetch_add((LAS unsigned*)(lds + L_CTRL), 1u, __ATOMIC_RELAXED, __HIP_MEMORY_SCOPE_WORKGROUP);
    const __amdgpu_buffer_rsrc_t rsO = mk_rsrc(P.wo, DM * DM * 2);
    const int id0 = ((j - 14) * 16 + rb) * 4 + wt;
    for (int id = id0; id < 224; id += 128) {
      const int nt = id >> 4, hd = id & 15;
      unsigned rp[16];
#pragma unroll
      for (int e = 0; e < 16; ++e)
        rp[e] = __builtin_amdgcn_raw_buffer_load_b16(rsO, (unsigned)(((4 * h + 8 * (e >> 2) + (e & 3)) * DM + r) * 2), (unsigned)((DH * hd * DM + 32 * nt) * 2), 0);
      u32x4 a, c2;
#pragma unroll
      for (int i = 0; i < 4; ++i) {
        a[i] = (rp[2 * i] & 0xffffu) | (rp[2 * i + 1] << 16);
        c2[i] = (rp[8 + 2 * i] & 0xffffu) | (rp[8 + 2 * i + 1] << 16);
      }
      c2[2] = h ? 0u : c2[2];
      c2[3] = h ? 0u : c2[3];
      st16(rsWs, a, lo, WOF_OFF + id * 2048);
      st16(rsWs, c2, lo + 1024, WOF_OFF + id * 2048);
      drain();
      if (lane == 0) put_granule(wsb, GW_OFF + id * 8, tag, 0);
    }
  }
  STAMP(4);
  if (idx >= 28) {
    if (b == 255 && wt == 0) {
      __builtin_amdgcn_s_sleep(127);
#pragma unroll
      for (int i = 0; i < 4; ++i) (void)poll_granule<64>(wsb, GQ_OFF + (i * 64 + lane) * 128, tag);
      if (lane == 0) __hip_atomic_store((gu64*)(P.ws + FL_OFF), ep + 1, __ATOMIC_RELAXED, __HIP_MEMORY_SCOPE_AGENT);
    }
    STAMP(10);
    STAMP_FLUSH(P, 0);
    return;
  }
  {
    const int mb = 4 * xcd + (idx & 3), nb = idx >> 2;
    const __amdgpu_buffer_rsrc_t rsW2 = mk_rsrc(P.w2, FF * DM * 2);
    const unsigned w2src = (unsigned)((wt * 448 + (lane >> 3)) * (DM * 2) + nb * 128 + ((((lane >> 2) & 1) ^ ((lane >> 4) & 1)) * 64) + (lane & 3) * 16);
    const unsigned ring0 = lds0 + L_W2 + wt * 24576;
    const int rot2 = (2 * nb + 3 * (idx & 3)) % 14;
    auto fr = [&](int f) { const int v = f + rot2; return v >= 14 ? v - 14 : v; };
    auto D = [&](int f) {
#pragma unroll
      for (int i = 0; i < 4; ++i) glds16(rsW2, ring0 + (f % W2S) * 4096 + i * 1024, w2src, (fr(f) * 32 + i * 8) * DM * 2);
    };
    (void)wait_lds_word(lds + L_CTRL, 4);
#pragma unroll
    for (int f = 0; f < W2S; ++f) D(f);
    (void)poll_granule<32>(wsb, GH_OFF + ((mb >> 1) * 16 + ((wt * 14 + (lane < 14 ? lane : 0)) >> 2)) * 128, tag);
    STAMP(5);
    u32x4 hb[HPRE][4];
    const unsigned hbase = HF_OFF + (mb * 2 * 56 + wt * 14) * 2048;
    auto H = [&](int f) {
#pragma unroll
      for (int e = 0; e < 4; ++e) hb[f % HPRE][e] = ld16c(rsWs, lo + (e & 1) * 1024, hbase + ((e >> 1) * 56 + fr(f)) * 2048);
    };
#pragma unroll
    for (int f = 0; f < HPRE; ++f) H(f);
    SBAR();
    const int q4 = (lane & 15) >> 2;
    unsigned woff[2];
#pragma unroll
    for (int nt = 0; nt < 2; ++nt) woff[nt] = (unsigned)(L_W2 + wt * 24576 + (4 * h + q4) * 128 + ((nt ^ (q4 >> 1)) * 64) + ((lane >> 4) & 1) * 32 + (lane & 3) * 8);
#pragma unroll
    for (int i = 0; i < 4; ++i) acc[i] = f32x16{};
    wait_vm<0>();
    STAMP(6);
    static_for<0, 14>([&](auto fc) {
      constexpr int f = decltype(fc)::value;
      if constexpr (f == NPRE) {
        bar_lds();
        STAMP(7);
      }
      if constexpr (f >= W2S) wait_vm<ffn2_younger(f, f)>();
      bf16x8 wf[2][2];
#pragma unroll
      for (int s = 0; s < 2; ++s)
#pragma unroll
        for (int nt = 0; nt < 2; ++nt) wf[s][nt] = tr_frag(lds + woff[nt] + (f % W2S) * 4096 + s * 2048, 1024);
#pragma unroll
      for (int s = 0; s < 2; ++s)
#pragma unroll
        for (int nt = 0; nt < 2; ++nt)
#pragma unroll
          for (int mt = 0; mt < 2; ++mt) acc[nt * 2 + mt] = MFMA(wf[s][nt], as_frag(hb[f % HPRE][mt * 2 + s]), acc[nt * 2 + mt]);
      SBAR();
      asm volatile("s_waitcnt lgkmcnt(0)" ::: "memory");
      if (f + W2S < 14) D(f + W2S);
      if (f + HPRE < 14) H(f + HPRE);
      SBAR();
    });
    STAMP(8);
#pragma unroll
    for (int t = 0; t < 4; ++t) st_tile(lds + L_W2 + wt * 24576 + t * 4096 + lo, acc[t]);
    STAMP(9);
    bar_lds();
  }
  STAMP(10);
  STAMP_FLUSH(P, 0);
}

extern __shared__ __attribute__((aligned(16))) unsigned char g_lds[];

__global__ __launch_bounds__(512, 2) void k_fused(Params P) {
  LAS unsigned char* lds = (LAS unsigned char*)g_lds;
  const int w = __builtin_amdgcn_readfirstlane(threadIdx.x >> 6);
  const int j = ((blockIdx.x & 7) >> 1) * 4 + (blockIdx.x >> 6);
  if (w < 4) role_a(P, lds, w);
  else if (j < 14) role_f<0>(P, lds, w - 4);
  else if (j == 14) role_f<1>(P, lds, w - 4);
  else role_f<2>(P, lds, w - 4);
}

extern "C" void kernel_launch(void* const* d_in, const int* in_sizes, int n_in, void* d_out, int out_size, void* d_ws,
                              size_t ws_size, hipStream_t stream) {
  Params P;
  P.x = (const unsigned short*)d_in[0];
  P.wqkv = (const unsigned short*)d_in[1];
  P.wo = (const unsigned short*)d_in[2];
  P.w1 = (const unsigned short*)d_in[4];
  P.w2 = (const unsigned short*)d_in[5];
  P.gw = (const unsigned short*)d_in[6];
  P.out = (unsigned short*)d_out;
  P.ws = (unsigned char*)d_ws;
  (void)hipFuncSetAttribute((const void*)k_fused, hipFuncAttributeMaxDynamicSharedMemorySize, LDS_BYTES);
  k_fused<<<256, 512, LDS_BYTES, stream>>>(P);
}
```

```cpp
#define SETB 150u
#include <hip/hip_runtime.h>
#include <hip/hip_bf16.h>
#include <stdint.h>

typedef short bf16x8 __attribute__((ext_vector_type(8)));
typedef short s16x4 __attribute__((ext_vector_type(4)));
typedef float f32x16 __attribute__((ext_vector_type(16)));
typedef float f32x4 __attribute__((ext_vector_type(4)));
typedef unsigned u32x4 __attribute__((ext_vector_type(4)));
typedef unsigned u32x2 __attribute__((ext_vector_type(2)));
#define LAS __attribute__((address_space(3)))
#define DI __device__ __forceinline__
#define MFMA(a, b, c) __builtin_amdgcn_mfma_f32_32x32x16_bf16((a), (b), (c), 0, 0, 0)
#define SBAR() __builtin_amdgcn_sched_barrier(0)

constexpr int SEQ = 2048, DM = 448, NH = 16, DH = 28, FF = 1792;
constexpr unsigned FL_OFF = 0;
constexpr unsigned SET_BASE = SETB << 20, SET_STRIDE = 24u << 20;
constexpr unsigned GQ_OFF = 0;
constexpr unsigned GH_OFF = GQ_OFF + 256 * 128;
constexpr unsigned GO_OFF = GH_OFF + 256 * 128;
constexpr unsigned GW_OFF = GO_OFF + 1024 * 128;
constexpr unsigned GG_OFF = GW_OFF + 256 * 8;
constexpr unsigned QF_OFF = 262144;
constexpr unsigned KV_OFF = QF_OFF + 16 * 64 * 2048;
constexpr unsigned HF_OFF = KV_OFF + 16 * 64 * 4096;
constexpr unsigned WOF_OFF = HF_OFF + 64 * 56 * 2048;
constexpr unsigned OF_OFF = WOF_OFF + 14 * 16 * 2048;
constexpr unsigned WS_END = OF_OFF + 64 * 16 * 2048;
static_assert(GG_OFF + 2048 * 8 <= QF_OFF && WS_END <= SET_STRIDE, "workspace map");

constexpr int L_X = 0;
constexpr int L_WQ = 114688;
constexpr int L_W1 = 114688;
constexpr int L_CTRL = 163776;
constexpr int L_W2 = 0;
constexpr int L_CMB = 98304;
constexpr int LDS_BYTES = 163840;

constexpr float QSCALE = 0.18898223650461362f * 1.4426950408889634f;
constexpr float LOG2E = 1.4426950408889634f;
constexpr float SAFE_BOUND = 60.0f;
constexpr unsigned SPIN_CAP = 1u << 18;
#define ROT1(rb, j) (((rb) + 2 * (j)) % 7)
#define POLL_A 4

struct Params {
  const unsigned short* x;
  const unsigned short* wqkv;
  const unsigned short* wo;
  const unsigned short* w1;
  const unsigned short* w2;
  const unsigned short* gw;
  unsigned short* out;
  unsigned char* ws;
};
typedef unsigned long long u64;
typedef __attribute__((address_space(1))) u64 gu64;

DI unsigned cvtpk(float lo, float hi) {
  typedef __bf16 bf2 __attribute__((ext_vector_type(2)));
  typedef float f2 __attribute__((ext_vector_type(2)));
  f2 v = {lo, hi};
  bf2 r = __builtin_convertvector(v, bf2);
  return __builtin_bit_cast(unsigned, r);
}
DI bf16x8 pack8(const f32x16& x, int s) {
  u32x4 p;
  p[0] = cvtpk(x[8 * s + 0], x[8 * s + 1]);
  p[1] = cvtpk(x[8 * s + 2], x[8 * s + 3]);
  p[2] = cvtpk(x[8 * s + 4], x[8 * s + 5]);
  p[3] = cvtpk(x[8 * s + 6], x[8 * s + 7]);
  return __builtin_bit_cast(bf16x8, p);
}
DI float swap_half(float v) {
  auto r = __builtin_amdgcn_permlane32_swap(__float_as_uint(v), __float_as_uint(v), false, false);
  return (threadIdx.x & 32) ? __uint_as_float(r[0]) : __uint_as_float(r[1]);
}
template <int CTRL> DI float dpp_mov(float v) {
  return __uint_as_float(__builtin_amdgcn_update_dpp(__float_as_uint(v), __float_as_uint(v), CTRL, 0xf, 0xf, false));
}
DI float row16_max(float v) {
  v = fmaxf(v, dpp_mov<0x128>(v));
  v = fmaxf(v, dpp_mov<0x124>(v));
  v = fmaxf(v, dpp_mov<0x122>(v));
  v = fmaxf(v, dpp_mov<0x121>(v));
  return v;
}
DI float rdlane(float v, int l) { return __uint_as_float(__builtin_amdgcn_readlane(__float_as_uint(v), l)); }
DI s16x4 tr_read(const LAS unsigned char* p) {
  typedef short v4i16_t __attribute__((ext_vector_type(4)));
  return __builtin_bit_cast(s16x4, __builtin_amdgcn_ds_read_tr16_b64_v4i16((LAS v4i16_t*)p));
}
DI bf16x8 tr_frag(const LAS unsigned char* p, int row_stride4) {
  s16x4 lo = tr_read(p), hi = tr_read(p + row_stride4);
  return __builtin_shufflevector(lo, hi, 0, 1, 2, 3, 4, 5, 6, 7);
}
DI __amdgpu_buffer_rsrc_t mk_rsrc(const void* p, unsigned bytes) {
  return __builtin_amdgcn_make_buffer_rsrc((void*)p, (short)0, (int)bytes, 0x00020000);
}
DI void glds16(__amdgpu_buffer_rsrc_t rs, unsigned dst, unsigned voff, unsigned soff) {
  asm volatile("s_mov_b32 m0, %0\n\ts_nop 0\n\tbuffer_load_dwordx4 %1, %2, %3 offen lds" ::"s"(dst), "v"(voff), "s"(rs), "s"(soff) : "memory");
}
DI u32x4 ld16(__amdgpu_buffer_rsrc_t rs, unsigned voff, unsigned soff) { return __builtin_amdgcn_raw_buffer_load_b128(rs, voff, soff, 0); }
DI u32x4 ld16c(__amdgpu_buffer_rsrc_t rs, unsigned voff, unsigned soff) { return __builtin_amdgcn_raw_buffer_load_b128(rs, voff, soff, 16); }
DI void st16(__amdgpu_buffer_rsrc_t rs, u32x4 v, unsigned voff, unsigned soff) { __builtin_amdgcn_raw_buffer_store_b128(v, rs, voff + soff, 0, 16); }
DI bf16x8 as_frag(u32x4 v) { return __builtin_bit_cast(bf16x8, v); }
DI u32x4 as_u4(bf16x8 v) { return __builtin_bit_cast(u32x4, v); }
template <int N> DI void wait_bar() { asm volatile("s_waitcnt vmcnt(%0) lgkmcnt(0)\n\ts_barrier" ::"n"(N) : "memory"); }

template <int V> struct IC { static constexpr int value = V; };
template <int I, int N, class Fn> DI void static_for(Fn&& fn) {
  if constexpr (I < N) { fn(IC<I>{}); static_for<I + 1, N>(fn); }
}
DI void drain() { asm volatile("s_waitcnt vmcnt(0)" ::: "memory"); }
template <int N> DI void wait_vm() { asm volatile("s_waitcnt vmcnt(%0)" ::"n"(N) : "memory"); }
DI void put_granule(unsigned char* ws, unsigned off, u64 tag, unsigned payload) {
  __hip_atomic_store((gu64*)(ws + off), (tag << 16) | (u64)(payload & 0xffffu), __ATOMIC_RELAXED, __HIP_MEMORY_SCOPE_AGENT);
}
template <int SLEEP> DI unsigned poll_granule(unsigned char* ws, unsigned off, u64 tag) {
  u64 v = ~(tag << 16);
  for (unsigned spins = 0;; ++spins) {
    if ((v >> 16) != tag) v = __hip_atomic_load((gu64*)(ws + off), __ATOMIC_RELAXED, __HIP_MEMORY_SCOPE_AGENT);
    if (__all((v >> 16) == tag) || spins > SPIN_CAP) break;
    __builtin_amdgcn_s_sleep(SLEEP);
  }
  __builtin_amdgcn_fence(__ATOMIC_ACQUIRE, "wavefront");
  return (unsigned)v & 0xffffu;
}
DI unsigned wait_lds_word(LAS unsigned char* p, unsigned want) {
  unsigned v = 0;
#pragma nounroll
  for (unsigned spins = 0; spins < (1u << 24); ++spins) {
    v = __builtin_amdgcn_readfirstlane(__hip_atomic_load((LAS unsigned*)p, __ATOMIC_RELAXED, __HIP_MEMORY_SCOPE_WORKGROUP));
    if (v >= want) break;
    __builtin_amdgcn_s_sleep(1);
  }
  asm volatile("" ::: "memory");
  return v;
}
template <int N> DI u64 wait_bar_epoch(const unsigned char* p) {
  u64 ep;
  asm volatile("s_load_dwordx2 %0, %1, 0x0\n\ts_waitcnt vmcnt(%2) lgkmcnt(0)\n\ts_barrier" : "=&s"(ep) : "s"(p), "n"(N) : "memory");
  return ep;
}
DI void bar_lds() { asm volatile("s_waitcnt lgkmcnt(0)\n\ts_barrier" ::: "memory"); }
DI void st_tile(LAS unsigned char* p, const f32x16& a) {
#pragma unroll
  for (int g = 0; g < 4; ++g) *(LAS f32x4*)(p + g * 1024) = f32x4{a[4 * g], a[4 * g + 1], a[4 * g + 2], a[4 * g + 3]};
}
DI void add_tile(f32x16& a, const LAS unsigned char* p) {
#pragma unroll
  for (int g = 0; g < 4; ++g) {
    const f32x4 v = *(const LAS f32x4*)(p + g * 1024);
    a[4 * g] += v[0]; a[4 * g + 1] += v[1]; a[4 * g + 2] += v[2]; a[4 * g + 3] += v[3];
  }
}

#define STAMP_DECL do {} while (0)
#define STAMP(id) do {} while (0)
#define QSTAMP(id) do {} while (0)
#define STAMP_FLUSH(P, ph) do {} while (0)

DI unsigned lds_add(LAS unsigned char* p) { return __hip_atomic_fetch_add((LAS unsigned*)p, 1u, __ATOMIC_RELAXED, __HIP_MEMORY_SCOPE_WORKGROUP); }

DI void role_a(const Params& P, LAS unsigned char* lds, const int wt) {
  u64 ep = 0;
  const int lane = threadIdx.x & 63, r = lane & 31, h = lane >> 5;
  const int b = blockIdx.x, xcd = b & 7, idx = b >> 3;
  const unsigned lo = lane * 16;
  STAMP_DECL;
  STAMP(0);
  __builtin_amdgcn_s_setprio(2);
  f32x16 acc[4];
  const int rb = (xcd & 1) * 8 + (idx & 7), j = (xcd >> 1) * 4 + (idx >> 3);
  {
    unsigned xoff[4];
#pragma unroll
    for (int ks = 0; ks < 4; ++ks) xoff[ks] = (unsigned)(L_X + wt * 4096 + r * 128 + (((2 * ks + h) ^ ((r >> 1) & 7)) << 4));
    const unsigned wqoff = (unsigned)(L_WQ + (8 * h + ((lane & 15) >> 2)) * 192 + ((lane >> 4) & 1) * 32 + (lane & 3) * 8);
#pragma unroll
    for (int i = 0; i < 3; ++i) acc[i] = f32x16{};
    auto kloop = [&](auto nvc) {
      constexpr int NV = decltype(nvc)::value;
      bf16x8 xf[4], wf[4][3];
      auto mm = [&](int ks, int ct) { acc[ct] = ct >= 3 - NV ? MFMA(xf[ks], wf[ks][ct], acc[ct]) : MFMA(wf[ks][ct], xf[ks], acc[ct]); };
      auto step = [&](int c, int ks, bool domm) {
        const int km = (ks + 2) & 3;
#pragma unroll
        for (int ct = 0; ct < 3; ++ct) {
          if (domm) mm(km, ct);
          SBAR();
          if (ct == 0) xf[ks] = *(const LAS bf16x8*)(lds + xoff[ks] + c * 16384);
          wf[ks][ct] = tr_frag(lds + wqoff + (c & 3) * 12288 + ks * 16 * 192 + ct * 56, 4 * 192);
          SBAR();
        }
      };
#pragma unroll
      for (int c = 0; c < 7; ++c) {
        if (c == 0) ep = wait_bar_epoch<63>(P.ws + FL_OFF);
        else bar_lds();
        if (c == 0) STAMP(1);
        QSTAMP(1 + c);
        step(c, 0, c > 0);
        step(c, 1, c > 0);
        step(c, 2, true);
        step(c, 3, true);
      }
      bar_lds();
      QSTAMP(8);
#pragma unroll
      for (int ct = 0; ct < 3; ++ct) { mm(2, ct); mm(3, ct); }
    };
    if (j < 10) kloop(IC<0>{});
    else if (j == 10) kloop(IC<1>{});
    else kloop(IC<3>{});
  }
  STAMP(2);
  const u64 tag = (ep + 1) & 0xffffffffffffull;
  unsigned char* const wsb = P.ws + SET_BASE + ((unsigned)tag & 3u) * SET_STRIDE;
  const __amdgpu_buffer_rsrc_t rsWs = mk_rsrc(wsb, WS_END);
  {
    const int rt = rb * 4 + wt;
    float kmax2 = 0.f;
#pragma unroll
    for (int ct = 0; ct < 3; ++ct) {
      const int u = 3 * j + ct, part = u >> 4, hd = u & 15;
      f32x16 t = acc[ct];
      if (part == 2) {
        const unsigned padv = r == DH ? 0x3f803f80u : 0u;
        u32x4 v0 = as_u4(pack8(t, 0)), v1 = as_u4(pack8(t, 1));
#pragma unroll
        for (int i = 0; i < 4; ++i) { v0[i] = r < DH ? v0[i] : padv; v1[i] = r < DH ? v1[i] : padv; }
        st16(rsWs, v0, lo + 2048, KV_OFF + (hd * 64 + rt) * 4096);
        st16(rsWs, v1, lo + 3072, KV_OFF + (hd * 64 + rt) * 4096);
      } else {
        const float sc = part == 0 ? QSCALE : 1.f;
        float na = 0.f, nb = 0.f;
#pragma unroll
        for (int i = 0; i < 16; ++i) {
          t[i] *= sc;
          if (i < 12) na = fmaf(t[i], t[i], na);
          else nb = fmaf(t[i], t[i], nb);
        }
        const unsigned so = part == 0 ? QF_OFF + (hd * 64 + rt) * 2048 : KV_OFF + (hd * 64 + rt) * 4096;
        st16(rsWs, as_u4(pack8(t, 0)), lo, so);
        na += h ? 0.f : nb;
        na += swap_half(na);
        u32x4 t1 = as_u4(pack8(t, 1));
        t1[2] = h ? (part == 0 ? cvtpk(__builtin_amdgcn_sqrtf(na), 0.f) : 0x3f803f80u) : t1[2];
        t1[3] = h ? (part == 0 ? 0u : 0x00003f80u) : t1[3];
        st16(rsWs, t1, lo + 1024, so);
        kmax2 = part == 1 ? fmaxf(kmax2, na) : kmax2;
      }
    }
    kmax2 = row16_max(kmax2);
    kmax2 = fmaxf(rdlane(kmax2, 0), rdlane(kmax2, 16));
    if (lane == 0) __hip_atomic_fetch_max((LAS unsigned*)(lds + L_CTRL + 12), __float_as_uint(kmax2), __ATOMIC_RELAXED, __HIP_MEMORY_SCOPE_WORKGROUP);
    STAMP(8);
    drain();
    STAMP(9);
    if (lane == 0) {
      if (lds_add(lds + L_CTRL + 8) == 3u) {
        const unsigned km = __hip_atomic_load((LAS unsigned*)(lds + L_CTRL + 12), __ATOMIC_RELAXED, __HIP_MEMORY_SCOPE_WORKGROUP);
        put_granule(wsb, GQ_OFF + (j * 16 + rb) * 128, tag, cvtpk(__uint_as_float(km), 0.f));
      }
    }
    STAMP(3);
  }
  const int head = xcd * 2 + (idx & 1), qb = idx >> 1;
  float kmax;
  if (wt == 0) {
    const int grp = lane < 16 ? (16 + head) / 3 : lane < 32 ? (32 + head) / 3 : head / 3;
    float kn = __uint_as_float(poll_granule<POLL_A>(wsb, GQ_OFF + (grp * 16 + (h ? qb : (lane & 15))) * 128, tag) << 16);
    kn = row16_max(kn);
    kmax = __builtin_amdgcn_sqrtf(rdlane(kn, 0));
    if (lane == 0) *(volatile LAS unsigned*)(lds + L_CTRL + 4) = __float_as_uint(kmax) | 1u;
  } else {
    kmax = __uint_as_float(wait_lds_word(lds + L_CTRL + 4, 1));
  }
  kmax = __uint_as_float(__float_as_uint(kmax) | 1u);
  STAMP(4);
  u32x4 q[4][2];
  const unsigned kvbase = KV_OFF + (head * 64 + wt * 16) * 4096;
  auto kvo = [&](int kt) { return kvbase + (unsigned)((kt + qb) & 15) * 4096; };
  auto qo = [&](int t) { return QF_OFF + (unsigned)(head * 64 + qb * 4 + ((wt + 1 + t) & 3)) * 2048; };
  u32x4 kv[4][4];
#pragma unroll
  for (int t = 0; t < 4; ++t) q[t][1] = ld16c(rsWs, lo + 1024, qo(t));
  SBAR();
  kv[0][0] = ld16c(rsWs, lo, kvo(0));
  kv[0][1] = ld16c(rsWs, lo + 1024, kvo(0));
  SBAR();
#pragma unroll
  for (int t = 0; t < 4; ++t) { q[t][0] = ld16c(rsWs, lo, qo(t)); SBAR(); }
#pragma unroll
  for (int e = 2; e < 12; ++e) { kv[e >> 2][e & 3] = ld16c(rsWs, lo + (e & 3) * 1024, kvo(e >> 2)); SBAR(); }
#pragma unroll
  for (int i = 0; i < 4; ++i) acc[i] = f32x16{};
  float m[4];
  bool unsafe = false;
#pragma unroll
  for (int t = 0; t < 4; ++t) {
    m[t] = __uint_as_float(q[t][1][2] << 16) * kmax;
    unsafe |= h && !(m[t] <= SAFE_BOUND);
  }
  if (__builtin_expect(__any(unsafe), 0)) {
    float mx[4];
#pragma unroll
    for (int t = 0; t < 4; ++t) { mx[t] = -INFINITY; if (h) { q[t][1][2] = 0; q[t][1][3] = 0; } }
    for (int kt = 0; kt < 64; ++kt) {
      const u32x4 k0 = ld16c(rsWs, lo, KV_OFF + (head * 64 + kt) * 4096), k1 = ld16c(rsWs, lo + 1024, KV_OFF + (head * 64 + kt) * 4096);
#pragma unroll
      for (int t = 0; t < 4; ++t) {
        f32x16 s = MFMA(as_frag(k0), as_frag(q[t][0]), f32x16{});
        s = MFMA(as_frag(k1), as_frag(q[t][1]), s);
#pragma unroll
        for (int i = 0; i < 16; ++i) mx[t] = fmaxf(mx[t], s[i]);
      }
    }
#pragma unroll
    for (int t = 0; t < 4; ++t) m[t] = fmaxf(mx[t], swap_half(mx[t]));
  }
#pragma unroll
  for (int t = 0; t < 4; ++t) {
    const float nm = -m[t];
    const unsigned hi = __float_as_uint(nm) & 0xffff0000u;
    const float r1 = nm - __uint_as_float(hi);
    const unsigned mid = __float_as_uint(r1) & 0xffff0000u;
    const float r2 = r1 - __uint_as_float(mid);
    if (h) { q[t][1][2] = (hi >> 16) | mid; q[t][1][3] = __float_as_uint(r2) >> 16; }
  }
  STAMP(5);
  __builtin_amdgcn_s_setprio(1);
  f32x16 sc = MFMA(as_frag(kv[0][0]), as_frag(q[0][0]), f32x16{});
  sc = MFMA(as_frag(kv[0][1]), as_frag(q[0][1]), sc);
  u32x4 p0, p1 = {0, 0, 0, 0};
  float e0 = 0.f, e1 = 0.f, e2 = 0.f, e3 = 0.f;
  u64 gv = 0;
  LAS unsigned char* const cmb = lds + L_CMB + wt * 12288 + lo;
#define EXP4(i) do { e0 = __builtin_amdgcn_exp2f(sc[i]); e1 = __builtin_amdgcn_exp2f(sc[(i) + 1]); e2 = __builtin_amdgcn_exp2f(sc[(i) + 2]); e3 = __builtin_amdgcn_exp2f(sc[(i) + 3]); \
    asm volatile("" : "+v"(e0), "+v"(e1), "+v"(e2), "+v"(e3)); SBAR(); } while (0)
#define CVT2(P, i) do { P[i] = cvtpk(e0, e1); P[(i) + 1] = cvtpk(e2, e3); asm volatile("" : "+v"(P)); SBAR(); } while (0)
#define PART(t, g) do { *(LAS f32x4*)(cmb + (t) * 4096 + (g) * 1024) = f32x4{acc[t][4 * (g)], acc[t][4 * (g) + 1], acc[t][4 * (g) + 2], acc[t][4 * (g) + 3]}; SBAR(); } while (0)
  static_for<0, 64>([&](auto ic) {
    constexpr int i = decltype(ic)::value;
    constexpr int kt = i >> 2, t = i & 3, kn_ = (i + 1) >> 2, tn = (i + 1) & 3, kp = (i - 1) >> 2, tp = (i - 1) & 3;
    if (i == 24) STAMP(11);
    if (i == 56) (void)wait_lds_word(lds + L_CTRL, 4);
    f32x16 sn;
    SBAR();
    if (i < 63) sn = MFMA(as_frag(kv[kn_ & 3][0]), as_frag(q[tn][0]), f32x16{});
    SBAR();
    if constexpr (i >= 62) PART(i - 62, 0);
    if (i > 0) CVT2(p1, 2);
    EXP4(0);
    if (i > 0) acc[tp] = MFMA(as_frag(kv[kp & 3][3]), as_frag(p1), acc[tp]);
    SBAR();
    if (kt + 3 < 16) kv[(kt + 3) & 3][t] = ld16c(rsWs, lo + t * 1024, kvo(kt + 3));
    if (i == 52) gv = __hip_atomic_load((gu64*)(wsb + GG_OFF + ((qb * 4 + wt) * 32 + r) * 8), __ATOMIC_RELAXED, __HIP_MEMORY_SCOPE_AGENT);
    SBAR();
    if constexpr (i >= 62) PART(i - 62, 1);
    CVT2(p0, 0);
    EXP4(4);
    if (i < 63) sn = MFMA(as_frag(kv[kn_ & 3][1]), as_frag(q[tn][1]), sn);
    SBAR();
    if constexpr (i >= 62) PART(i - 62, 2);
    CVT2(p0, 2);
    EXP4(8);
    acc[t] = MFMA(as_frag(kv[kt & 3][2]), as_frag(p0), acc[t]);
    SBAR();
    if constexpr (i >= 62) PART(i - 62, 3);
    CVT2(p1, 0);
    EXP4(12);
    if (i < 63) sc = sn;
  });
  CVT2(p1, 2);
  acc[3] = MFMA(as_frag(kv[3][3]), as_frag(p1), acc[3]);
  SBAR();
  PART(2, 0); PART(2, 1); PART(2, 2); PART(2, 3);
#undef EXP4
#undef CVT2
#undef PART
  __builtin_amdgcn_s_setprio(2);
  STAMP(6);
  asm volatile("s_waitcnt lgkmcnt(0)" ::: "memory");
  if (lane == 0) (void)lds_add(lds + L_CTRL + 20);
  unsigned gp = (unsigned)gv & 0xffffu;
  if (!__all((gv >> 16) == tag)) gp = poll_granule<2>(wsb, GG_OFF + ((qb * 4 + wt) * 32 + r) * 8, tag);
  (void)wait_lds_word(lds + L_CTRL + 20, 4);
  f32x16 o = acc[3];
#pragma unroll
  for (int u = 0; u < 3; ++u) add_tile(o, lds + L_CMB + ((wt + 3 - u) & 3) * 12288 + u * 4096 + lo);
  const float lsw = swap_half(o[12]);
  const float inv = __uint_as_float(gp << 16) * __builtin_amdgcn_rcpf(h ? o[12] : lsw);
#pragma unroll
  for (int i = 0; i < 16; ++i) o[i] *= inv;
  u32x4 o1 = as_u4(pack8(o, 1));
  o1[2] = h ? 0u : o1[2];
  o1[3] = h ? 0u : o1[3];
  const unsigned so = OF_OFF + ((qb * 4 + wt) * 16 + head) * 2048;
  st16(rsWs, as_u4(pack8(o, 0)), lo, so);
  st16(rsWs, o1, lo + 1024, so);
  STAMP(7);
  drain();
  if (lane == 0) put_granule(wsb, GO_OFF + ((qb * 4 + wt) * 16 + head) * 128, tag, 0);
  STAMP(10);
  STAMP_FLUSH(P, 0);
}

constexpr int W2S = 6;
constexpr int HPRE = 5;
#define LE0 10
#define LU0 6
#define PACE 4
#define PACE2 2
constexpr int ffn2_ops(int f) { return (f + W2S < 14 ? 4 : 0) + 4; }
constexpr int ffn2_younger(int f, int upto) {
  int n = 4;
  for (int i = f - W2S + 1; i < upto; ++i) n += ffn2_ops(i);
  return n;
}

DI void role_f(const Params& P, LAS unsigned char* lds, const int wt) {
  u64 ep;
  const int lane = threadIdx.x & 63, r = lane & 31, h = lane >> 5;
  const int b = blockIdx.x, xcd = b & 7, idx = b >> 3;
  const int rb = (xcd & 1) * 8 + (idx & 7), j = (xcd >> 1) * 4 + (idx >> 3);
  const unsigned lo = lane * 16, lds0 = (unsigned)(uintptr_t)lds;
  const bool ffn = j < 14;
  STAMP_DECL;
  STAMP(0);
  f32x16 acc[4];
  u32x4 wl[28];
  const __amdgpu_buffer_rsrc_t rsL = mk_rsrc(ffn ? P.w1 : P.gw, ffn ? DM * FF * 2 : DM * 2);
  const unsigned lsrc = ffn ? (unsigned)((lane >> 2) * (FF * 2) + (j * 4 + wt) * 64 + (lane & 3) * 16) : (unsigned)(h * 16);
  const unsigned lstep = ffn ? 16 * FF * 2 : 32;
  const int rot = ROT1(rb, j);
  auto kc = [&](int c) { const int v = c + rot; return v >= 7 ? v - 7 : v; };
  auto L = [&](int u0, int u1) {
#pragma unroll
    for (int u = u0; u < u1; ++u) wl[u] = ld16(rsL, lsrc, (kc(u >> 2) * 4 + (u & 3)) * lstep);
    SBAR();
  };
  {
    const __amdgpu_buffer_rsrc_t rsX = mk_rsrc(P.x, SEQ * DM * 2);
    const __amdgpu_buffer_rsrc_t rsW = mk_rsrc(P.wqkv, DM * 3 * DM * 2);
    unsigned xsrc[2], wsrc[3];
#pragma unroll
    for (int i = 0; i < 2; ++i) {
      const int row = 32 * wt + 8 * i + (lane >> 3), ch = (lane & 7) ^ ((row >> 1) & 7);
      xsrc[i] = (unsigned)((rb * 128 + row) * (DM * 2) + ch * 16);
    }
#pragma unroll
    for (int i = 0; i < 3; ++i) {
      const int p = 64 * (3 * wt + i) + lane, kr = p / 12, ch = p % 12;
      wsrc[i] = (unsigned)(kr * (3 * DM * 2) + j * 168 + (ch < 11 ? ch : 10) * 16);
    }
    auto X = [&](int c) {
#pragma unroll
      for (int i = 0; i < 4; ++i) glds16(rsX, lds0 + L_X + c * 16384 + (4 * wt + i) * 1024, xsrc[i & 1], kc(c) * 128 + (i >> 1) * 16 * DM * 2);
    };
    auto W = [&](int c) {
#pragma unroll
      for (int i = 0; i < 3; ++i) glds16(rsW, lds0 + L_WQ + (c & 3) * 12288 + (3 * wt + i) * 1024, wsrc[i], kc(c) * 64 * 3 * DM * 2);
    };
    X(0); W(0); X(1); W(1); X(2);
    QSTAMP(1);
    wait_bar<11>();
    STAMP(1);
    W(2); X(3); W(3);
    QSTAMP(2);
    wait_bar<14>();
    X(4); W(4);
    QSTAMP(3);
    wait_bar<14>();
    X(5); W(5);
    QSTAMP(4);
    wait_bar<14>();
    X(6); W(6);
    QSTAMP(5);
    wait_bar<14>();
    if (wt == 0 && lane < 8) *(LAS unsigned*)(lds + L_CTRL + lane * 4) = 0u;
    L(0, 5);
    QSTAMP(6);
    ep = wait_bar_epoch<12>(P.ws + FL_OFF);
    L(5, LE0);
    QSTAMP(7);
    wait_bar<LE0>();
    QSTAMP(8);
    bar_lds();
  }
  STAMP(2);
  const u64 tag = (ep + 1) & 0xffffffffffffull;
  unsigned char* const wsb = P.ws + SET_BASE + ((unsigned)tag & 3u) * SET_STRIDE;
  const __amdgpu_buffer_rsrc_t rsWs = mk_rsrc(wsb, WS_END);
  unsigned xoff[4];
#pragma unroll
  for (int ks = 0; ks < 4; ++ks) xoff[ks] = (unsigned)(L_X + r * 128 + (((2 * ks + h) ^ ((r >> 1) & 7)) << 4));
  if (ffn) {
    LAS unsigned char* ring = lds + L_W1 + wt * 8192;
    const unsigned troff = (unsigned)((8 * h + ((lane & 15) >> 2)) * 64 + ((lane >> 4) & 1) * 32 + (lane & 3) * 8);
#pragma unroll
    for (int i = 0; i < 4; ++i) acc[i] = f32x16{};
    bf16x8 xf[2][4], wf[2];
    auto wr = [&](int u) { *(LAS u32x4*)(ring + (u & 7) * 1024 + lo) = wl[u]; };
    auto rdw = [&](int u) { wf[u & 1] = tr_frag(ring + (u & 7) * 1024 + troff, 4 * 64); };
    auto rdx = [&](int u, int t) { xf[u & 1][t] = *(const LAS bf16x8*)(lds + xoff[u & 3] + (u >> 2) * 16384 + t * 4096); };
    wr(0); wr(1); wr(2);
    rdw(0);
#pragma unroll
    for (int t = 0; t < 4; ++t) rdx(0, t);
    SBAR();
#pragma unroll
    for (int u = 0; u < 28; ++u) {
#pragma unroll
      for (int t = 0; t < 4; ++t) {
        if (u + 1 < 28) {
          if (t == 0) rdw(u + 1);
          rdx(u + 1, t);
          if (t == 3 && u + 3 < 28) wr(u + 3);
        }
        SBAR();
        acc[t] = MFMA(wf[u & 1], xf[u & 1][t], acc[t]);
        SBAR();
        if (u >= LU0 && LE0 + (u - LU0) * 4 + t < 28) L(LE0 + (u - LU0) * 4 + t, LE0 + (u - LU0) * 4 + t + 1);
      }
    }
    if (lane == 0) (void)lds_add(lds + L_CTRL);
    STAMP(3);
    __builtin_amdgcn_s_setprio(3);
#pragma unroll
    for (int t = 0; t < 4; ++t) {
      f32x16 z = acc[t];
#pragma unroll
      for (int i = 0; i < 16; ++i) z[i] = z[i] * __builtin_amdgcn_rcpf(1.f + __builtin_amdgcn_exp2f(-LOG2E * z[i]));
      const unsigned so = HF_OFF + ((rb * 4 + t) * 56 + j * 4 + wt) * 2048;
      st16(rsWs, as_u4(pack8(z, 0)), lo, so);
      st16(rsWs, as_u4(pack8(z, 1)), lo + 1024, so);
    }
    drain();
    if (lane == 0) {
      if (lds_add(lds + L_CTRL + 16) == 3u) put_granule(wsb, GH_OFF + (rb * 16 + j) * 128, tag, 0);
    }
  } else {
    __builtin_amdgcn_s_setprio(3);
    if (j == 14) {
      L(LE0, 28);
      acc[0] = f32x16{};
#pragma unroll
      for (int u = 0; u < 28; ++u) {
        const bf16x8 xf = *(const LAS bf16x8*)(lds + xoff[u & 3] + (u >> 2) * 16384 + wt * 4096);
        acc[0] = MFMA(as_frag(wl[u]), xf, acc[0]);
      }
      const float g = __builtin_amdgcn_rcpf(1.f + __builtin_amdgcn_exp2f(-LOG2E * acc[0][0]));
      if (h == 0) put_granule(wsb, GG_OFF + ((rb * 4 + wt) * 32 + r) * 8, tag, cvtpk(g, 0.f));
    }
    if (lane == 0) (void)lds_add(lds + L_CTRL);
    const __amdgpu_buffer_rsrc_t rsO = mk_rsrc(P.wo, DM * DM * 2);
    const int id0 = ((j - 14) * 16 + rb) * 4 + wt;
    for (int id = id0; id < 224; id += 128) {
      const int nt = id >> 4, hd = id & 15;
      unsigned rp[16];
#pragma unroll
      for (int e = 0; e < 16; ++e)
        rp[e] = __builtin_amdgcn_raw_buffer_load_b16(rsO, (unsigned)(((4 * h + 8 * (e >> 2) + (e & 3)) * DM + r) * 2), (unsigned)((DH * hd * DM + 32 * nt) * 2), 0);
      u32x4 a, c2;
#pragma unroll
      for (int i = 0; i < 4; ++i) {
        a[i] = (rp[2 * i] & 0xffffu) | (rp[2 * i + 1] << 16);
        c2[i] = (rp[8 + 2 * i] & 0xffffu) | (rp[8 + 2 * i + 1] << 16);
      }
      c2[2] = h ? 0u : c2[2];
      c2[3] = h ? 0u : c2[3];
      st16(rsWs, a, lo, WOF_OFF + id * 2048);
      st16(rsWs, c2, lo + 1024, WOF_OFF + id * 2048);
      drain();
      if (lane == 0) put_granule(wsb, GW_OFF + id * 8, tag, 0);
    }
  }
  STAMP(4);
  if (idx >= 28) {
    if (b == 255 && wt == 0) {
      __builtin_amdgcn_s_sleep(127);
#pragma unroll
      for (int i = 0; i < 4; ++i) (void)poll_granule<64>(wsb, GQ_OFF + (i * 64 + lane) * 128, tag);
      if (lane == 0) __hip_atomic_store((gu64*)(P.ws + FL_OFF), ep + 1, __ATOMIC_RELAXED, __HIP_MEMORY_SCOPE_AGENT);
    }
    STAMP(10);
    STAMP_FLUSH(P, 0);
    return;
  }
  const int mb = 4 * xcd + (idx & 3), nb = idx >> 2;
  const __amdgpu_buffer_rsrc_t rsW2 = mk_rsrc(P.w2, FF * DM * 2);
  const unsigned w2src = (unsigned)((wt * 448 + (lane >> 3)) * (DM * 2) + nb * 128 + ((((lane >> 2) & 1) ^ ((lane >> 4) & 1)) * 64) + (lane & 3) * 16);
  const unsigned ring0 = lds0 + L_W2 + wt * 24576;
  const int rot2 = (2 * nb + 3 * (idx & 3)) % 14;
  auto fr = [&](int f) { const int v = f + rot2; return v >= 14 ? v - 14 : v; };
  auto D = [&](int f) {
#pragma unroll
    for (int i = 0; i < 4; ++i) glds16(rsW2, ring0 + (f % W2S) * 4096 + i * 1024, w2src, (fr(f) * 32 + i * 8) * DM * 2);
  };
  (void)wait_lds_word(lds + L_CTRL, 4);
#pragma unroll
  for (int f = 0; f < W2S; ++f) { D(f); __builtin_amdgcn_s_sleep(PACE); }
  (void)poll_granule<8>(wsb, (lane & 16) ? GW_OFF + ((nb * 2 + ((lane >> 2) & 1)) * 16 + wt * 4 + (lane & 3)) * 8
                                        : GH_OFF + ((mb >> 1) * 16 + ((wt * 14 + ((lane & 15) < 14 ? (lane & 15) : 0)) >> 2)) * 128, tag);
  STAMP(5);
  u32x4 hb[HPRE][4], wo[16];
  const unsigned hbase = HF_OFF + (mb * 2 * 56 + wt * 14) * 2048;
  auto H = [&](int f) {
#pragma unroll
    for (int e = 0; e < 4; ++e) hb[f % HPRE][e] = ld16c(rsWs, lo + (e & 1) * 1024, hbase + ((e >> 1) * 56 + fr(f)) * 2048);
  };
#pragma unroll
  for (int f = 0; f < HPRE; ++f) { H(f); SBAR(); }
  const float gl = __uint_as_float(poll_granule<8>(wsb, GG_OFF + (mb * 64 + lane) * 8, tag) << 16), gs = swap_half(gl);
  const float og0 = 1.f - (h ? gs : gl), og1 = 1.f - (h ? gl : gs);
  const int q4 = (lane & 15) >> 2;
  unsigned woff[2];
#pragma unroll
  for (int nt = 0; nt < 2; ++nt) woff[nt] = (unsigned)(L_W2 + wt * 24576 + (4 * h + q4) * 128 + ((nt ^ (q4 >> 1)) * 64) + ((lane >> 4) & 1) * 32 + (lane & 3) * 8);
#pragma unroll
  for (int i = 0; i < 4; ++i) acc[i] = f32x16{};
  STAMP(6);
  __builtin_amdgcn_s_setprio(0);
  bf16x8 wf[2][2][2];
  auto RW = [&](int f) {
#pragma unroll
    for (int s = 0; s < 2; ++s)
#pragma unroll
      for (int nt = 0; nt < 2; ++nt) wf[f & 1][s][nt] = tr_frag(lds + woff[nt] + (f % W2S) * 4096 + s * 2048, 1024);
  };
  RW(0);
  static_for<0, 14>([&](auto fc) {
    constexpr int f = decltype(fc)::value;
    asm volatile("s_waitcnt lgkmcnt(0)" ::: "memory");
    SBAR();
    if constexpr (f + 1 < 14) {
      if constexpr (f + 1 >= W2S) wait_vm<ffn2_younger(f + 1, f)>();
      RW(f + 1);
    }
    SBAR();
#pragma unroll
    for (int s = 0; s < 2; ++s)
#pragma unroll
      for (int nt = 0; nt < 2; ++nt)
#pragma unroll
        for (int mt = 0; mt < 2; ++mt) acc[nt * 2 + mt] = MFMA(wf[f & 1][s][nt], as_frag(hb[f % HPRE][mt * 2 + s]), acc[nt * 2 + mt]);
    SBAR();
    if constexpr (f + W2S < 14) D(f + W2S);
    if constexpr (f + HPRE < 14) H(f + HPRE);
    else if constexpr (f + HPRE < 18) {
      constexpr int e0 = (f + HPRE - 14) * 4;
#pragma unroll
      for (int e = e0; e < e0 + 4; ++e) wo[e] = ld16c(rsWs, lo, WOF_OFF + ((nb * 2 + (e >> 3)) * 16 + wt * 4) * 2048 + (e & 7) * 1024);
    }
    SBAR();
    __builtin_amdgcn_s_sleep(PACE2);
    SBAR();
  });
  STAMP(7);
  __builtin_amdgcn_s_setprio(3);
#pragma unroll
  for (int i = 0; i < 16; ++i) { acc[0][i] *= og0; acc[1][i] *= og1; acc[2][i] *= og0; acc[3][i] *= og1; }
  (void)wait_lds_word(lds + L_CTRL + 20, 1);
  (void)poll_granule<POLL_A>(wsb, GO_OFF + ((mb * 2 + ((lane >> 2) & 1)) * 16 + wt * 4 + (lane & 3)) * 128, tag);
  STAMP(8);
  u32x4 of[16];
#pragma unroll
  for (int e = 0; e < 8; ++e)
#pragma unroll
    for (int mt = 0; mt < 2; ++mt) of[mt * 8 + e] = ld16c(rsWs, lo, OF_OFF + ((mb * 2 + mt) * 16 + wt * 4) * 2048 + e * 1024);
  SBAR();
#pragma unroll
  for (int e = 0; e < 8; ++e)
#pragma unroll
    for (int nt = 0; nt < 2; ++nt)
#pragma unroll
      for (int mt = 0; mt < 2; ++mt) acc[nt * 2 + mt] = MFMA(as_frag(wo[nt * 8 + e]), as_frag(of[mt * 8 + e]), acc[nt * 2 + mt]);
#pragma unroll
  for (int t = 0; t < 4; ++t) st_tile(lds + L_W2 + wt * 24576 + t * 4096 + lo, acc[t]);
  STAMP(9);
  bar_lds();
  f32x16 a = f32x16{};
#pragma unroll
  for (int f = 0; f < 4; ++f) add_tile(a, lds + L_W2 + f * 24576 + wt * 4096 + lo);
  unsigned short* dst = P.out + (size_t)((mb * 2 + (wt & 1)) * 32 + r) * DM + (nb * 2 + (wt >> 1)) * 32 + 8 * h;
#pragma unroll
  for (int k = 0; k < 2; ++k) {
    const auto x = __builtin_amdgcn_permlane32_swap(cvtpk(a[8 * k], a[8 * k + 1]), cvtpk(a[8 * k + 4], a[8 * k + 5]), false, false);
    const auto y = __builtin_amdgcn_permlane32_swap(cvtpk(a[8 * k + 2], a[8 * k + 3]), cvtpk(a[8 * k + 6], a[8 * k + 7]), false, false);
    *(u32x4*)(dst + 16 * k) = u32x4{x[0], y[0], x[1], y[1]};
  }
  STAMP(10);
  STAMP_FLUSH(P, 0);
}

extern __shared__ __attribute__((aligned(16))) unsigned char g_lds[];

__global__ __launch_bounds__(512, 2) void k_fused(Params P) {
  LAS unsigned char* lds = (LAS unsigned char*)g_lds;
  const int w = __builtin_amdgcn_readfirstlane(threadIdx.x >> 6);
  if (w < 4) role_a(P, lds, w);
  else role_f(P, lds, w - 4);
}

extern "C" void kernel_launch(void* const* d_in, const int* in_sizes, int n_in, void* d_out, int out_size, void* d_ws,
                              size_t ws_size, hipStream_t stream) {
  Params P;
  P.x = (const unsigned short*)d_in[0];
  P.wqkv = (const unsigned short*)d_in[1];
  P.wo = (const unsigned short*)d_in[2];
  P.w1 = (const unsigned short*)d_in[4];
  P.w2 = (const unsigned short*)d_in[5];
  P.gw = (const unsigned short*)d_in[6];
  P.out = (unsigned short*)d_out;
  P.ws = (unsigned char*)d_ws;
  (void)hipFuncSetAttribute((const void*)k_fused, hipFuncAttributeMaxDynamicSharedMemorySize, LDS_BYTES);
  k_fused<<<256, 512, LDS_BYTES, stream>>>(P);
}
```

```cpp
#define ROT1(rb,j) (((rb) + 2 * (j)) % 7)
#include <hip/hip_runtime.h>
#include <hip/hip_bf16.h>
#include <stdint.h>

typedef short bf16x8 __attribute__((ext_vector_type(8)));
typedef short s16x4 __attribute__((ext_vector_type(4)));
typedef float f32x16 __attribute__((ext_vector_type(16)));
typedef float f32x4 __attribute__((ext_vector_type(4)));
typedef unsigned u32x4 __attribute__((ext_vector_type(4)));
typedef unsigned u32x2 __attribute__((ext_vector_type(2)));
#define LAS __attribute__((address_space(3)))
#define DI __device__ __forceinline__
#define MFMA(a, b, c) __builtin_amdgcn_mfma_f32_32x32x16_bf16((a), (b), (c), 0, 0, 0)
#define SBAR() __builtin_amdgcn_sched_barrier(0)

constexpr int SEQ = 2048, DM = 448, NH = 16, DH = 28, FF = 1792;
constexpr unsigned FL_OFF = 0;
#define SETB 1u
constexpr unsigned SET_BASE = SETB << 20, SET_STRIDE = 24u << 20;
#define GQS 16u
constexpr unsigned GQW = GQS == 16u ? 128u : 4u * GQS;
constexpr unsigned GQ_OFF = 0;
constexpr unsigned GH_OFF = GQ_OFF + 256 * GQW;
constexpr unsigned GO_OFF = GH_OFF + 256 * 128;
constexpr unsigned GW_OFF = GO_OFF + 1024 * 128;
constexpr unsigned GG_OFF = GW_OFF + 256 * 8;
constexpr unsigned QF_OFF = 524288;
constexpr unsigned KV_OFF = QF_OFF + 16 * 64 * 2048;
constexpr unsigned HF_OFF = KV_OFF + 16 * 64 * 4096;
constexpr unsigned WOF_OFF = HF_OFF + 64 * 56 * 2048;
constexpr unsigned OF_OFF = WOF_OFF + 14 * 16 * 2048;
constexpr unsigned WS_END = OF_OFF + 64 * 16 * 2048;
static_assert(GG_OFF + 2048 * 8 <= QF_OFF && WS_END <= SET_STRIDE, "workspace map");

constexpr int L_X = 0;
constexpr int L_WQ = 114688;
constexpr int L_W1 = 114688;
constexpr int L_CTRL = 163776;
constexpr int L_W2 = 0;
constexpr int L_CMB = 98304;
constexpr int LDS_BYTES = 163840;

constexpr float QSCALE = 0.18898223650461362f * 1.4426950408889634f;
constexpr float LOG2E = 1.4426950408889634f;
constexpr float SAFE_BOUND = 60.0f;
constexpr unsigned SPIN_CAP = 1u << 18;
#define KVD 4
#define ROTA 0
#define ROT2 0
#define PDELAY 16
#define ODELAY 28
#define FDELAY 18
#define POLL_A 4

struct Params {
  const unsigned short* x;
  const unsigned short* wqkv;
  const unsigned short* wo;
  const unsigned short* w1;
  const unsigned short* w2;
  const unsigned short* gw;
  unsigned short* out;
  unsigned char* ws;
};
typedef unsigned long long u64;
typedef __attribute__((address_space(1))) u64 gu64;

DI unsigned cvtpk(float lo, float hi) {
  typedef __bf16 bf2 __attribute__((ext_vector_type(2)));
  typedef float f2 __attribute__((ext_vector_type(2)));
  f2 v = {lo, hi};
  bf2 r = __builtin_convertvector(v, bf2);
  return __builtin_bit_cast(unsigned, r);
}
DI bf16x8 pack8(const f32x16& x, int s) {
  u32x4 p;
  p[0] = cvtpk(x[8 * s + 0], x[8 * s + 1]);
  p[1] = cvtpk(x[8 * s + 2], x[8 * s + 3]);
  p[2] = cvtpk(x[8 * s + 4], x[8 * s + 5]);
  p[3] = cvtpk(x[8 * s + 6], x[8 * s + 7]);
  return __builtin_bit_cast(bf16x8, p);
}
DI float swap_half(float v) {
  auto r = __builtin_amdgcn_permlane32_swap(__float_as_uint(v), __float_as_uint(v), false, false);
  return (threadIdx.x & 32) ? __uint_as_float(r[0]) : __uint_as_float(r[1]);
}
template <int CTRL> DI float dpp_mov(float v) {
  return __uint_as_float(__builtin_amdgcn_update_dpp(__float_as_uint(v), __float_as_uint(v), CTRL, 0xf, 0xf, false));
}
DI float row16_max(float v) {
  v = fmaxf(v, dpp_mov<0x128>(v));
  v = fmaxf(v, dpp_mov<0x124>(v));
  v = fmaxf(v, dpp_mov<0x122>(v));
  v = fmaxf(v, dpp_mov<0x121>(v));
  return v;
}
DI unsigned row16_umax(unsigned v) {
  v = max(v, (unsigned)__builtin_amdgcn_update_dpp(v, v, 0x128, 0xf, 0xf, false));
  v = max(v, (unsigned)__builtin_amdgcn_update_dpp(v, v, 0x124, 0xf, 0xf, false));
  v = max(v, (unsigned)__builtin_amdgcn_update_dpp(v, v, 0x122, 0xf, 0xf, false));
  v = max(v, (unsigned)__builtin_amdgcn_update_dpp(v, v, 0x121, 0xf, 0xf, false));
  return v;
}
DI float rdlane(float v, int l) { return __uint_as_float(__builtin_amdgcn_readlane(__float_as_uint(v), l)); }
DI s16x4 tr_read(const LAS unsigned char* p) {
  typedef short v4i16_t __attribute__((ext_vector_type(4)));
  return __builtin_bit_cast(s16x4, __builtin_amdgcn_ds_read_tr16_b64_v4i16((LAS v4i16_t*)p));
}
DI bf16x8 tr_frag(const LAS unsigned char* p, int row_stride4) {
  s16x4 lo = tr_read(p), hi = tr_read(p + row_stride4);
  return __builtin_shufflevector(lo, hi, 0, 1, 2, 3, 4, 5, 6, 7);
}
DI __amdgpu_buffer_rsrc_t mk_rsrc(const void* p, unsigned bytes) {
  return __builtin_amdgcn_make_buffer_rsrc((void*)p, (short)0, (int)bytes, 0x00020000);
}
DI void glds16(__amdgpu_buffer_rsrc_t rs, unsigned dst, unsigned voff, unsigned soff) {
  asm volatile("s_mov_b32 m0, %0\n\ts_nop 0\n\tbuffer_load_dwordx4 %1, %2, %3 offen lds" ::"s"(dst), "v"(voff), "s"(rs), "s"(soff) : "memory");
}
DI u32x4 ld16(__amdgpu_buffer_rsrc_t rs, unsigned voff, unsigned soff) { return __builtin_amdgcn_raw_buffer_load_b128(rs, voff, soff, 0); }
DI u32x4 ld16c(__amdgpu_buffer_rsrc_t rs, unsigned voff, unsigned soff) { return __builtin_amdgcn_raw_buffer_load_b128(rs, voff, soff, 16); }
DI void st16(__amdgpu_buffer_rsrc_t rs, u32x4 v, unsigned voff, unsigned soff) { __builtin_amdgcn_raw_buffer_store_b128(v, rs, voff + soff, 0, 16); }
DI bf16x8 as_frag(u32x4 v) { return __builtin_bit_cast(bf16x8, v); }
DI u32x4 as_u4(bf16x8 v) { return __builtin_bit_cast(u32x4, v); }
template <int N> DI void wait_bar() { asm volatile("s_waitcnt vmcnt(%0) lgkmcnt(0)\n\ts_barrier" ::"n"(N) : "memory"); }

template <int V> struct IC { static constexpr int value = V; };
template <int I, int N, class Fn> DI void static_for(Fn&& fn) {
  if constexpr (I < N) { fn(IC<I>{}); static_for<I + 1, N>(fn); }
}
DI void drain() { asm volatile("s_waitcnt vmcnt(0)" ::: "memory"); }
template <int N> DI void wait_vm() { asm volatile("s_waitcnt vmcnt(%0)" ::"n"(N) : "memory"); }
DI void put_granule(unsigned char* ws, unsigned off, u64 tag16, unsigned payload) {
  __hip_atomic_store((gu64*)(ws + off), tag16 | (u64)(payload & 0xffffu), __ATOMIC_RELAXED, __HIP_MEMORY_SCOPE_AGENT);
}
template <int SLEEP> DI unsigned poll_granule(unsigned char* ws, unsigned off, u64 tag16) {
  u64 v = ~tag16;
  for (unsigned spins = 0;; ++spins) {
    if (v - tag16 > 0xffffull) v = __hip_atomic_load((gu64*)(ws + off), __ATOMIC_RELAXED, __HIP_MEMORY_SCOPE_AGENT);
    if (__all(v - tag16 <= 0xffffull) || spins > SPIN_CAP) break;
    __builtin_amdgcn_s_sleep(SLEEP);
  }
  __builtin_amdgcn_fence(__ATOMIC_ACQUIRE, "wavefront");
  return (unsigned)v & 0xffffu;
}
DI unsigned wait_lds_word(LAS unsigned char* p, unsigned want) {
  unsigned v = 0;
#pragma nounroll
  for (unsigned spins = 0; spins < (1u << 24); ++spins) {
    v = __builtin_amdgcn_readfirstlane(__hip_atomic_load((LAS unsigned*)p, __ATOMIC_RELAXED, __HIP_MEMORY_SCOPE_WORKGROUP));
    if (v >= want) break;
    __builtin_amdgcn_s_sleep(1);
  }
  asm volatile("" ::: "memory");
  return v;
}
template <int N> DI u64 wait_bar_epoch(const unsigned char* p) {
  u64 ep;
  asm volatile("s_load_dwordx2 %0, %1, 0x0\n\ts_waitcnt vmcnt(%2) lgkmcnt(0)\n\ts_barrier" : "=&s"(ep) : "s"(p), "n"(N) : "memory");
  return ep;
}
DI void bar_lds() { asm volatile("s_waitcnt lgkmcnt(0)\n\ts_barrier" ::: "memory"); }
DI void st_tile(LAS unsigned char* p, const f32x16& a) {
#pragma unroll
  for (int g = 0; g < 4; ++g) *(LAS f32x4*)(p + g * 1024) = f32x4{a[4 * g], a[4 * g + 1], a[4 * g + 2], a[4 * g + 3]};
}
DI void add_tile(f32x16& a, const LAS unsigned char* p) {
#pragma unroll
  for (int g = 0; g < 4; ++g) {
    const f32x4 v = *(const LAS f32x4*)(p + g * 1024);
    a[4 * g] += v[0]; a[4 * g + 1] += v[1]; a[4 * g + 2] += v[2]; a[4 * g + 3] += v[3];
  }
}

#define STAMP_DECL do {} while (0)
#define STAMP(id) do {} while (0)
#define QSTAMP(id) do {} while (0)
#define RSTAMP(id) do {} while (0)
#define STAMP_FLUSH(P, ph) do {} while (0)

DI void lds_arrive(LAS unsigned char* p) {
  asm volatile("s_mov_b64 exec, 1\n\tds_add_u32 %0, %1\n\ts_mov_b64 exec, -1" ::"v"((unsigned)(uintptr_t)p), "v"(1u) : "memory");
}

#define PINV(x) asm volatile("" : "+v"(x))
#define PINS(x) asm volatile("" : "+s"(x))
DI float sumsq(const u32x4& a, const u32x4& b) {
  typedef __bf16 bf2 __attribute__((ext_vector_type(2)));
  float n0 = 0.f, n1 = 0.f;
#pragma unroll
  for (int i = 0; i < 4; ++i) {
    n0 = __builtin_amdgcn_fdot2_f32_bf16(__builtin_bit_cast(bf2, a[i]), __builtin_bit_cast(bf2, a[i]), n0, false);
    n1 = __builtin_amdgcn_fdot2_f32_bf16(__builtin_bit_cast(bf2, b[i]), __builtin_bit_cast(bf2, b[i]), n1, false);
  }
  return n0 + n1;
}

DI void role_a(const Params& P, LAS unsigned char* lds, const int wt) {
  const int lane = threadIdx.x & 63, r = lane & 31, h = lane >> 5;
  const int b = blockIdx.x, xcd = b & 7, idx = b >> 3;
  const unsigned lo = lane * 16;
  STAMP_DECL;
  STAMP(0);
  __builtin_amdgcn_s_setprio(2);
  f32x16 acc[4];
  const int rb = (xcd & 1) * 8 + (idx & 7), j = (xcd >> 1) * 4 + (idx >> 3);
  const int head = xcd * 2 + (idx & 1), qb = idx >> 1;
  unsigned sto[3];
#pragma unroll
  for (int ct = 0; ct < 3; ++ct) {
    const int u = 3 * j + ct, part = u >> 4, t = (u & 15) * 64 + rb * 4 + wt;
    sto[ct] = lo + (part == 0 ? QF_OFF + t * 2048 : KV_OFF + t * 4096 + (part - 1) * 2048);
    PINV(sto[ct]);
  }
  unsigned padv = r == DH ? 0x3f803f80u : 0u;
  PINV(padv);
  const int gq = head / 3, gk = (16 + head) / 3, gvv = (32 + head) / 3;
  unsigned pub_off = GQ_OFF + (j * 16 + rb) * GQW + wt * GQS + (lane & 1) * 8;
  unsigned poll_off = GQ_OFF + (h ? gq * 16 + qb : (lane < 16 ? gk : gvv) * 16 + wt * 4 + ((lane >> 2) & 3)) * GQW + (lane & 3) * GQS + h * 8;
  unsigned chk_off = GQ_OFF + (gk * 16 + (lane >> 2)) * GQW + (lane & 3) * GQS;
  unsigned g_off = GG_OFF + ((qb * 4 + wt) * 32 + r) * 8;
  unsigned o_off = lo + OF_OFF + ((qb * 4 + wt) * 16 + head) * 2048;
  PINV(pub_off); PINV(poll_off); PINV(chk_off); PINV(g_off); PINV(o_off);
  unsigned kvbase = KV_OFF + (head * 64 + wt * 16) * 4096, go_off = GO_OFF + ((qb * 4 + wt) * 16 + head) * 128;
  unsigned qofs[4];
#pragma unroll
  for (int t = 0; t < 4; ++t) { qofs[t] = QF_OFF + (unsigned)(head * 64 + qb * 4 + ((wt + 1 + t) & 3)) * 2048; PINS(qofs[t]); }
  PINS(kvbase); PINS(go_off);
  unsigned xoff[4];
#pragma unroll
  for (int ks = 0; ks < 4; ++ks) xoff[ks] = (unsigned)(L_X + wt * 4096 + r * 128 + (((2 * ks + h) ^ ((r >> 1) & 7)) << 4));
  const unsigned wqoff = (unsigned)(L_WQ + (8 * h + ((lane & 15) >> 2)) * 192 + ((lane >> 4) & 1) * 32 + (lane & 3) * 8);
  u64 ep;
  asm volatile("s_load_dwordx2 %0, %1, 0x0\n\ts_waitcnt lgkmcnt(0)" : "=&s"(ep) : "s"(P.ws + FL_OFF) : "memory");
  u64 tag16 = (ep + 1) << 16;
  unsigned char* wsb = P.ws + SET_BASE + (((unsigned)ep + 1u) & 3u) * SET_STRIDE;
  PINS(tag16); PINS(wsb);
  const __amdgpu_buffer_rsrc_t rsWs = mk_rsrc(wsb, WS_END);
#pragma unroll
  for (int i = 0; i < 3; ++i) acc[i] = f32x16{};
  auto kloop = [&](auto nvc) {
    constexpr int NV = decltype(nvc)::value;
    bf16x8 xf[4], wf[4][3];
    auto mm = [&](int ks, int ct) { acc[ct] = ct >= 3 - NV ? MFMA(xf[ks], wf[ks][ct], acc[ct]) : MFMA(wf[ks][ct], xf[ks], acc[ct]); };
    auto step = [&](int c, int ks, bool domm) {
      const int km = (ks + 2) & 3;
#pragma unroll
      for (int ct = 0; ct < 3; ++ct) {
        if (domm) mm(km, ct);
        SBAR();
        if (ct == 0) xf[ks] = *(const LAS bf16x8*)(lds + xoff[ks] + c * 16384);
        wf[ks][ct] = tr_frag(lds + wqoff + (c & 3) * 12288 + ks * 16 * 192 + ct * 56, 4 * 192);
        SBAR();
      }
    };
#pragma unroll
    for (int c = 0; c < 7; ++c) {
      bar_lds();
      if (c == 0) STAMP(1);
      QSTAMP(1 + c);
      step(c, 0, c > 0);
      step(c, 1, c > 0);
      step(c, 2, true);
      step(c, 3, true);
    }
    bar_lds();
    QSTAMP(8);
#pragma unroll
    for (int ct = 0; ct < 3; ++ct) { mm(2, ct); mm(3, ct); SBAR(); }
  };
  auto epi = [&](auto nqc, auto nvc) {
    constexpr int NQ = decltype(nqc)::value, NV = decltype(nvc)::value;
    STAMP(2);
    u32x4 t0[3], t1[3];
#pragma unroll
    for (int ct = 0; ct < 3; ++ct) {
      if (ct < NQ) acc[ct] *= QSCALE;
      t0[ct] = as_u4(pack8(acc[ct], 0));
      if (ct >= 3 - NV) {
#pragma unroll
        for (int i = 0; i < 4; ++i) t0[ct][i] = r < DH ? t0[ct][i] : padv;
      }
      st16(rsWs, t0[ct], sto[ct], 0);
      t1[ct] = as_u4(pack8(acc[ct], 1));
      if (ct >= 3 - NV) {
#pragma unroll
        for (int i = 0; i < 4; ++i) t1[ct][i] = r < DH ? t1[ct][i] : padv;
      } else {
        t1[ct][2] = h ? (ct < NQ ? 0u : 0x3f803f80u) : t1[ct][2];
        t1[ct][3] = h ? (ct < NQ ? 0u : 0x00003f80u) : t1[ct][3];
      }
      st16(rsWs, t1[ct], sto[ct], 1024);
      SBAR();
    }
    STAMP(8);
    unsigned mq = 0, mk = 0;
#pragma unroll
    for (int ct = 0; ct < 3; ++ct) {
      const unsigned n = __float_as_uint(sumsq(t0[ct], t1[ct])) & 0x7fffffffu;
      if (ct < NQ) mq = max(mq, n); else mk = max(mk, n);
    }
    auto bound = [&](unsigned m) {
      m = row16_umax(m);
      const float lo2 = __uint_as_float(max((unsigned)__builtin_amdgcn_readlane(m, 0), (unsigned)__builtin_amdgcn_readlane(m, 16)));
      const float hi2 = __uint_as_float(max((unsigned)__builtin_amdgcn_readlane(m, 32), (unsigned)__builtin_amdgcn_readlane(m, 48)));
      return ((__float_as_uint(lo2 + hi2) >> 16) + 1u) & 0xffffu;
    };
    const unsigned pq = NQ > 0 ? bound(mq) : 0u, pk = NQ < 3 ? bound(mk) : 0u;
    u64 gran = tag16 | ((lane & 1) ? pq : pk);
    PINV(gran);
    drain();
    STAMP(9);
    if (lane < 2) __hip_atomic_store((gu64*)(wsb + pub_off), gran, __ATOMIC_RELAXED, __HIP_MEMORY_SCOPE_AGENT);
  };
  if (j < 10) {
    kloop(IC<0>{});
    if (j < 5) epi(IC<3>{}, IC<0>{});
    else if (j == 5) epi(IC<1>{}, IC<0>{});
    else epi(IC<0>{}, IC<0>{});
  } else if (j == 10) {
    kloop(IC<1>{});
    epi(IC<0>{}, IC<1>{});
  } else {
    kloop(IC<3>{});
    epi(IC<0>{}, IC<3>{});
  }
  STAMP(3);
  __builtin_amdgcn_s_sleep(PDELAY);
  const unsigned pay = poll_granule<POLL_A>(wsb, poll_off, tag16);
  STAMP(4);
  u32x4 q[4][2];
  LAS unsigned char* const cmb = lds + L_CMB + wt * 12288 + lo;
  u64 gv = 0;
  unsigned thr = 0;
#pragma nounroll
  for (int pass = 0;; ++pass) {
    u32x4 kv[KVD][4];
    kv[0][0] = ld16c(rsWs, lo, kvbase);
    SBAR();
    if (pass == 0) { q[0][0] = ld16c(rsWs, lo, qofs[0]); q[0][1] = ld16c(rsWs, lo + 1024, qofs[0]); }
    SBAR();
    kv[0][1] = ld16c(rsWs, lo + 1024, kvbase);
    SBAR();
    if (pass == 0) {
#pragma unroll
      for (int t = 1; t < 4; ++t) { q[t][0] = ld16c(rsWs, lo, qofs[t]); q[t][1] = ld16c(rsWs, lo + 1024, qofs[t]); SBAR(); }
    }
#pragma unroll
    for (int e = 2; e < 4 * (KVD - 1); ++e) { kv[e >> 2][e & 3] = ld16c(rsWs, lo + (e & 3) * 1024, kvbase + (e >> 2) * 4096); SBAR(); }
#pragma unroll
    for (int i = 0; i < 4; ++i) acc[i] = f32x16{};
    {
      const float q2 = __uint_as_float(__builtin_amdgcn_readlane(row16_umax(pay), 32) << 16);
      const float lim = SAFE_BOUND * SAFE_BOUND * __builtin_amdgcn_rcpf(q2);
      thr = lim < 1e30f ? __float_as_uint(lim) >> 16 : (lim >= 1e30f ? 0x7000u : 0u);
      PINV(thr);
    }
    __builtin_amdgcn_s_setprio(1);
    f32x16 sc = MFMA(as_frag(kv[0][0]), as_frag(q[0][0]), f32x16{});
    sc = MFMA(as_frag(kv[0][1]), as_frag(q[0][1]), sc);
    STAMP(5);
    u32x4 p0, p1 = {0, 0, 0, 0};
    float e0 = 0.f, e1 = 0.f, e2 = 0.f, e3 = 0.f;
    u64 ck = 0, cv = 0;
#define EXP4(i) do { e0 = __builtin_amdgcn_exp2f(sc[i]); e1 = __builtin_amdgcn_exp2f(sc[(i) + 1]); e2 = __builtin_amdgcn_exp2f(sc[(i) + 2]); e3 = __builtin_amdgcn_exp2f(sc[(i) + 3]); \
    asm volatile("" : "+v"(e0), "+v"(e1), "+v"(e2), "+v"(e3)); SBAR(); } while (0)
#define CVT2(P, i) do { P[i] = cvtpk(e0, e1); P[(i) + 1] = cvtpk(e2, e3); asm volatile("" : "+v"(P)); SBAR(); } while (0)
#define PART(t, g) do { *(LAS f32x4*)(cmb + (t) * 4096 + (g) * 1024) = f32x4{acc[t][4 * (g)], acc[t][4 * (g) + 1], acc[t][4 * (g) + 2], acc[t][4 * (g) + 3]}; SBAR(); } while (0)
    static_for<0, 64>([&](auto ic) {
      constexpr int i = decltype(ic)::value;
      constexpr int kt = i >> 2, t = i & 3, kn_ = (i + 1) >> 2, tn = (i + 1) & 3, kp = (i - 1) >> 2, tp = (i - 1) & 3;
      constexpr int kl = kt + KVD - 1;
      if (i == 24) STAMP(11);
      if (i == 56) (void)wait_lds_word(lds + L_CTRL, 4);
      f32x16 sn;
      SBAR();
      if (i < 63) sn = MFMA(as_frag(kv[kn_ % KVD][0]), as_frag(q[tn][0]), f32x16{});
      SBAR();
      if constexpr (i >= 62) PART(i - 62, 0);
      if (i > 0) CVT2(p1, 2);
      EXP4(0);
      if (i > 0) acc[tp] = MFMA(as_frag(kv[(kp + KVD) % KVD][3]), as_frag(p1), acc[tp]);
      SBAR();
      if (kl < 16) kv[kl % KVD][t] = ld16c(rsWs, lo + t * 1024, kvbase + kl * 4096);
      if (i == 52) gv = __hip_atomic_load((gu64*)(wsb + g_off), __ATOMIC_RELAXED, __HIP_MEMORY_SCOPE_AGENT);
      if (i == 53) ck = __hip_atomic_load((gu64*)(wsb + chk_off), __ATOMIC_RELAXED, __HIP_MEMORY_SCOPE_AGENT);
      if (i == 54) cv = __hip_atomic_load((gu64*)(wsb + (gvv - gk) * 16 * GQW + chk_off), __ATOMIC_RELAXED, __HIP_MEMORY_SCOPE_AGENT);
      SBAR();
      if constexpr (i >= 62) PART(i - 62, 1);
      CVT2(p0, 0);
      EXP4(4);
      if (i < 63) sn = MFMA(as_frag(kv[kn_ % KVD][1]), as_frag(q[tn][1]), sn);
      SBAR();
      if constexpr (i >= 62) PART(i - 62, 2);
      CVT2(p0, 2);
      EXP4(8);
      acc[t] = MFMA(as_frag(kv[kt % KVD][2]), as_frag(p0), acc[t]);
      SBAR();
      if constexpr (i >= 62) PART(i - 62, 3);
      CVT2(p1, 0);
      EXP4(12);
      if (i < 63) sc = sn;
    });
    CVT2(p1, 2);
    acc[3] = MFMA(as_frag(kv[15 % KVD][3]), as_frag(p1), acc[3]);
    SBAR();
    PART(2, 0); PART(2, 1); PART(2, 2); PART(2, 3);
#undef EXP4
#undef CVT2
#undef PART
    __builtin_amdgcn_s_setprio(2);
    if (__builtin_expect(__all(ck - tag16 <= (u64)thr && cv - tag16 <= 0x7000ull && gv - tag16 <= 0xffffull), 1)) break;
    gv = poll_granule<2>(wsb, g_off, tag16);
    if (pass) break;
    const unsigned pk = poll_granule<2>(wsb, chk_off, tag16), pv = poll_granule<2>(wsb, (gvv - gk) * 16 * GQW + chk_off, tag16);
    if (__all(pk <= thr && pv <= 0x7000u)) break;
    float mx[4];
#pragma unroll
    for (int t = 0; t < 4; ++t) mx[t] = -INFINITY;
    for (int kt = 0; kt < 64; ++kt) {
      const u32x4 k0 = ld16c(rsWs, lo, KV_OFF + (head * 64 + kt) * 4096), k1 = ld16c(rsWs, lo + 1024, KV_OFF + (head * 64 + kt) * 4096);
#pragma unroll
      for (int t = 0; t < 4; ++t) {
        f32x16 s = MFMA(as_frag(k0), as_frag(q[t][0]), f32x16{});
        s = MFMA(as_frag(k1), as_frag(q[t][1]), s);
#pragma unroll
        for (int i = 0; i < 16; ++i) mx[t] = fmaxf(mx[t], s[i]);
      }
    }
#pragma unroll
    for (int t = 0; t < 4; ++t) {
      const float nm = -fmaxf(mx[t], swap_half(mx[t]));
      const unsigned hi = __float_as_uint(nm) & 0xffff0000u;
      const float r1 = nm - __uint_as_float(hi);
      const unsigned mid = __float_as_uint(r1) & 0xffff0000u;
      const float r2 = r1 - __uint_as_float(mid);
      if (h) { q[t][1][2] = (hi >> 16) | mid; q[t][1][3] = __float_as_uint(r2) >> 16; }
    }
  }
  STAMP(6);
  asm volatile("s_waitcnt lgkmcnt(0)" ::: "memory");
  lds_arrive(lds + L_CTRL + 20);
  (void)wait_lds_word(lds + L_CTRL + 20, 4);
  f32x16 o = acc[3];
#pragma unroll
  for (int u = 0; u < 3; ++u) add_tile(o, lds + L_CMB + ((wt + 3 - u) & 3) * 12288 + u * 4096 + lo);
  const auto lsw = __builtin_amdgcn_permlane32_swap(__float_as_uint(o[12]), __float_as_uint(o[12]), false, false);
  const float inv = __uint_as_float((unsigned)gv << 16) * __builtin_amdgcn_rcpf(__uint_as_float(lsw[1]));
  o *= inv;
  st16(rsWs, as_u4(pack8(o, 0)), o_off, 0);
  u32x4 o1 = as_u4(pack8(o, 1));
  o1[2] = h ? 0u : o1[2];
  o1[3] = h ? 0u : o1[3];
  st16(rsWs, o1, o_off, 1024);
  STAMP(7);
  drain();
  if (lane == 0) __hip_atomic_store((gu64*)(wsb + go_off), tag16, __ATOMIC_RELAXED, __HIP_MEMORY_SCOPE_AGENT);
  STAMP(10);
  STAMP_FLUSH(P, 0);
}

constexpr int W2S = 6;
constexpr int HPRE = 5;
#define LEA 5
#define LEB 11
#define LE0 16
#define LU0 3
#define F1LA 2
#define PACE 4
#define PACE2 2
constexpr int ffn2_ops(int f) { return (f + W2S < 14 ? 4 : 0) + 4; }
constexpr int ffn2_younger(int f, int upto) {
  int n = 4;
  for (int i = f - W2S + 1; i < upto; ++i) n += ffn2_ops(i);
  return n;
}

DI void role_f(const Params& P, LAS unsigned char* lds, const int wt) {
  u64 ep;
  const int lane = threadIdx.x & 63, r = lane & 31, h = lane >> 5;
  const int b = blockIdx.x, xcd = b & 7, idx = b >> 3;
  const int rb = (xcd & 1) * 8 + (idx & 7), j = (xcd >> 1) * 4 + (idx >> 3);
  const unsigned lo = lane * 16, lds0 = (unsigned)(uintptr_t)lds;
  const bool ffn = j < 14;
  STAMP_DECL;
  STAMP(0);
  f32x16 acc[4];
  u32x4 wl[28];
  const __amdgpu_buffer_rsrc_t rsL = mk_rsrc(ffn ? P.w1 : P.gw, ffn ? DM * FF * 2 : DM * 2);
  const unsigned lsrc = ffn ? (unsigned)((lane >> 2) * (FF * 2) + (j * 4 + wt) * 64 + (lane & 3) * 16) : (unsigned)(h * 16);
  const unsigned lstep = ffn ? 16 * FF * 2 : 32;
  const int rot = ROT1(rb, j);
  auto kc = [&](int c) { const int v = c + rot; return v >= 7 ? v - 7 : v; };
  auto L = [&](int u0, int u1) {
#pragma unroll
    for (int u = u0; u < u1; ++u) wl[u] = ld16(rsL, lsrc, (kc(u >> 2) * 4 + (u & 3)) * lstep);
    SBAR();
  };
  {
    const __amdgpu_buffer_rsrc_t rsX = mk_rsrc(P.x, SEQ * DM * 2);
    const __amdgpu_buffer_rsrc_t rsW = mk_rsrc(P.wqkv, DM * 3 * DM * 2);
    unsigned xsrc[2], wsrc[3];
#pragma unroll
    for (int i = 0; i < 2; ++i) {
      const int row = 32 * wt + 8 * i + (lane >> 3), ch = (lane & 7) ^ ((row >> 1) & 7);
      xsrc[i] = (unsigned)((rb * 128 + row) * (DM * 2) + ch * 16);
    }
#pragma unroll
    for (int i = 0; i < 3; ++i) {
      const int p = 64 * (3 * wt + i) + lane, kr = p / 12, ch = p % 12;
      wsrc[i] = (unsigned)(kr * (3 * DM * 2) + j * 168 + (ch < 11 ? ch : 10) * 16);
    }
    auto X = [&](int c) {
#pragma unroll
      for (int i = 0; i < 4; ++i) glds16(rsX, lds0 + L_X + c * 16384 + (4 * wt + i) * 1024, xsrc[i & 1], kc(c) * 128 + (i >> 1) * 16 * DM * 2);
    };
    auto W = [&](int c) {
#pragma unroll
      for (int i = 0; i < 3; ++i) glds16(rsW, lds0 + L_WQ + (c & 3) * 12288 + (3 * wt + i) * 1024, wsrc[i], kc(c) * 64 * 3 * DM * 2);
    };
    X(0); W(0); X(1); W(1); X(2);
    QSTAMP(1);
    wait_bar<11>();
    STAMP(1);
    W(2); X(3); W(3);
    QSTAMP(2);
    wait_bar<14>();
    X(4); W(4);
    QSTAMP(3);
    wait_bar<14>();
    X(5); W(5);
    QSTAMP(4);
    wait_bar<14>();
    X(6); W(6);
    QSTAMP(5);
    wait_bar<14>();
    if (wt == 0 && lane < 8) *(LAS unsigned*)(lds + L_CTRL + lane * 4) = 0u;
    L(0, LEA);
    QSTAMP(6);
    ep = wait_bar_epoch<7 + LEA>(P.ws + FL_OFF);
    L(LEA, LEB);
    QSTAMP(7);
    wait_bar<LEB>();
    L(LEB, LE0);
    QSTAMP(8);
    bar_lds();
    __builtin_amdgcn_s_sleep(FDELAY);
  }
  STAMP(2);
  const u64 tag = (ep + 1) << 16;
  unsigned char* const wsb = P.ws + SET_BASE + (((unsigned)ep + 1u) & 3u) * SET_STRIDE;
  const __amdgpu_buffer_rsrc_t rsWs = mk_rsrc(wsb, WS_END);
  unsigned xoff[4];
#pragma unroll
  for (int ks = 0; ks < 4; ++ks) xoff[ks] = (unsigned)(L_X + r * 128 + (((2 * ks + h) ^ ((r >> 1) & 7)) << 4));
  if (ffn) {
    LAS unsigned char* ring = lds + L_W1 + wt * 8192;
    const unsigned troff = (unsigned)((8 * h + ((lane & 15) >> 2)) * 64 + ((lane >> 4) & 1) * 32 + (lane & 3) * 8);
#pragma unroll
    for (int i = 0; i < 4; ++i) acc[i] = f32x16{};
    RSTAMP(1);
    constexpr int LA = F1LA;
    bf16x8 xf[LA + 1][4], wf[LA + 1];
    auto wr = [&](int u) { *(LAS u32x4*)(ring + (u & 7) * 1024 + lo) = wl[u]; };
    auto rdw = [&](int u) { wf[u % (LA + 1)] = tr_frag(ring + (u & 7) * 1024 + troff, 4 * 64); };
    auto rdx = [&](int u, int t) { xf[u % (LA + 1)][t] = *(const LAS bf16x8*)(lds + xoff[u & 3] + (u >> 2) * 16384 + t * 4096); };
#pragma unroll
    for (int u = 0; u < LA + 2; ++u) wr(u);
#pragma unroll
    for (int u = 0; u < LA; ++u) {
      rdw(u);
#pragma unroll
      for (int t = 0; t < 4; ++t) rdx(u, t);
    }
    SBAR();
#pragma unroll
    for (int u = 0; u < 28; ++u) {
      if ((u & 3) == 0) RSTAMP(2 + (u >> 2));
#pragma unroll
      for (int t = 0; t < 4; ++t) {
        if (u + LA < 28) {
          if (t == 1) rdw(u + LA);
          rdx(u + LA, t);
          if (t == 3 && u + LA + 2 < 28) wr(u + LA + 2);
        }
        SBAR();
        acc[t] = MFMA(wf[u % (LA + 1)], xf[u % (LA + 1)][t], acc[t]);
        SBAR();
        if (u >= LU0 && LE0 + (u - LU0) * 4 + t < 28) L(LE0 + (u - LU0) * 4 + t, LE0 + (u - LU0) * 4 + t + 1);
      }
    }
    lds_arrive(lds + L_CTRL);
    RSTAMP(9);
    STAMP(3);
    __builtin_amdgcn_s_setprio(3);
#pragma unroll
    for (int t = 0; t < 4; ++t) {
      f32x16 z = acc[t];
#pragma unroll
      for (int i = 0; i < 16; ++i) z[i] = z[i] * __builtin_amdgcn_rcpf(1.f + __builtin_amdgcn_exp2f(-LOG2E * z[i]));
      const unsigned so = HF_OFF + ((rb * 4 + t) * 56 + j * 4 + wt) * 2048;
      st16(rsWs, as_u4(pack8(z, 0)), lo, so);
      st16(rsWs, as_u4(pack8(z, 1)), lo + 1024, so);
    }
    drain();
    if (lane == 0) put_granule(wsb, GH_OFF + (rb * 16 + j) * 128 + wt * 8, tag, 0);
  } else {
    __builtin_amdgcn_s_setprio(3);
    if (j == 14) {
      L(LE0, 28);
      acc[0] = f32x16{};
#pragma unroll
      for (int u = 0; u < 28; ++u) {
        const bf16x8 xf = *(const LAS bf16x8*)(lds + xoff[u & 3] + (u >> 2) * 16384 + wt * 4096);
        acc[0] = MFMA(as_frag(wl[u]), xf, acc[0]);
      }
      const float g = __builtin_amdgcn_rcpf(1.f + __builtin_amdgcn_exp2f(-LOG2E * acc[0][0]));
      if (h == 0) put_granule(wsb, GG_OFF + ((rb * 4 + wt) * 32 + r) * 8, tag, cvtpk(g, 0.f));
    }
    lds_arrive(lds + L_CTRL);
    const __amdgpu_buffer_rsrc_t rsO = mk_rsrc(P.wo, DM * DM * 2);
    const int id0 = ((j - 14) * 16 + rb) * 4 + wt;
    for (int id = id0; id < 224; id += 128) {
      const int nt = id >> 4, hd = id & 15;
      unsigned rp[16];
#pragma unroll
      for (int e = 0; e < 16; ++e)
        rp[e] = __builtin_amdgcn_raw_buffer_load_b16(rsO, (unsigned)(((4 * h + 8 * (e >> 2) + (e & 3)) * DM + r) * 2), (unsigned)((DH * hd * DM + 32 * nt) * 2), 0);
      u32x4 a, c2;
#pragma unroll
      for (int i = 0; i < 4; ++i) {
        a[i] = (rp[2 * i] & 0xffffu) | (rp[2 * i + 1] << 16);
        c2[i] = (rp[8 + 2 * i] & 0xffffu) | (rp[8 + 2 * i + 1] << 16);
      }
      c2[2] = h ? 0u : c2[2];
      c2[3] = h ? 0u : c2[3];
      st16(rsWs, a, lo, WOF_OFF + id * 2048);
      st16(rsWs, c2, lo + 1024, WOF_OFF + id * 2048);
      drain();
      if (lane == 0) put_granule(wsb, GW_OFF + id * 8, tag, 0);
    }
  }
  STAMP(4);
  if (idx >= 28) {
    if (b == 255 && wt == 0) {
      __builtin_amdgcn_s_sleep(127);
#pragma unroll
      for (int i = 0; i < 4; ++i) (void)poll_granule<64>(wsb, GQ_OFF + (i * 64 + lane) * GQW, tag);
      if (lane == 0) __hip_atomic_store((gu64*)(P.ws + FL_OFF), ep + 1, __ATOMIC_RELAXED, __HIP_MEMORY_SCOPE_AGENT);
    }
    STAMP(10);
    STAMP_FLUSH(P, 0);
    return;
  }
  const int mb = 4 * xcd + (idx & 3), nb = idx >> 2;
  const __amdgpu_buffer_rsrc_t rsW2 = mk_rsrc(P.w2, FF * DM * 2), rsH = rsWs;
  const unsigned w2src = (unsigned)((wt * 448 + (lane >> 3)) * (DM * 2) + nb * 128 + ((((lane >> 2) & 1) ^ ((lane >> 4) & 1)) * 64) + (lane & 3) * 16);
  const unsigned ring0 = lds0 + L_W2 + wt * 24576;
  const int rot2 = ROT2;
  auto fr = [&](int f) { const int v = f + rot2; return v >= 14 ? v - 14 : v; };
  auto D = [&](int f) {
#pragma unroll
    for (int i = 0; i < 4; ++i) glds16(rsW2, ring0 + (f % W2S) * 4096 + i * 1024, w2src, (fr(f) * 32 + i * 8) * DM * 2);
  };
  (void)wait_lds_word(lds + L_CTRL, 4);
#pragma unroll
  for (int f = 0; f < W2S; ++f) { D(f); __builtin_amdgcn_s_sleep(PACE); }
  const int ftl = wt * 14 + ((lane & 15) < 14 ? (lane & 15) : 0);
  (void)poll_granule<8>(wsb, (lane & 16) ? GW_OFF + ((nb * 2 + ((lane >> 2) & 1)) * 16 + wt * 4 + (lane & 3)) * 8
                                        : GH_OFF + ((mb >> 1) * 16 + (ftl >> 2)) * 128 + (ftl & 3) * 8, tag);
  STAMP(5);
  u32x4 hb[HPRE][4], wo[16];
  const unsigned hbase = HF_OFF + (mb * 2 * 56 + wt * 14) * 2048;
  auto H = [&](int f) {
#pragma unroll
    for (int e = 0; e < 4; ++e) hb[f % HPRE][e] = ld16c(rsH, lo + (e & 1) * 1024, hbase + ((e >> 1) * 56 + fr(f)) * 2048);
  };
#pragma unroll
  for (int f = 0; f < HPRE; ++f) { H(f); SBAR(); }
  const float gl = __uint_as_float(poll_granule<8>(wsb, GG_OFF + (mb * 64 + lane) * 8, tag) << 16), gs = swap_half(gl);
  const float og0 = 1.f - (h ? gs : gl), og1 = 1.f - (h ? gl : gs);
  const int q4 = (lane & 15) >> 2;
  unsigned woff[2];
#pragma unroll
  for (int nt = 0; nt < 2; ++nt) woff[nt] = (unsigned)(L_W2 + wt * 24576 + (4 * h + q4) * 128 + ((nt ^ (q4 >> 1)) * 64) + ((lane >> 4) & 1) * 32 + (lane & 3) * 8);
#pragma unroll
  for (int i = 0; i < 4; ++i) acc[i] = f32x16{};
  STAMP(6);
  __builtin_amdgcn_s_setprio(0);
  bf16x8 wf[2][2][2];
  auto RW = [&](int f) {
#pragma unroll
    for (int s = 0; s < 2; ++s)
#pragma unroll
      for (int nt = 0; nt < 2; ++nt) wf[f & 1][s][nt] = tr_frag(lds + woff[nt] + (f % W2S) * 4096 + s * 2048, 1024);
  };
  RW(0);
  static_for<0, 14>([&](auto fc) {
    constexpr int f = decltype(fc)::value;
    asm volatile("s_waitcnt lgkmcnt(0)" ::: "memory");
    SBAR();
    if constexpr (f + 1 < 14) {
      if constexpr (f + 1 >= W2S) wait_vm<ffn2_younger(f + 1, f)>();
      RW(f + 1);
    }
    SBAR();
#pragma unroll
    for (int s = 0; s < 2; ++s)
#pragma unroll
      for (int nt = 0; nt < 2; ++nt)
#pragma unroll
        for (int mt = 0; mt < 2; ++mt) {
          acc[nt * 2 + mt] = MFMA(wf[f & 1][s][nt], as_frag(hb[f % HPRE][mt * 2 + s]), acc[nt * 2 + mt]);
        }
    SBAR();
    if constexpr (f + W2S < 14) D(f + W2S);
    if constexpr (f + HPRE < 14) H(f + HPRE);
    else if constexpr (f + HPRE < 18) {
      constexpr int e0 = (f + HPRE - 14) * 4;
#pragma unroll
      for (int e = e0; e < e0 + 4; ++e) wo[e] = ld16c(rsWs, lo, WOF_OFF + ((nb * 2 + (e >> 3)) * 16 + wt * 4) * 2048 + (e & 7) * 1024);
    }
    SBAR();
    __builtin_amdgcn_s_sleep(PACE2);
    SBAR();
  });
  STAMP(7);
  __builtin_amdgcn_s_setprio(3);
#pragma unroll
  for (int i = 0; i < 16; ++i) { acc[0][i] *= og0; acc[1][i] *= og1; acc[2][i] *= og0; acc[3][i] *= og1; }
  (void)wait_lds_word(lds + L_CTRL + 20, 1);
  __builtin_amdgcn_s_sleep(ODELAY);
  (void)poll_granule<POLL_A>(wsb, GO_OFF + ((mb * 2 + ((lane >> 2) & 1)) * 16 + wt * 4 + (lane & 3)) * 128, tag);
  STAMP(8);
  u32x4 of[16];
#pragma unroll
  for (int e = 0; e < 8; ++e)
#pragma unroll
    for (int mt = 0; mt < 2; ++mt) of[mt * 8 + e] = ld16c(rsWs, lo, OF_OFF + ((mb * 2 + mt) * 16 + wt * 4) * 2048 + e * 1024);
  SBAR();
#pragma unroll
  for (int e = 0; e < 8; ++e)
#pragma unroll
    for (int nt = 0; nt < 2; ++nt)
#pragma unroll
      for (int mt = 0; mt < 2; ++mt) acc[nt * 2 + mt] = MFMA(as_frag(wo[nt * 8 + e]), as_frag(of[mt * 8 + e]), acc[nt * 2 + mt]);
#pragma unroll
  for (int t = 0; t < 4; ++t) st_tile(lds + L_W2 + wt * 24576 + t * 4096 + lo, acc[t]);
  STAMP(9);
  bar_lds();
  f32x16 a = f32x16{};
#pragma unroll
  for (int f = 0; f < 4; ++f) add_tile(a, lds + L_W2 + f * 24576 + wt * 4096 + lo);
  unsigned short* dst = P.out + (size_t)((mb * 2 + (wt & 1)) * 32 + r) * DM + (nb * 2 + (wt >> 1)) * 32 + 8 * h;
#pragma unroll
  for (int k = 0; k < 2; ++k) {
    const auto x = __builtin_amdgcn_permlane32_swap(cvtpk(a[8 * k], a[8 * k + 1]), cvtpk(a[8 * k + 4], a[8 * k + 5]), false, false);
    const auto y = __builtin_amdgcn_permlane32_swap(cvtpk(a[8 * k + 2], a[8 * k + 3]), cvtpk(a[8 * k + 6], a[8 * k + 7]), false, false);
    *(u32x4*)(dst + 16 * k) = u32x4{x[0], y[0], x[1], y[1]};
  }
  STAMP(10);
  STAMP_FLUSH(P, 0);
}

extern __shared__ __attribute__((aligned(16))) unsigned char g_lds[];

__global__ __launch_bounds__(512, 2) void k_fused(Params P) {
  LAS unsigned char* lds = (LAS unsigned char*)g_lds;
  const int w = __builtin_amdgcn_readfirstlane(threadIdx.x >> 6);
  if (w < 4) role_a(P, lds, w);
  else role_f(P, lds, w - 4);
}

extern "C" void kernel_launch(void* const* d_in, const int* in_sizes, int n_in, void* d_out, int out_size, void* d_ws,
                              size_t ws_size, hipStream_t stream) {
  Params P;
  P.x = (const unsigned short*)d_in[0];
  P.wqkv = (const unsigned short*)d_in[1];
  P.wo = (const unsigned short*)d_in[2];
  P.w1 = (const unsigned short*)d_in[4];
  P.w2 = (const unsigned short*)d_in[5];
  P.gw = (const unsigned short*)d_in[6];
  P.out = (unsigned short*)d_out;
  P.ws = (unsigned char*)d_ws;
  (void)hipFuncSetAttribute((const void*)k_fused, hipFuncAttributeMaxDynamicSharedMemorySize, LDS_BYTES);
  k_fused<<<256, 512, LDS_BYTES, stream>>>(P);
}
```

```cpp
#define XSTAMP 0
#define XWAVE 4
#include <hip/hip_runtime.h>
#include <hip/hip_bf16.h>
#include <stdint.h>

typedef short bf16x8 __attribute__((ext_vector_type(8)));
typedef short s16x4 __attribute__((ext_vector_type(4)));
typedef float f32x16 __attribute__((ext_vector_type(16)));
typedef float f32x4 __attribute__((ext_vector_type(4)));
typedef unsigned u32x4 __attribute__((ext_vector_type(4)));
typedef unsigned u32x2 __attribute__((ext_vector_type(2)));
#define LAS __attribute__((address_space(3)))
#define DI __device__ __forceinline__
#define MFMA(a, b, c) __builtin_amdgcn_mfma_f32_32x32x16_bf16((a), (b), (c), 0, 0, 0)
#define SBAR() __builtin_amdgcn_sched_barrier(0)

constexpr int SEQ = 2048, DM = 448, NH = 16, DH = 28, FF = 1792;
constexpr unsigned FL_OFF = 0;
#define SETB 1u
constexpr unsigned SET_BASE = SETB << 20, SET_STRIDE = 24u << 20;
#define GQS 16u
constexpr unsigned GQW = GQS == 16u ? 128u : 4u * GQS;
constexpr unsigned GQ_OFF = 0;
constexpr unsigned GH_OFF = GQ_OFF + 256 * GQW;
constexpr unsigned GO_OFF = GH_OFF + 256 * 128;
constexpr unsigned GW_OFF = GO_OFF + 1024 * 128;
constexpr unsigned GG_OFF = GW_OFF + 256 * 8;
constexpr unsigned QF_OFF = 524288;
constexpr unsigned KV_OFF = QF_OFF + 16 * 64 * 2048;
constexpr unsigned HF_OFF = KV_OFF + 16 * 64 * 4096;
constexpr unsigned WOF_OFF = HF_OFF + 64 * 56 * 2048;
constexpr unsigned OF_OFF = WOF_OFF + 14 * 16 * 2048;
constexpr unsigned WS_END = OF_OFF + 64 * 16 * 2048;
static_assert(GG_OFF + 2048 * 8 <= QF_OFF && WS_END <= SET_STRIDE, "workspace map");

constexpr int L_X = 0;
constexpr int L_WQ = 114688;
constexpr int L_W1 = 114688;
constexpr int L_CTRL = 163776;
constexpr int L_W2 = 0;
constexpr int L_CMB = 98304;
constexpr int LDS_BYTES = 163840;

constexpr float QSCALE = 0.18898223650461362f * 1.4426950408889634f;
constexpr float LOG2E = 1.4426950408889634f;
constexpr float SAFE_BOUND = 60.0f;
constexpr unsigned SPIN_CAP = 1u << 18;
#define ROT1(rb, j) 0
#define KVD 4
#define ROTA 0
#define ROT2 0
#define PDELAY 16
#define ODELAY 28
#define FDELAY 18
#define POLL_A 4

struct Params {
  const unsigned short* x;
  const unsigned short* wqkv;
  const unsigned short* wo;
  const unsigned short* w1;
  const unsigned short* w2;
  const unsigned short* gw;
  unsigned short* out;
  unsigned char* ws;
};
typedef unsigned long long u64;
typedef __attribute__((address_space(1))) u64 gu64;

DI unsigned cvtpk(float lo, float hi) {
  typedef __bf16 bf2 __attribute__((ext_vector_type(2)));
  typedef float f2 __attribute__((ext_vector_type(2)));
  f2 v = {lo, hi};
  bf2 r = __builtin_convertvector(v, bf2);
  return __builtin_bit_cast(unsigned, r);
}
DI bf16x8 pack8(const f32x16& x, int s) {
  u32x4 p;
  p[0] = cvtpk(x[8 * s + 0], x[8 * s + 1]);
  p[1] = cvtpk(x[8 * s + 2], x[8 * s + 3]);
  p[2] = cvtpk(x[8 * s + 4], x[8 * s + 5]);
  p[3] = cvtpk(x[8 * s + 6], x[8 * s + 7]);
  return __builtin_bit_cast(bf16x8, p);
}
DI float swap_half(float v) {
  auto r = __builtin_amdgcn_permlane32_swap(__float_as_uint(v), __float_as_uint(v), false, false);
  return (threadIdx.x & 32) ? __uint_as_float(r[0]) : __uint_as_float(r[1]);
}
template <int CTRL> DI float dpp_mov(float v) {
  return __uint_as_float(__builtin_amdgcn_update_dpp(__float_as_uint(v), __float_as_uint(v), CTRL, 0xf, 0xf, false));
}
DI float row16_max(float v) {
  v = fmaxf(v, dpp_mov<0x128>(v));
  v = fmaxf(v, dpp_mov<0x124>(v));
  v = fmaxf(v, dpp_mov<0x122>(v));
  v = fmaxf(v, dpp_mov<0x121>(v));
  return v;
}
DI unsigned row16_umax(unsigned v) {
  v = max(v, (unsigned)__builtin_amdgcn_update_dpp(v, v, 0x128, 0xf, 0xf, false));
  v = max(v, (unsigned)__builtin_amdgcn_update_dpp(v, v, 0x124, 0xf, 0xf, false));
  v = max(v, (unsigned)__builtin_amdgcn_update_dpp(v, v, 0x122, 0xf, 0xf, false));
  v = max(v, (unsigned)__builtin_amdgcn_update_dpp(v, v, 0x121, 0xf, 0xf, false));
  return v;
}
DI float rdlane(float v, int l) { return __uint_as_float(__builtin_amdgcn_readlane(__float_as_uint(v), l)); }
DI s16x4 tr_read(const LAS unsigned char* p) {
  typedef short v4i16_t __attribute__((ext_vector_type(4)));
  return __builtin_bit_cast(s16x4, __builtin_amdgcn_ds_read_tr16_b64_v4i16((LAS v4i16_t*)p));
}
DI bf16x8 tr_frag(const LAS unsigned char* p, int row_stride4) {
  s16x4 lo = tr_read(p), hi = tr_read(p + row_stride4);
  return __builtin_shufflevector(lo, hi, 0, 1, 2, 3, 4, 5, 6, 7);
}
DI __amdgpu_buffer_rsrc_t mk_rsrc(const void* p, unsigned bytes) {
  return __builtin_amdgcn_make_buffer_rsrc((void*)p, (short)0, (int)bytes, 0x00020000);
}
DI void glds16(__amdgpu_buffer_rsrc_t rs, unsigned dst, unsigned voff, unsigned soff) {
  asm volatile("s_mov_b32 m0, %0\n\ts_nop 0\n\tbuffer_load_dwordx4 %1, %2, %3 offen lds" ::"s"(dst), "v"(voff), "s"(rs), "s"(soff) : "memory");
}
DI u32x4 ld16(__amdgpu_buffer_rsrc_t rs, unsigned voff, unsigned soff) { return __builtin_amdgcn_raw_buffer_load_b128(rs, voff, soff, 0); }
DI u32x4 ld16c(__amdgpu_buffer_rsrc_t rs, unsigned voff, unsigned soff) { return __builtin_amdgcn_raw_buffer_load_b128(rs, voff, soff, 16); }
DI void st16(__amdgpu_buffer_rsrc_t rs, u32x4 v, unsigned voff, unsigned soff) { __builtin_amdgcn_raw_buffer_store_b128(v, rs, voff + soff, 0, 16); }
DI bf16x8 as_frag(u32x4 v) { return __builtin_bit_cast(bf16x8, v); }
DI u32x4 as_u4(bf16x8 v) { return __builtin_bit_cast(u32x4, v); }
template <int N> DI void wait_bar() { asm volatile("s_waitcnt vmcnt(%0) lgkmcnt(0)\n\ts_barrier" ::"n"(N) : "memory"); }

template <int V> struct IC { static constexpr int value = V; };
template <int I, int N, class Fn> DI void static_for(Fn&& fn) {
  if constexpr (I < N) { fn(IC<I>{}); static_for<I + 1, N>(fn); }
}
DI void drain() { asm volatile("s_waitcnt vmcnt(0)" ::: "memory"); }
template <int N> DI void wait_vm() { asm volatile("s_waitcnt vmcnt(%0)" ::"n"(N) : "memory"); }
DI void put_granule(unsigned char* ws, unsigned off, u64 tag16, unsigned payload) {
  __hip_atomic_store((gu64*)(ws + off), tag16 | (u64)(payload & 0xffffu), __ATOMIC_RELAXED, __HIP_MEMORY_SCOPE_AGENT);
}
template <int SLEEP> DI unsigned poll_granule(unsigned char* ws, unsigned off, u64 tag16) {
  u64 v = ~tag16;
  for (unsigned spins = 0;; ++spins) {
    if (v - tag16 > 0xffffull) v = __hip_atomic_load((gu64*)(ws + off), __ATOMIC_RELAXED, __HIP_MEMORY_SCOPE_AGENT);
    if (__all(v - tag16 <= 0xffffull) || spins > SPIN_CAP) break;
    __builtin_amdgcn_s_sleep(SLEEP);
  }
  __builtin_amdgcn_fence(__ATOMIC_ACQUIRE, "wavefront");
  return (unsigned)v & 0xffffu;
}
DI unsigned wait_lds_word(LAS unsigned char* p, unsigned want) {
  unsigned v = 0;
#pragma nounroll
  for (unsigned spins = 0; spins < (1u << 24); ++spins) {
    v = __builtin_amdgcn_readfirstlane(__hip_atomic_load((LAS unsigned*)p, __ATOMIC_RELAXED, __HIP_MEMORY_SCOPE_WORKGROUP));
    if (v >= want) break;
    __builtin_amdgcn_s_sleep(1);
  }
  asm volatile("" ::: "memory");
  return v;
}
template <int N> DI u64 wait_bar_epoch(const unsigned char* p) {
  u64 ep;
  asm volatile("s_load_dwordx2 %0, %1, 0x0\n\ts_waitcnt vmcnt(%2) lgkmcnt(0)\n\ts_barrier" : "=&s"(ep) : "s"(p), "n"(N) : "memory");
  return ep;
}
DI void bar_lds() { asm volatile("s_waitcnt lgkmcnt(0)\n\ts_barrier" ::: "memory"); }
DI void st_tile(LAS unsigned char* p, const f32x16& a) {
#pragma unroll
  for (int g = 0; g < 4; ++g) *(LAS f32x4*)(p + g * 1024) = f32x4{a[4 * g], a[4 * g + 1], a[4 * g + 2], a[4 * g + 3]};
}
DI void add_tile(f32x16& a, const LAS unsigned char* p) {
#pragma unroll
  for (int g = 0; g < 4; ++g) {
    const f32x4 v = *(const LAS f32x4*)(p + g * 1024);
    a[4 * g] += v[0]; a[4 * g + 1] += v[1]; a[4 * g + 2] += v[2]; a[4 * g + 3] += v[3];
  }
}

#define STAMP_DECL unsigned long long rt0_ = __builtin_amdgcn_s_memrealtime(), rtx_ = rt0_
#define STAMP(id) do { if ((id) == XSTAMP) { SBAR(); asm volatile("s_memrealtime %0\n\ts_waitcnt lgkmcnt(0)" : "=s"(rtx_) :: "memory"); SBAR(); } } while (0)
#define QSTAMP(id) do {} while (0)
#define RSTAMP(id) do {} while (0)
#define STAMP_FLUSH(P, ph) do { if (blockIdx.x == 9 && __builtin_amdgcn_readfirstlane(threadIdx.x >> 6) == XWAVE) { const unsigned long long until_ = rt0_ + 5000 + 4 * (rtx_ - rt0_); \
  while (__builtin_amdgcn_s_memrealtime() < until_) __builtin_amdgcn_s_sleep(2); } } while (0)

DI void lds_arrive(LAS unsigned char* p) {
  asm volatile("s_mov_b64 exec, 1\n\tds_add_u32 %0, %1\n\ts_mov_b64 exec, -1" ::"v"((unsigned)(uintptr_t)p), "v"(1u) : "memory");
}

#define PINV(x) asm volatile("" : "+v"(x))
#define PINS(x) asm volatile("" : "+s"(x))
DI float sumsq(const u32x4& a, const u32x4& b) {
  typedef __bf16 bf2 __attribute__((ext_vector_type(2)));
  float n0 = 0.f, n1 = 0.f;
#pragma unroll
  for (int i = 0; i < 4; ++i) {
    n0 = __builtin_amdgcn_fdot2_f32_bf16(__builtin_bit_cast(bf2, a[i]), __builtin_bit_cast(bf2, a[i]), n0, false);
    n1 = __builtin_amdgcn_fdot2_f32_bf16(__builtin_bit_cast(bf2, b[i]), __builtin_bit_cast(bf2, b[i]), n1, false);
  }
  return n0 + n1;
}

DI void role_a(const Params& P, LAS unsigned char* lds, const int wt) {
  const int lane = threadIdx.x & 63, r = lane & 31, h = lane >> 5;
  const int b = blockIdx.x, xcd = b & 7, idx = b >> 3;
  const unsigned lo = lane * 16;
  STAMP_DECL;
  STAMP(0);
  __builtin_amdgcn_s_setprio(2);
  f32x16 acc[4];
  const int rb = (xcd & 1) * 8 + (idx & 7), j = (xcd >> 1) * 4 + (idx >> 3);
  const int head = xcd * 2 + (idx & 1), qb = idx >> 1;
  unsigned sto[3];
#pragma unroll
  for (int ct = 0; ct < 3; ++ct) {
    const int u = 3 * j + ct, part = u >> 4, t = (u & 15) * 64 + rb * 4 + wt;
    sto[ct] = lo + (part == 0 ? QF_OFF + t * 2048 : KV_OFF + t * 4096 + (part - 1) * 2048);
    PINV(sto[ct]);
  }
  unsigned padv = r == DH ? 0x3f803f80u : 0u;
  PINV(padv);
  const int gq = head / 3, gk = (16 + head) / 3, gvv = (32 + head) / 3;
  unsigned pub_off = GQ_OFF + (j * 16 + rb) * GQW + wt * GQS + (lane & 1) * 8;
  unsigned poll_off = GQ_OFF + (h ? gq * 16 + qb : (lane < 16 ? gk : gvv) * 16 + wt * 4 + ((lane >> 2) & 3)) * GQW + (lane & 3) * GQS + h * 8;
  unsigned chk_off = GQ_OFF + (gk * 16 + (lane >> 2)) * GQW + (lane & 3) * GQS;
  unsigned g_off = GG_OFF + ((qb * 4 + wt) * 32 + r) * 8;
  unsigned o_off = lo + OF_OFF + ((qb * 4 + wt) * 16 + head) * 2048;
  PINV(pub_off); PINV(poll_off); PINV(chk_off); PINV(g_off); PINV(o_off);
  unsigned kvbase = KV_OFF + (head * 64 + wt * 16) * 4096, go_off = GO_OFF + ((qb * 4 + wt) * 16 + head) * 128;
  unsigned qofs[4];
#pragma unroll
  for (int t = 0; t < 4; ++t) { qofs[t] = QF_OFF + (unsigned)(head * 64 + qb * 4 + ((wt + 1 + t) & 3)) * 2048; PINS(qofs[t]); }
  PINS(kvbase); PINS(go_off);
  unsigned xoff[4];
#pragma unroll
  for (int ks = 0; ks < 4; ++ks) xoff[ks] = (unsigned)(L_X + wt * 4096 + r * 128 + (((2 * ks + h) ^ ((r >> 1) & 7)) << 4));
  const unsigned wqoff = (unsigned)(L_WQ + (8 * h + ((lane & 15) >> 2)) * 192 + ((lane >> 4) & 1) * 32 + (lane & 3) * 8);
  u64 ep;
  asm volatile("s_load_dwordx2 %0, %1, 0x0\n\ts_waitcnt lgkmcnt(0)" : "=&s"(ep) : "s"(P.ws + FL_OFF) : "memory");
  u64 tag16 = (ep + 1) << 16;
  unsigned char* wsb = P.ws + SET_BASE + (((unsigned)ep + 1u) & 3u) * SET_STRIDE;
  PINS(tag16); PINS(wsb);
  const __amdgpu_buffer_rsrc_t rsWs = mk_rsrc(wsb, WS_END);
#pragma unroll
  for (int i = 0; i < 3; ++i) acc[i] = f32x16{};
  auto kloop = [&](auto nvc) {
    constexpr int NV = decltype(nvc)::value;
    bf16x8 xf[4], wf[4][3];
    auto mm = [&](int ks, int ct) { acc[ct] = ct >= 3 - NV ? MFMA(xf[ks], wf[ks][ct], acc[ct]) : MFMA(wf[ks][ct], xf[ks], acc[ct]); };
    auto step = [&](int c, int ks, bool domm) {
      const int km = (ks + 2) & 3;
#pragma unroll
      for (int ct = 0; ct < 3; ++ct) {
        if (domm) mm(km, ct);
        SBAR();
        if (ct == 0) xf[ks] = *(const LAS bf16x8*)(lds + xoff[ks] + c * 16384);
        wf[ks][ct] = tr_frag(lds + wqoff + (c & 3) * 12288 + ks * 16 * 192 + ct * 56, 4 * 192);
        SBAR();
      }
    };
#pragma unroll
    for (int c = 0; c < 7; ++c) {
      bar_lds();
      if (c == 0) STAMP(1);
      QSTAMP(1 + c);
      step(c, 0, c > 0);
      step(c, 1, c > 0);
      step(c, 2, true);
      step(c, 3, true);
    }
    bar_lds();
    QSTAMP(8);
#pragma unroll
    for (int ct = 0; ct < 3; ++ct) { mm(2, ct); mm(3, ct); SBAR(); }
  };
  auto epi = [&](auto nqc, auto nvc) {
    constexpr int NQ = decltype(nqc)::value, NV = decltype(nvc)::value;
    STAMP(2);
    u32x4 t0[3], t1[3];
#pragma unroll
    for (int ct = 0; ct < 3; ++ct) {
      if (ct < NQ) acc[ct] *= QSCALE;
      t0[ct] = as_u4(pack8(acc[ct], 0));
      if (ct >= 3 - NV) {
#pragma unroll
        for (int i = 0; i < 4; ++i) t0[ct][i] = r < DH ? t0[ct][i] : padv;
      }
      st16(rsWs, t0[ct], sto[ct], 0);
      t1[ct] = as_u4(pack8(acc[ct], 1));
      if (ct >= 3 - NV) {
#pragma unroll
        for (int i = 0; i < 4; ++i) t1[ct][i] = r < DH ? t1[ct][i] : padv;
      } else {
        t1[ct][2] = h ? (ct < NQ ? 0u : 0x3f803f80u) : t1[ct][2];
        t1[ct][3] = h ? (ct < NQ ? 0u : 0x00003f80u) : t1[ct][3];
      }
      st16(rsWs, t1[ct], sto[ct], 1024);
      SBAR();
    }
    STAMP(8);
    unsigned mq = 0, mk = 0;
#pragma unroll
    for (int ct = 0; ct < 3; ++ct) {
      const unsigned n = __float_as_uint(sumsq(t0[ct], t1[ct])) & 0x7fffffffu;
      if (ct < NQ) mq = max(mq, n); else mk = max(mk, n);
    }
    auto bound = [&](unsigned m) {
      m = row16_umax(m);
      const float lo2 = __uint_as_float(max((unsigned)__builtin_amdgcn_readlane(m, 0), (unsigned)__builtin_amdgcn_readlane(m, 16)));
      const float hi2 = __uint_as_float(max((unsigned)__builtin_amdgcn_readlane(m, 32), (unsigned)__builtin_amdgcn_readlane(m, 48)));
      return ((__float_as_uint(lo2 + hi2) >> 16) + 1u) & 0xffffu;
    };
    const unsigned pq = NQ > 0 ? bound(mq) : 0u, pk = NQ < 3 ? bound(mk) : 0u;
    u64 gran = tag16 | ((lane & 1) ? pq : pk);
    PINV(gran);
    drain();
    STAMP(9);
    if (lane < 2) __hip_atomic_store((gu64*)(wsb + pub_off), gran, __ATOMIC_RELAXED, __HIP_MEMORY_SCOPE_AGENT);
  };
  if (j < 10) {
    kloop(IC<0>{});
    if (j < 5) epi(IC<3>{}, IC<0>{});
    else if (j == 5) epi(IC<1>{}, IC<0>{});
    else epi(IC<0>{}, IC<0>{});
  } else if (j == 10) {
    kloop(IC<1>{});
    epi(IC<0>{}, IC<1>{});
  } else {
    kloop(IC<3>{});
    epi(IC<0>{}, IC<3>{});
  }
  STAMP(3);
  __builtin_amdgcn_s_sleep(PDELAY);
  const unsigned pay = poll_granule<POLL_A>(wsb, poll_off, tag16);
  STAMP(4);
  u32x4 q[4][2];
  LAS unsigned char* const cmb = lds + L_CMB + wt * 12288 + lo;
  u64 gv = 0;
  unsigned thr = 0;
#pragma nounroll
  for (int pass = 0;; ++pass) {
    u32x4 kv[KVD][4];
    kv[0][0] = ld16c(rsWs, lo, kvbase);
    SBAR();
    if (pass == 0) { q[0][0] = ld16c(rsWs, lo, qofs[0]); q[0][1] = ld16c(rsWs, lo + 1024, qofs[0]); }
    SBAR();
    kv[0][1] = ld16c(rsWs, lo + 1024, kvbase);
    SBAR();
    if (pass == 0) {
#pragma unroll
      for (int t = 1; t < 4; ++t) { q[t][0] = ld16c(rsWs, lo, qofs[t]); q[t][1] = ld16c(rsWs, lo + 1024, qofs[t]); SBAR(); }
    }
#pragma unroll
    for (int e = 2; e < 4 * (KVD - 1); ++e) { kv[e >> 2][e & 3] = ld16c(rsWs, lo + (e & 3) * 1024, kvbase + (e >> 2) * 4096); SBAR(); }
#pragma unroll
    for (int i = 0; i < 4; ++i) acc[i] = f32x16{};
    {
      const float q2 = __uint_as_float(__builtin_amdgcn_readlane(row16_umax(pay), 32) << 16);
      const float lim = SAFE_BOUND * SAFE_BOUND * __builtin_amdgcn_rcpf(q2);
      thr = lim < 1e30f ? __float_as_uint(lim) >> 16 : (lim >= 1e30f ? 0x7000u : 0u);
      PINV(thr);
    }
    __builtin_amdgcn_s_setprio(1);
    f32x16 sc = MFMA(as_frag(kv[0][0]), as_frag(q[0][0]), f32x16{});
    sc = MFMA(as_frag(kv[0][1]), as_frag(q[0][1]), sc);
    STAMP(5);
    u32x4 p0, p1 = {0, 0, 0, 0};
    float e0 = 0.f, e1 = 0.f, e2 = 0.f, e3 = 0.f;
    u64 ck = 0, cv = 0;
#define EXP4(i) do { e0 = __builtin_amdgcn_exp2f(sc[i]); e1 = __builtin_amdgcn_exp2f(sc[(i) + 1]); e2 = __builtin_amdgcn_exp2f(sc[(i) + 2]); e3 = __builtin_amdgcn_exp2f(sc[(i) + 3]); \
    asm volatile("" : "+v"(e0), "+v"(e1), "+v"(e2), "+v"(e3)); SBAR(); } while (0)
#define CVT2(P, i) do { P[i] = cvtpk(e0, e1); P[(i) + 1] = cvtpk(e2, e3); asm volatile("" : "+v"(P)); SBAR(); } while (0)
#define PART(t, g) do { *(LAS f32x4*)(cmb + (t) * 4096 + (g) * 1024) = f32x4{acc[t][4 * (g)], acc[t][4 * (g) + 1], acc[t][4 * (g) + 2], acc[t][4 * (g) + 3]}; SBAR(); } while (0)
    static_for<0, 64>([&](auto ic) {
      constexpr int i = decltype(ic)::value;
      constexpr int kt = i >> 2, t = i & 3, kn_ = (i + 1) >> 2, tn = (i + 1) & 3, kp = (i - 1) >> 2, tp = (i - 1) & 3;
      constexpr int kl = kt + KVD - 1;
      if (i == 24) STAMP(11);
      if (i == 56) (void)wait_lds_word(lds + L_CTRL, 4);
      f32x16 sn;
      SBAR();
      if (i < 63) sn = MFMA(as_frag(kv[kn_ % KVD][0]), as_frag(q[tn][0]), f32x16{});
      SBAR();
      if constexpr (i >= 62) PART(i - 62, 0);
      if (i > 0) CVT2(p1, 2);
      EXP4(0);
      if (i > 0) acc[tp] = MFMA(as_frag(kv[(kp + KVD) % KVD][3]), as_frag(p1), acc[tp]);
      SBAR();
      if (kl < 16) kv[kl % KVD][t] = ld16c(rsWs, lo + t * 1024, kvbase + kl * 4096);
      if (i == 52) gv = __hip_atomic_load((gu64*)(wsb + g_off), __ATOMIC_RELAXED, __HIP_MEMORY_SCOPE_AGENT);
      if (i == 53) ck = __hip_atomic_load((gu64*)(wsb + chk_off), __ATOMIC_RELAXED, __HIP_MEMORY_SCOPE_AGENT);
      if (i == 54) cv = __hip_atomic_load((gu64*)(wsb + (gvv - gk) * 16 * GQW + chk_off), __ATOMIC_RELAXED, __HIP_MEMORY_SCOPE_AGENT);
      SBAR();
      if constexpr (i >= 62) PART(i - 62, 1);
      CVT2(p0, 0);
      EXP4(4);
      if (i < 63) sn = MFMA(as_frag(kv[kn_ % KVD][1]), as_frag(q[tn][1]), sn);
      SBAR();
      if constexpr (i >= 62) PART(i - 62, 2);
      CVT2(p0, 2);
      EXP4(8);
      acc[t] = MFMA(as_frag(kv[kt % KVD][2]), as_frag(p0), acc[t]);
      SBAR();
      if constexpr (i >= 62) PART(i - 62, 3);
      CVT2(p1, 0);
      EXP4(12);
      if (i < 63) sc = sn;
    });
    CVT2(p1, 2);
    acc[3] = MFMA(as_frag(kv[15 % KVD][3]), as_frag(p1), acc[3]);
    SBAR();
    PART(2, 0); PART(2, 1); PART(2, 2); PART(2, 3);
#undef EXP4
#undef CVT2
#undef PART
    __builtin_amdgcn_s_setprio(2);
    if (__builtin_expect(__all(ck - tag16 <= (u64)thr && cv - tag16 <= 0x7000ull && gv - tag16 <= 0xffffull), 1)) break;
    gv = poll_granule<2>(wsb, g_off, tag16);
    if (pass) break;
    const unsigned pk = poll_granule<2>(wsb, chk_off, tag16), pv = poll_granule<2>(wsb, (gvv - gk) * 16 * GQW + chk_off, tag16);
    if (__all(pk <= thr && pv <= 0x7000u)) break;
    float mx[4];
#pragma unroll
    for (int t = 0; t < 4; ++t) mx[t] = -INFINITY;
    for (int kt = 0; kt < 64; ++kt) {
      const u32x4 k0 = ld16c(rsWs, lo, KV_OFF + (head * 64 + kt) * 4096), k1 = ld16c(rsWs, lo + 1024, KV_OFF + (head * 64 + kt) * 4096);
#pragma unroll
      for (int t = 0; t < 4; ++t) {
        f32x16 s = MFMA(as_frag(k0), as_frag(q[t][0]), f32x16{});
        s = MFMA(as_frag(k1), as_frag(q[t][1]), s);
#pragma unroll
        for (int i = 0; i < 16; ++i) mx[t] = fmaxf(mx[t], s[i]);
      }
    }
#pragma unroll
    for (int t = 0; t < 4; ++t) {
      const float nm = -fmaxf(mx[t], swap_half(mx[t]));
      const unsigned hi = __float_as_uint(nm) & 0xffff0000u;
      const float r1 = nm - __uint_as_float(hi);
      const unsigned mid = __float_as_uint(r1) & 0xffff0000u;
      const float r2 = r1 - __uint_as_float(mid);
      if (h) { q[t][1][2] = (hi >> 16) | mid; q[t][1][3] = __float_as_uint(r2) >> 16; }
    }
  }
  STAMP(6);
  asm volatile("s_waitcnt lgkmcnt(0)" ::: "memory");
  lds_arrive(lds + L_CTRL + 20);
  (void)wait_lds_word(lds + L_CTRL + 20, 4);
  f32x16 o = acc[3];
#pragma unroll
  for (int u = 0; u < 3; ++u) add_tile(o, lds + L_CMB + ((wt + 3 - u) & 3) * 12288 + u * 4096 + lo);
  const auto lsw = __builtin_amdgcn_permlane32_swap(__float_as_uint(o[12]), __float_as_uint(o[12]), false, false);
  const float inv = __uint_as_float((unsigned)gv << 16) * __builtin_amdgcn_rcpf(__uint_as_float(lsw[1]));
  o *= inv;
  st16(rsWs, as_u4(pack8(o, 0)), o_off, 0);
  u32x4 o1 = as_u4(pack8(o, 1));
  o1[2] = h ? 0u : o1[2];
  o1[3] = h ? 0u : o1[3];
  st16(rsWs, o1, o_off, 1024);
  STAMP(7);
  drain();
  if (lane == 0) __hip_atomic_store((gu64*)(wsb + go_off), tag16, __ATOMIC_RELAXED, __HIP_MEMORY_SCOPE_AGENT);
  STAMP(10);
  STAMP_FLUSH(P, 0);
}

constexpr int W2S = 6;
constexpr int HPRE = 5;
#define LEA 5
#define LEB 11
#define LE0 16
#define LU0 3
#define F1LA 2
#define PACE 4
#define PACE2 2
constexpr int ffn2_ops(int f) { return (f + W2S < 14 ? 4 : 0) + 4; }
constexpr int ffn2_younger(int f, int upto) {
  int n = 4;
  for (int i = f - W2S + 1; i < upto; ++i) n += ffn2_ops(i);
  return n;
}

DI void role_f(const Params& P, LAS unsigned char* lds, const int wt) {
  u64 ep;
  const int lane = threadIdx.x & 63, r = lane & 31, h = lane >> 5;
  const int b = blockIdx.x, xcd = b & 7, idx = b >> 3;
  const int rb = (xcd & 1) * 8 + (idx & 7), j = (xcd >> 1) * 4 + (idx >> 3);
  const unsigned lo = lane * 16, lds0 = (unsigned)(uintptr_t)lds;
  const bool ffn = j < 14;
  STAMP_DECL;
  STAMP(0);
  f32x16 acc[4];
  u32x4 wl[28];
  const __amdgpu_buffer_rsrc_t rsL = mk_rsrc(ffn ? P.w1 : P.gw, ffn ? DM * FF * 2 : DM * 2);
  const unsigned lsrc = ffn ? (unsigned)((lane >> 2) * (FF * 2) + (j * 4 + wt) * 64 + (lane & 3) * 16) : (unsigned)(h * 16);
  const unsigned lstep = ffn ? 16 * FF * 2 : 32;
  const int rot = ROT1(rb, j);
  auto kc = [&](int c) { const int v = c + rot; return v >= 7 ? v - 7 : v; };
  auto L = [&](int u0, int u1) {
#pragma unroll
    for (int u = u0; u < u1; ++u) wl[u] = ld16(rsL, lsrc, (kc(u >> 2) * 4 + (u & 3)) * lstep);
    SBAR();
  };
  {
    const __amdgpu_buffer_rsrc_t rsX = mk_rsrc(P.x, SEQ * DM * 2);
    const __amdgpu_buffer_rsrc_t rsW = mk_rsrc(P.wqkv, DM * 3 * DM * 2);
    unsigned xsrc[2], wsrc[3];
#pragma unroll
    for (int i = 0; i < 2; ++i) {
      const int row = 32 * wt + 8 * i + (lane >> 3), ch = (lane & 7) ^ ((row >> 1) & 7);
      xsrc[i] = (unsigned)((rb * 128 + row) * (DM * 2) + ch * 16);
    }
#pragma unroll
    for (int i = 0; i < 3; ++i) {
      const int p = 64 * (3 * wt + i) + lane, kr = p / 12, ch = p % 12;
      wsrc[i] = (unsigned)(kr * (3 * DM * 2) + j * 168 + (ch < 11 ? ch : 10) * 16);
    }
    auto X = [&](int c) {
#pragma unroll
      for (int i = 0; i < 4; ++i) glds16(rsX, lds0 + L_X + c * 16384 + (4 * wt + i) * 1024, xsrc[i & 1], kc(c) * 128 + (i >> 1) * 16 * DM * 2);
    };
    auto W = [&](int c) {
#pragma unroll
      for (int i = 0; i < 3; ++i) glds16(rsW, lds0 + L_WQ + (c & 3) * 12288 + (3 * wt + i) * 1024, wsrc[i], kc(c) * 64 * 3 * DM * 2);
    };
    X(0); W(0); X(1); W(1); X(2);
    QSTAMP(1);
    wait_bar<11>();
    STAMP(1);
    W(2); X(3); W(3);
    QSTAMP(2);
    wait_bar<14>();
    X(4); W(4);
    QSTAMP(3);
    wait_bar<14>();
    X(5); W(5);
    QSTAMP(4);
    wait_bar<14>();
    X(6); W(6);
    QSTAMP(5);
    wait_bar<14>();
    if (wt == 0 && lane < 8) *(LAS unsigned*)(lds + L_CTRL + lane * 4) = 0u;
    L(0, LEA);
    QSTAMP(6);
    ep = wait_bar_epoch<7 + LEA>(P.ws + FL_OFF);
    L(LEA, LEB);
    QSTAMP(7);
    wait_bar<LEB>();
    L(LEB, LE0);
    QSTAMP(8);
    bar_lds();
    __builtin_amdgcn_s_sleep(FDELAY);
  }
  STAMP(2);
  const u64 tag = (ep + 1) << 16;
  unsigned char* const wsb = P.ws + SET_BASE + (((unsigned)ep + 1u) & 3u) * SET_STRIDE;
  const __amdgpu_buffer_rsrc_t rsWs = mk_rsrc(wsb, WS_END);
  unsigned xoff[4];
#pragma unroll
  for (int ks = 0; ks < 4; ++ks) xoff[ks] = (unsigned)(L_X + r * 128 + (((2 * ks + h) ^ ((r >> 1) & 7)) << 4));
  if (ffn) {
    LAS unsigned char* ring = lds + L_W1 + wt * 8192;
    const unsigned troff = (unsigned)((8 * h + ((lane & 15) >> 2)) * 64 + ((lane >> 4) & 1) * 32 + (lane & 3) * 8);
#pragma unroll
    for (int i = 0; i < 4; ++i) acc[i] = f32x16{};
    RSTAMP(1);
    constexpr int LA = F1LA;
    bf16x8 xf[LA + 1][4], wf[LA + 1];
    auto wr = [&](int u) { *(LAS u32x4*)(ring + (u & 7) * 1024 + lo) = wl[u]; };
    auto rdw = [&](int u) { wf[u % (LA + 1)] = tr_frag(ring + (u & 7) * 1024 + troff, 4 * 64); };
    auto rdx = [&](int u, int t) { xf[u % (LA + 1)][t] = *(const LAS bf16x8*)(lds + xoff[u & 3] + (u >> 2) * 16384 + t * 4096); };
#pragma unroll
    for (int u = 0; u < LA + 2; ++u) wr(u);
#pragma unroll
    for (int u = 0; u < LA; ++u) {
      rdw(u);
#pragma unroll
      for (int t = 0; t < 4; ++t) rdx(u, t);
    }
    SBAR();
#pragma unroll
    for (int u = 0; u < 28; ++u) {
      if ((u & 3) == 0) RSTAMP(2 + (u >> 2));
#pragma unroll
      for (int t = 0; t < 4; ++t) {
        if (u + LA < 28) {
          if (t == 1) rdw(u + LA);
          rdx(u + LA, t);
          if (t == 3 && u + LA + 2 < 28) wr(u + LA + 2);
        }
        SBAR();
        acc[t] = MFMA(wf[u % (LA + 1)], xf[u % (LA + 1)][t], acc[t]);
        SBAR();
        if (u >= LU0 && LE0 + (u - LU0) * 4 + t < 28) L(LE0 + (u - LU0) * 4 + t, LE0 + (u - LU0) * 4 + t + 1);
      }
    }
    lds_arrive(lds + L_CTRL);
    RSTAMP(9);
    STAMP(3);
    __builtin_amdgcn_s_setprio(3);
#pragma unroll
    for (int t = 0; t < 4; ++t) {
      f32x16 z = acc[t];
#pragma unroll
      for (int i = 0; i < 16; ++i) z[i] = z[i] * __builtin_amdgcn_rcpf(1.f + __builtin_amdgcn_exp2f(-LOG2E * z[i]));
      const unsigned so = HF_OFF + ((rb * 4 + t) * 56 + j * 4 + wt) * 2048;
      st16(rsWs, as_u4(pack8(z, 0)), lo, so);
      st16(rsWs, as_u4(pack8(z, 1)), lo + 1024, so);
    }
    drain();
    if (lane == 0) put_granule(wsb, GH_OFF + (rb * 16 + j) * 128 + wt * 8, tag, 0);
  } else {
    __builtin_amdgcn_s_setprio(3);
    if (j == 14) {
      L(LE0, 28);
      acc[0] = f32x16{};
#pragma unroll
      for (int u = 0; u < 28; ++u) {
        const bf16x8 xf = *(const LAS bf16x8*)(lds + xoff[u & 3] + (u >> 2) * 16384 + wt * 4096);
        acc[0] = MFMA(as_frag(wl[u]), xf, acc[0]);
      }
      const float g = __builtin_amdgcn_rcpf(1.f + __builtin_amdgcn_exp2f(-LOG2E * acc[0][0]));
      if (h == 0) put_granule(wsb, GG_OFF + ((rb * 4 + wt) * 32 + r) * 8, tag, cvtpk(g, 0.f));
    }
    lds_arrive(lds + L_CTRL);
    const __amdgpu_buffer_rsrc_t rsO = mk_rsrc(P.wo, DM * DM * 2);
    const int id0 = ((j - 14) * 16 + rb) * 4 + wt;
    for (int id = id0; id < 224; id += 128) {
      const int nt = id >> 4, hd = id & 15;
      unsigned rp[16];
#pragma unroll
      for (int e = 0; e < 16; ++e)
        rp[e] = __builtin_amdgcn_raw_buffer_load_b16(rsO, (unsigned)(((4 * h + 8 * (e >> 2) + (e & 3)) * DM + r) * 2), (unsigned)((DH * hd * DM + 32 * nt) * 2), 0);
      u32x4 a, c2;
#pragma unroll
      for (int i = 0; i < 4; ++i) {
        a[i] = (rp[2 * i] & 0xffffu) | (rp[2 * i + 1] << 16);
        c2[i] = (rp[8 + 2 * i] & 0xffffu) | (rp[8 + 2 * i + 1] << 16);
      }
      c2[2] = h ? 0u : c2[2];
      c2[3] = h ? 0u : c2[3];
      st16(rsWs, a, lo, WOF_OFF + id * 2048);
      st16(rsWs, c2, lo + 1024, WOF_OFF + id * 2048);
      drain();
      if (lane == 0) put_granule(wsb, GW_OFF + id * 8, tag, 0);
    }
  }
  STAMP(4);
  if (idx >= 28) {
    if (b == 255 && wt == 0) {
      __builtin_amdgcn_s_sleep(127);
#pragma unroll
      for (int i = 0; i < 4; ++i) (void)poll_granule<64>(wsb, GQ_OFF + (i * 64 + lane) * GQW, tag);
      if (lane == 0) __hip_atomic_store((gu64*)(P.ws + FL_OFF), ep + 1, __ATOMIC_RELAXED, __HIP_MEMORY_SCOPE_AGENT);
    }
    STAMP(10);
    STAMP_FLUSH(P, 0);
    return;
  }
  const int mb = 4 * xcd + (idx & 3), nb = idx >> 2;
  const __amdgpu_buffer_rsrc_t rsW2 = mk_rsrc(P.w2, FF * DM * 2), rsH = rsWs;
  const unsigned w2src = (unsigned)((wt * 448 + (lane >> 3)) * (DM * 2) + nb * 128 + ((((lane >> 2) & 1) ^ ((lane >> 4) & 1)) * 64) + (lane & 3) * 16);
  const unsigned ring0 = lds0 + L_W2 + wt * 24576;
  const int rot2 = ROT2;
  auto fr = [&](int f) { const int v = f + rot2; return v >= 14 ? v - 14 : v; };
  auto D = [&](int f) {
#pragma unroll
    for (int i = 0; i < 4; ++i) glds16(rsW2, ring0 + (f % W2S) * 4096 + i * 1024, w2src, (fr(f) * 32 + i * 8) * DM * 2);
  };
  (void)wait_lds_word(lds + L_CTRL, 4);
#pragma unroll
  for (int f = 0; f < W2S; ++f) { D(f); __builtin_amdgcn_s_sleep(PACE); }
  const int ftl = wt * 14 + ((lane & 15) < 14 ? (lane & 15) : 0);
  (void)poll_granule<8>(wsb, (lane & 16) ? GW_OFF + ((nb * 2 + ((lane >> 2) & 1)) * 16 + wt * 4 + (lane & 3)) * 8
                                        : GH_OFF + ((mb >> 1) * 16 + (ftl >> 2)) * 128 + (ftl & 3) * 8, tag);
  STAMP(5);
  u32x4 hb[HPRE][4], wo[16];
  const unsigned hbase = HF_OFF + (mb * 2 * 56 + wt * 14) * 2048;
  auto H = [&](int f) {
#pragma unroll
    for (int e = 0; e < 4; ++e) hb[f % HPRE][e] = ld16c(rsH, lo + (e & 1) * 1024, hbase + ((e >> 1) * 56 + fr(f)) * 2048);
  };
#pragma unroll
  for (int f = 0; f < HPRE; ++f) { H(f); SBAR(); }
  const float gl = __uint_as_float(poll_granule<8>(wsb, GG_OFF + (mb * 64 + lane) * 8, tag) << 16), gs = swap_half(gl);
  const float og0 = 1.f - (h ? gs : gl), og1 = 1.f - (h ? gl : gs);
  const int q4 = (lane & 15) >> 2;
  unsigned woff[2];
#pragma unroll
  for (int nt = 0; nt < 2; ++nt) woff[nt] = (unsigned)(L_W2 + wt * 24576 + (4 * h + q4) * 128 + ((nt ^ (q4 >> 1)) * 64) + ((lane >> 4) & 1) * 32 + (lane & 3) * 8);
#pragma unroll
  for (int i = 0; i < 4; ++i) acc[i] = f32x16{};
  STAMP(6);
  __builtin_amdgcn_s_setprio(0);
  bf16x8 wf[2][2][2];
  auto RW = [&](int f) {
#pragma unroll
    for (int s = 0; s < 2; ++s)
#pragma unroll
      for (int nt = 0; nt < 2; ++nt) wf[f & 1][s][nt] = tr_frag(lds + woff[nt] + (f % W2S) * 4096 + s * 2048, 1024);
  };
  RW(0);
  static_for<0, 14>([&](auto fc) {
    constexpr int f = decltype(fc)::value;
    asm volatile("s_waitcnt lgkmcnt(0)" ::: "memory");
    SBAR();
    if constexpr (f + 1 < 14) {
      if constexpr (f + 1 >= W2S) wait_vm<ffn2_younger(f + 1, f)>();
      RW(f + 1);
    }
    SBAR();
#pragma unroll
    for (int s = 0; s < 2; ++s)
#pragma unroll
      for (int nt = 0; nt < 2; ++nt)
#pragma unroll
        for (int mt = 0; mt < 2; ++mt) {
          acc[nt * 2 + mt] = MFMA(wf[f & 1][s][nt], as_frag(hb[f % HPRE][mt * 2 + s]), acc[nt * 2 + mt]);
        }
    SBAR();
    if constexpr (f + W2S < 14) D(f + W2S);
    if constexpr (f + HPRE < 14) H(f + HPRE);
    else if constexpr (f + HPRE < 18) {
      constexpr int e0 = (f + HPRE - 14) * 4;
#pragma unroll
      for (int e = e0; e < e0 + 4; ++e) wo[e] = ld16c(rsWs, lo, WOF_OFF + ((nb * 2 + (e >> 3)) * 16 + wt * 4) * 2048 + (e & 7) * 1024);
    }
    SBAR();
    __builtin_amdgcn_s_sleep(PACE2);
    SBAR();
  });
  STAMP(7);
  __builtin_amdgcn_s_setprio(3);
#pragma unroll
  for (int i = 0; i < 16; ++i) { acc[0][i] *= og0; acc[1][i] *= og1; acc[2][i] *= og0; acc[3][i] *= og1; }
  (void)wait_lds_word(lds + L_CTRL + 20, 1);
  __builtin_amdgcn_s_sleep(ODELAY);
  (void)poll_granule<POLL_A>(wsb, GO_OFF + ((mb * 2 + ((lane >> 2) & 1)) * 16 + wt * 4 + (lane & 3)) * 128, tag);
  STAMP(8);
  u32x4 of[16];
#pragma unroll
  for (int e = 0; e < 8; ++e)
#pragma unroll
    for (int mt = 0; mt < 2; ++mt) of[mt * 8 + e] = ld16c(rsWs, lo, OF_OFF + ((mb * 2 + mt) * 16 + wt * 4) * 2048 + e * 1024);
  SBAR();
#pragma unroll
  for (int e = 0; e < 8; ++e)
#pragma unroll
    for (int nt = 0; nt < 2; ++nt)
#pragma unroll
      for (int mt = 0; mt < 2; ++mt) acc[nt * 2 + mt] = MFMA(as_frag(wo[nt * 8 + e]), as_frag(of[mt * 8 + e]), acc[nt * 2 + mt]);
#pragma unroll
  for (int t = 0; t < 4; ++t) st_tile(lds + L_W2 + wt * 24576 + t * 4096 + lo, acc[t]);
  STAMP(9);
  bar_lds();
  f32x16 a = f32x16{};
#pragma unroll
  for (int f = 0; f < 4; ++f) add_tile(a, lds + L_W2 + f * 24576 + wt * 4096 + lo);
  unsigned short* dst = P.out + (size_t)((mb * 2 + (wt & 1)) * 32 + r) * DM + (nb * 2 + (wt >> 1)) * 32 + 8 * h;
#pragma unroll
  for (int k = 0; k < 2; ++k) {
    const auto x = __builtin_amdgcn_permlane32_swap(cvtpk(a[8 * k], a[8 * k + 1]), cvtpk(a[8 * k + 4], a[8 * k + 5]), false, false);
    const auto y = __builtin_amdgcn_permlane32_swap(cvtpk(a[8 * k + 2], a[8 * k + 3]), cvtpk(a[8 * k + 6], a[8 * k + 7]), false, false);
    *(u32x4*)(dst + 16 * k) = u32x4{x[0], y[0], x[1], y[1]};
  }
  STAMP(10);
  STAMP_FLUSH(P, 0);
}

extern __shared__ __attribute__((aligned(16))) unsigned char g_lds[];

__global__ __launch_bounds__(512, 2) void k_fused(Params P) {
  LAS unsigned char* lds = (LAS unsigned char*)g_lds;
  const int w = __builtin_amdgcn_readfirstlane(threadIdx.x >> 6);
  if (w < 4) role_a(P, lds, w);
  else role_f(P, lds, w - 4);
}

extern "C" void kernel_launch(void* const* d_in, const int* in_sizes, int n_in, void* d_out, int out_size, void* d_ws,
                              size_t ws_size, hipStream_t stream) {
  Params P;
  P.x = (const unsigned short*)d_in[0];
  P.wqkv = (const unsigned short*)d_in[1];
  P.wo = (const unsigned short*)d_in[2];
  P.w1 = (const unsigned short*)d_in[4];
  P.w2 = (const unsigned short*)d_in[5];
  P.gw = (const unsigned short*)d_in[6];
  P.out = (unsigned short*)d_out;
  P.ws = (unsigned char*)d_ws;
  (void)hipFuncSetAttribute((const void*)k_fused, hipFuncAttributeMaxDynamicSharedMemorySize, LDS_BYTES);
  k_fused<<<256, 512, LDS_BYTES, stream>>>(P);
}
```

```cpp
#include <hip/hip_runtime.h>
#include <hip/hip_bf16.h>
#include <stdint.h>

typedef short bf16x8 __attribute__((ext_vector_type(8)));
typedef short s16x4 __attribute__((ext_vector_type(4)));
typedef float f32x16 __attribute__((ext_vector_type(16)));
typedef float f32x4 __attribute__((ext_vector_type(4)));
typedef unsigned u32x4 __attribute__((ext_vector_type(4)));
typedef unsigned u32x2 __attribute__((ext_vector_type(2)));
#define LAS __attribute__((address_space(3)))
#define DI __device__ __forceinline__
#define MFMA(a, b, c) __builtin_amdgcn_mfma_f32_32x32x16_bf16((a), (b), (c), 0, 0, 0)
#define SBAR() __builtin_amdgcn_sched_barrier(0)

constexpr int SEQ = 2048, DM = 448, NH = 16, DH = 28, FF = 1792;
constexpr unsigned FL_OFF = 0;
#define SETB 1u
constexpr unsigned SET_BASE = SETB << 20, SET_STRIDE = 24u << 20;
#define GQS 16u
constexpr unsigned GQW = GQS == 16u ? 128u : 4u * GQS;
constexpr unsigned GQ_OFF = 0;
constexpr unsigned GH_OFF = GQ_OFF + 256 * GQW;
constexpr unsigned GO_OFF = GH_OFF + 256 * 128;
constexpr unsigned GW_OFF = GO_OFF + 1024 * 128;
constexpr unsigned GG_OFF = GW_OFF + 256 * 8;
constexpr unsigned QF_OFF = 524288;
constexpr unsigned KV_OFF = QF_OFF + 16 * 64 * 2048;
constexpr unsigned HF_OFF = KV_OFF + 16 * 64 * 4096;
constexpr unsigned WOF_OFF = HF_OFF + 64 * 56 * 2048;
constexpr unsigned OF_OFF = WOF_OFF + 14 * 16 * 2048;
constexpr unsigned WS_END = OF_OFF + 64 * 16 * 2048;
static_assert(GG_OFF + 2048 * 8 <= QF_OFF && WS_END <= SET_STRIDE, "workspace map");

constexpr int L_X = 0;
constexpr int L_WQ = 114688;
constexpr int L_W1 = 114688;
constexpr int L_CTRL = 163776;
constexpr int L_W2 = 0;
constexpr int L_CMB = 98304;
constexpr int LDS_BYTES = 163840;

constexpr float QSCALE = 0.18898223650461362f * 1.4426950408889634f;
constexpr float LOG2E = 1.4426950408889634f;
constexpr float SAFE_BOUND = 60.0f;
constexpr unsigned SPIN_CAP = 1u << 18;
#define ROT1(rb, j) 0
#define KVD 4
#define ROTA 0
#define ROT2 0
#define PDELAY 16
#define ODELAY 28
#define FDELAY 18
#define POLL_A 4

struct Params {
  const unsigned short* x;
  const unsigned short* wqkv;
  const unsigned short* wo;
  const unsigned short* w1;
  const unsigned short* w2;
  const unsigned short* gw;
  unsigned short* out;
  unsigned char* ws;
};
typedef unsigned long long u64;
typedef __attribute__((address_space(1))) u64 gu64;

DI unsigned cvtpk(float lo, float hi) {
  typedef __bf16 bf2 __attribute__((ext_vector_type(2)));
  typedef float f2 __attribute__((ext_vector_type(2)));
  f2 v = {lo, hi};
  bf2 r = __builtin_convertvector(v, bf2);
  return __builtin_bit_cast(unsigned, r);
}
DI bf16x8 pack8(const f32x16& x, int s) {
  u32x4 p;
  p[0] = cvtpk(x[8 * s + 0], x[8 * s + 1]);
  p[1] = cvtpk(x[8 * s + 2], x[8 * s + 3]);
  p[2] = cvtpk(x[8 * s + 4], x[8 * s + 5]);
  p[3] = cvtpk(x[8 * s + 6], x[8 * s + 7]);
  return __builtin_bit_cast(bf16x8, p);
}
DI float swap_half(float v) {
  auto r = __builtin_amdgcn_permlane32_swap(__float_as_uint(v), __float_as_uint(v), false, false);
  return (threadIdx.x & 32) ? __uint_as_float(r[0]) : __uint_as_float(r[1]);
}
template <int CTRL> DI float dpp_mov(float v) {
  return __uint_as_float(__builtin_amdgcn_update_dpp(__float_as_uint(v), __float_as_uint(v), CTRL, 0xf, 0xf, false));
}
DI float row16_max(float v) {
  v = fmaxf(v, dpp_mov<0x128>(v));
  v = fmaxf(v, dpp_mov<0x124>(v));
  v = fmaxf(v, dpp_mov<0x122>(v));
  v = fmaxf(v, dpp_mov<0x121>(v));
  return v;
}
DI unsigned row16_umax(unsigned v) {
  v = max(v, (unsigned)__builtin_amdgcn_update_dpp(v, v, 0x128, 0xf, 0xf, false));
  v = max(v, (unsigned)__builtin_amdgcn_update_dpp(v, v, 0x124, 0xf, 0xf, false));
  v = max(v, (unsigned)__builtin_amdgcn_update_dpp(v, v, 0x122, 0xf, 0xf, false));
  v = max(v, (unsigned)__builtin_amdgcn_update_dpp(v, v, 0x121, 0xf, 0xf, false));
  return v;
}
DI float rdlane(float v, int l) { return __uint_as_float(__builtin_amdgcn_readlane(__float_as_uint(v), l)); }
DI s16x4 tr_read(const LAS unsigned char* p) {
  typedef short v4i16_t __attribute__((ext_vector_type(4)));
  return __builtin_bit_cast(s16x4, __builtin_amdgcn_ds_read_tr16_b64_v4i16((LAS v4i16_t*)p));
}
DI bf16x8 tr_frag(const LAS unsigned char* p, int row_stride4) {
  s16x4 lo = tr_read(p), hi = tr_read(p + row_stride4);
  return __builtin_shufflevector(lo, hi, 0, 1, 2, 3, 4, 5, 6, 7);
}
DI __amdgpu_buffer_rsrc_t mk_rsrc(const void* p, unsigned bytes) {
  return __builtin_amdgcn_make_buffer_rsrc((void*)p, (short)0, (int)bytes, 0x00020000);
}
DI void glds16(__amdgpu_buffer_rsrc_t rs, unsigned dst, unsigned voff, unsigned soff) {
  asm volatile("s_mov_b32 m0, %0\n\ts_nop 0\n\tbuffer_load_dwordx4 %1, %2, %3 offen lds" ::"s"(dst), "v"(voff), "s"(rs), "s"(soff) : "memory");
}
DI u32x4 ld16(__amdgpu_buffer_rsrc_t rs, unsigned voff, unsigned soff) { return __builtin_amdgcn_raw_buffer_load_b128(rs, voff, soff, 0); }
DI u32x4 ld16c(__amdgpu_buffer_rsrc_t rs, unsigned voff, unsigned soff) { return __builtin_amdgcn_raw_buffer_load_b128(rs, voff, soff, 16); }
DI void st16(__amdgpu_buffer_rsrc_t rs, u32x4 v, unsigned voff, unsigned soff) { __builtin_amdgcn_raw_buffer_store_b128(v, rs, voff + soff, 0, 16); }
DI bf16x8 as_frag(u32x4 v) { return __builtin_bit_cast(bf16x8, v); }
DI u32x4 as_u4(bf16x8 v) { return __builtin_bit_cast(u32x4, v); }
template <int N> DI void wait_bar() { asm volatile("s_waitcnt vmcnt(%0) lgkmcnt(0)\n\ts_barrier" ::"n"(N) : "memory"); }

template <int V> struct IC { static constexpr int value = V; };
template <int I, int N, class Fn> DI void static_for(Fn&& fn) {
  if constexpr (I < N) { fn(IC<I>{}); static_for<I + 1, N>(fn); }
}
DI void drain() { asm volatile("s_waitcnt vmcnt(0)" ::: "memory"); }
template <int N> DI void wait_vm() { asm volatile("s_waitcnt vmcnt(%0)" ::"n"(N) : "memory"); }
DI void put_granule(unsigned char* ws, unsigned off, u64 tag16, unsigned payload) {
  __hip_atomic_store((gu64*)(ws + off), tag16 | (u64)(payload & 0xffffu), __ATOMIC_RELAXED, __HIP_MEMORY_SCOPE_AGENT);
}
template <int SLEEP> DI unsigned poll_granule(unsigned char* ws, unsigned off, u64 tag16) {
  u64 v = ~tag16;
  for (unsigned spins = 0;; ++spins) {
    if (v - tag16 > 0xffffull) v = __hip_atomic_load((gu64*)(ws + off), __ATOMIC_RELAXED, __HIP_MEMORY_SCOPE_AGENT);
    if (__all(v - tag16 <= 0xffffull) || spins > SPIN_CAP) break;
    __builtin_amdgcn_s_sleep(SLEEP);
  }
  __builtin_amdgcn_fence(__ATOMIC_ACQUIRE, "wavefront");
  return (unsigned)v & 0xffffu;
}
DI unsigned wait_lds_word(LAS unsigned char* p, unsigned want) {
  unsigned v = 0;
#pragma nounroll
  for (unsigned spins = 0; spins < (1u << 24); ++spins) {
    v = __builtin_amdgcn_readfirstlane(__hip_atomic_load((LAS unsigned*)p, __ATOMIC_RELAXED, __HIP_MEMORY_SCOPE_WORKGROUP));
    if (v >= want) break;
    __builtin_amdgcn_s_sleep(1);
  }
  asm volatile("" ::: "memory");
  return v;
}
template <int N> DI u64 wait_bar_epoch(const unsigned char* p) {
  u64 ep;
  asm volatile("s_load_dwordx2 %0, %1, 0x0\n\ts_waitcnt vmcnt(%2) lgkmcnt(0)\n\ts_barrier" : "=&s"(ep) : "s"(p), "n"(N) : "memory");
  return ep;
}
DI void bar_lds() { asm volatile("s_waitcnt lgkmcnt(0)\n\ts_barrier" ::: "memory"); }
DI void st_tile(LAS unsigned char* p, const f32x16& a) {
#pragma unroll
  for (int g = 0; g < 4; ++g) *(LAS f32x4*)(p + g * 1024) = f32x4{a[4 * g], a[4 * g + 1], a[4 * g + 2], a[4 * g + 3]};
}
DI void add_tile(f32x16& a, const LAS unsigned char* p) {
#pragma unroll
  for (int g = 0; g < 4; ++g) {
    const f32x4 v = *(const LAS f32x4*)(p + g * 1024);
    a[4 * g] += v[0]; a[4 * g + 1] += v[1]; a[4 * g + 2] += v[2]; a[4 * g + 3] += v[3];
  }
}

#define STAMP_DECL do {} while (0)
#define STAMP(id) do {} while (0)
#define QSTAMP(id) do {} while (0)
#define RSTAMP(id) do {} while (0)
#define STAMP_FLUSH(P, ph) do {} while (0)

DI void lds_arrive(LAS unsigned char* p) {
  asm volatile("s_mov_b64 exec, 1\n\tds_add_u32 %0, %1\n\ts_mov_b64 exec, -1" ::"v"((unsigned)(uintptr_t)p), "v"(1u) : "memory");
}

#define PINV(x) asm volatile("" : "+v"(x))
#define PINS(x) asm volatile("" : "+s"(x))
DI float sumsq(const u32x4& a, const u32x4& b) {
  typedef __bf16 bf2 __attribute__((ext_vector_type(2)));
  float n0 = 0.f, n1 = 0.f;
#pragma unroll
  for (int i = 0; i < 4; ++i) {
    n0 = __builtin_amdgcn_fdot2_f32_bf16(__builtin_bit_cast(bf2, a[i]), __builtin_bit_cast(bf2, a[i]), n0, false);
    n1 = __builtin_amdgcn_fdot2_f32_bf16(__builtin_bit_cast(bf2, b[i]), __builtin_bit_cast(bf2, b[i]), n1, false);
  }
  return n0 + n1;
}

DI void role_a(const Params& P, LAS unsigned char* lds, const int wt) {
  const int lane = threadIdx.x & 63, r = lane & 31, h = lane >> 5;
  const int b = blockIdx.x, xcd = b & 7, idx = b >> 3;
  const unsigned lo = lane * 16;
  STAMP_DECL;
  STAMP(0);
  __builtin_amdgcn_s_setprio(2);
  f32x16 acc[4];
  const int rb = (xcd & 1) * 8 + (idx & 7), j = (xcd >> 1) * 4 + (idx >> 3);
  const int head = xcd * 2 + (idx & 1), qb = idx >> 1;
  unsigned sto[3];
#pragma unroll
  for (int ct = 0; ct < 3; ++ct) {
    const int u = 3 * j + ct, part = u >> 4, t = (u & 15) * 64 + rb * 4 + wt;
    sto[ct] = lo + (part == 0 ? QF_OFF + t * 2048 : KV_OFF + t * 4096 + (part - 1) * 2048);
    PINV(sto[ct]);
  }
  unsigned padv = r == DH ? 0x3f803f80u : 0u;
  PINV(padv);
  const int gq = head / 3, gk = (16 + head) / 3, gvv = (32 + head) / 3;
  unsigned pub_off = GQ_OFF + (j * 16 + rb) * GQW + wt * GQS + (lane & 1) * 8;
  unsigned poll_off = GQ_OFF + (h ? gq * 16 + qb : (lane < 16 ? gk : gvv) * 16 + wt * 4 + ((lane >> 2) & 3)) * GQW + (lane & 3) * GQS + h * 8;
  unsigned chk_off = GQ_OFF + (gk * 16 + (lane >> 2)) * GQW + (lane & 3) * GQS;
  unsigned g_off = GG_OFF + ((qb * 4 + wt) * 32 + r) * 8;
  unsigned o_off = lo + OF_OFF + ((qb * 4 + wt) * 16 + head) * 2048;
  PINV(pub_off); PINV(poll_off); PINV(chk_off); PINV(g_off); PINV(o_off);
  unsigned kvbase = KV_OFF + (head * 64 + wt * 16) * 4096, go_off = GO_OFF + ((qb * 4 + wt) * 16 + head) * 128;
  unsigned qofs[4];
#pragma unroll
  for (int t = 0; t < 4; ++t) { qofs[t] = QF_OFF + (unsigned)(head * 64 + qb * 4 + ((wt + 1 + t) & 3)) * 2048; PINS(qofs[t]); }
  PINS(kvbase); PINS(go_off);
  unsigned xoff[4];
#pragma unroll
  for (int ks = 0; ks < 4; ++ks) xoff[ks] = (unsigned)(L_X + wt * 4096 + r * 128 + (((2 * ks + h) ^ ((r >> 1) & 7)) << 4));
  const unsigned wqoff = (unsigned)(L_WQ + (8 * h + ((lane & 15) >> 2)) * 192 + ((lane >> 4) & 1) * 32 + (lane & 3) * 8);
  u64 ep;
  asm volatile("s_load_dwordx2 %0, %1, 0x0\n\ts_waitcnt lgkmcnt(0)" : "=&s"(ep) : "s"(P.ws + FL_OFF) : "memory");
  u64 tag16 = (ep + 1) << 16;
  unsigned char* wsb = P.ws + SET_BASE + (((unsigned)ep + 1u) & 3u) * SET_STRIDE;
  PINS(tag16); PINS(wsb);
  const __amdgpu_buffer_rsrc_t rsWs = mk_rsrc(wsb, WS_END);
#pragma unroll
  for (int i = 0; i < 3; ++i) acc[i] = f32x16{};
  auto kloop = [&](auto nvc) {
    constexpr int NV = decltype(nvc)::value;
    bf16x8 xf[4], wf[4][3];
    auto mm = [&](int ks, int ct) { acc[ct] = ct >= 3 - NV ? MFMA(xf[ks], wf[ks][ct], acc[ct]) : MFMA(wf[ks][ct], xf[ks], acc[ct]); };
    auto step = [&](int c, int ks, bool domm) {
      const int km = (ks + 2) & 3;
#pragma unroll
      for (int ct = 0; ct < 3; ++ct) {
        if (domm) mm(km, ct);
        SBAR();
        if (ct == 0) xf[ks] = *(const LAS bf16x8*)(lds + xoff[ks] + c * 16384);
        wf[ks][ct] = tr_frag(lds + wqoff + (c & 3) * 12288 + ks * 16 * 192 + ct * 56, 4 * 192);
        SBAR();
      }
    };
#pragma unroll
    for (int c = 0; c < 7; ++c) {
      bar_lds();
      if (c == 0) STAMP(1);
      QSTAMP(1 + c);
      step(c, 0, c > 0);
      step(c, 1, c > 0);
      step(c, 2, true);
      step(c, 3, true);
    }
    bar_lds();
    QSTAMP(8);
#pragma unroll
    for (int ct = 0; ct < 3; ++ct) { mm(2, ct); mm(3, ct); SBAR(); }
  };
  auto epi = [&](auto nqc, auto nvc) {
    constexpr int NQ = decltype(nqc)::value, NV = decltype(nvc)::value;
    STAMP(2);
    u32x4 t0[3], t1[3];
#pragma unroll
    for (int ct = 0; ct < 3; ++ct) {
      if (ct < NQ) acc[ct] *= QSCALE;
      t0[ct] = as_u4(pack8(acc[ct], 0));
      if (ct >= 3 - NV) {
#pragma unroll
        for (int i = 0; i < 4; ++i) t0[ct][i] = r < DH ? t0[ct][i] : padv;
      }
      st16(rsWs, t0[ct], sto[ct], 0);
      t1[ct] = as_u4(pack8(acc[ct], 1));
      if (ct >= 3 - NV) {
#pragma unroll
        for (int i = 0; i < 4; ++i) t1[ct][i] = r < DH ? t1[ct][i] : padv;
      } else {
        t1[ct][2] = h ? (ct < NQ ? 0u : 0x3f803f80u) : t1[ct][2];
        t1[ct][3] = h ? (ct < NQ ? 0u : 0x00003f80u) : t1[ct][3];
      }
      st16(rsWs, t1[ct], sto[ct], 1024);
      SBAR();
    }
    STAMP(8);
    unsigned mq = 0, mk = 0;
#pragma unroll
    for (int ct = 0; ct < 3; ++ct) {
      const unsigned n = __float_as_uint(sumsq(t0[ct], t1[ct])) & 0x7fffffffu;
      if (ct < NQ) mq = max(mq, n); else mk = max(mk, n);
    }
    auto bound = [&](unsigned m) {
      m = row16_umax(m);
      const float lo2 = __uint_as_float(max((unsigned)__builtin_amdgcn_readlane(m, 0), (unsigned)__builtin_amdgcn_readlane(m, 16)));
      const float hi2 = __uint_as_float(max((unsigned)__builtin_amdgcn_readlane(m, 32), (unsigned)__builtin_amdgcn_readlane(m, 48)));
      return ((__float_as_uint(lo2 + hi2) >> 16) + 1u) & 0xffffu;
    };
    const unsigned pq = NQ > 0 ? bound(mq) : 0u, pk = NQ < 3 ? bound(mk) : 0u;
    u64 gran = tag16 | ((lane & 1) ? pq : pk);
    PINV(gran);
    drain();
    STAMP(9);
    if (lane < 2) __hip_atomic_store((gu64*)(wsb + pub_off), gran, __ATOMIC_RELAXED, __HIP_MEMORY_SCOPE_AGENT);
  };
  if (j < 10) {
    kloop(IC<0>{});
    if (j < 5) epi(IC<3>{}, IC<0>{});
    else if (j == 5) epi(IC<1>{}, IC<0>{});
    else epi(IC<0>{}, IC<0>{});
  } else if (j == 10) {
    kloop(IC<1>{});
    epi(IC<0>{}, IC<1>{});
  } else {
    kloop(IC<3>{});
    epi(IC<0>{}, IC<3>{});
  }
  STAMP(3);
  __builtin_amdgcn_s_sleep(PDELAY);
  const unsigned pay = poll_granule<POLL_A>(wsb, poll_off, tag16);
  STAMP(4);
  u32x4 q[4][2];
  LAS unsigned char* const cmb = lds + L_CMB + wt * 12288 + lo;
  u64 gv = 0;
  unsigned thr = 0;
#pragma nounroll
  for (int pass = 0;; ++pass) {
    u32x4 kv[KVD][4];
    kv[0][0] = ld16c(rsWs, lo, kvbase);
    SBAR();
    if (pass == 0) { q[0][0] = ld16c(rsWs, lo, qofs[0]); q[0][1] = ld16c(rsWs, lo + 1024, qofs[0]); }
    SBAR();
    kv[0][1] = ld16c(rsWs, lo + 1024, kvbase);
    SBAR();
    if (pass == 0) {
#pragma unroll
      for (int t = 1; t < 4; ++t) { q[t][0] = ld16c(rsWs, lo, qofs[t]); q[t][1] = ld16c(rsWs, lo + 1024, qofs[t]); SBAR(); }
    }
#pragma unroll
    for (int e = 2; e < 4 * (KVD - 1); ++e) { kv[e >> 2][e & 3] = ld16c(rsWs, lo + (e & 3) * 1024, kvbase + (e >> 2) * 4096); SBAR(); }
#pragma unroll
    for (int i = 0; i < 4; ++i) acc[i] = f32x16{};
    {
      const float q2 = __uint_as_float(__builtin_amdgcn_readlane(row16_umax(pay), 32) << 16);
      const float lim = SAFE_BOUND * SAFE_BOUND * __builtin_amdgcn_rcpf(q2);
      thr = lim < 1e30f ? __float_as_uint(lim) >> 16 : (lim >= 1e30f ? 0x7000u : 0u);
      PINV(thr);
    }
    __builtin_amdgcn_s_setprio(1);
    f32x16 sc = MFMA(as_frag(kv[0][0]), as_frag(q[0][0]), f32x16{});
    sc = MFMA(as_frag(kv[0][1]), as_frag(q[0][1]), sc);
    STAMP(5);
    u32x4 p0, p1 = {0, 0, 0, 0};
    float e0 = 0.f, e1 = 0.f, e2 = 0.f, e3 = 0.f;
    u64 ck = 0, cv = 0;
#define EXP4(i) do { e0 = __builtin_amdgcn_exp2f(sc[i]); e1 = __builtin_amdgcn_exp2f(sc[(i) + 1]); e2 = __builtin_amdgcn_exp2f(sc[(i) + 2]); e3 = __builtin_amdgcn_exp2f(sc[(i) + 3]); \
    asm volatile("" : "+v"(e0), "+v"(e1), "+v"(e2), "+v"(e3)); SBAR(); } while (0)
#define CVT2(P, i) do { P[i] = cvtpk(e0, e1); P[(i) + 1] = cvtpk(e2, e3); asm volatile("" : "+v"(P)); SBAR(); } while (0)
#define PART(t, g) do { *(LAS f32x4*)(cmb + (t) * 4096 + (g) * 1024) = f32x4{acc[t][4 * (g)], acc[t][4 * (g) + 1], acc[t][4 * (g) + 2], acc[t][4 * (g) + 3]}; SBAR(); } while (0)
    static_for<0, 64>([&](auto ic) {
      constexpr int i = decltype(ic)::value;
      constexpr int kt = i >> 2, t = i & 3, kn_ = (i + 1) >> 2, tn = (i + 1) & 3, kp = (i - 1) >> 2, tp = (i - 1) & 3;
      constexpr int kl = kt + KVD - 1;
      if (i == 24) STAMP(11);
      if (i == 56) (void)wait_lds_word(lds + L_CTRL, 4);
      f32x16 sn;
      SBAR();
      if (i < 63) sn = MFMA(as_frag(kv[kn_ % KVD][0]), as_frag(q[tn][0]), f32x16{});
      SBAR();
      if constexpr (i >= 62) PART(i - 62, 0);
      if (i > 0) CVT2(p1, 2);
      EXP4(0);
      if (i > 0) acc[tp] = MFMA(as_frag(kv[(kp + KVD) % KVD][3]), as_frag(p1), acc[tp]);
      SBAR();
      if (kl < 16) kv[kl % KVD][t] = ld16c(rsWs, lo + t * 1024, kvbase + kl * 4096);
      if (i == 52) gv = __hip_atomic_load((gu64*)(wsb + g_off), __ATOMIC_RELAXED, __HIP_MEMORY_SCOPE_AGENT);
      if (i == 53) ck = __hip_atomic_load((gu64*)(wsb + chk_off), __ATOMIC_RELAXED, __HIP_MEMORY_SCOPE_AGENT);
      if (i == 54) cv = __hip_atomic_load((gu64*)(wsb + (gvv - gk) * 16 * GQW + chk_off), __ATOMIC_RELAXED, __HIP_MEMORY_SCOPE_AGENT);
      SBAR();
      if constexpr (i >= 62) PART(i - 62, 1);
      CVT2(p0, 0);
      EXP4(4);
      if (i < 63) sn = MFMA(as_frag(kv[kn_ % KVD][1]), as_frag(q[tn][1]), sn);
      SBAR();
      if constexpr (i >= 62) PART(i - 62, 2);
      CVT2(p0, 2);
      EXP4(8);
      acc[t] = MFMA(as_frag(kv[kt % KVD][2]), as_frag(p0), acc[t]);
      SBAR();
      if constexpr (i >= 62) PART(i - 62, 3);
      CVT2(p1, 0);
      EXP4(12);
      if (i < 63) sc = sn;
    });
    CVT2(p1, 2);
    acc[3] = MFMA(as_frag(kv[15 % KVD][3]), as_frag(p1), acc[3]);
    SBAR();
    PART(2, 0); PART(2, 1); PART(2, 2); PART(2, 3);
#undef EXP4
#undef CVT2
#undef PART
    __builtin_amdgcn_s_setprio(2);
    if (__builtin_expect(__all(ck - tag16 <= (u64)thr && cv - tag16 <= 0x7000ull && gv - tag16 <= 0xffffull), 1)) break;
    gv = poll_granule<2>(wsb, g_off, tag16);
    if (pass) break;
    const unsigned pk = poll_granule<2>(wsb, chk_off, tag16), pv = poll_granule<2>(wsb, (gvv - gk) * 16 * GQW + chk_off, tag16);
    if (__all(pk <= thr && pv <= 0x7000u)) break;
    float mx[4];
#pragma unroll
    for (int t = 0; t < 4; ++t) mx[t] = -INFINITY;
    for (int kt = 0; kt < 64; ++kt) {
      const u32x4 k0 = ld16c(rsWs, lo, KV_OFF + (head * 64 + kt) * 4096), k1 = ld16c(rsWs, lo + 1024, KV_OFF + (head * 64 + kt) * 4096);
#pragma unroll
      for (int t = 0; t < 4; ++t) {
        f32x16 s = MFMA(as_frag(k0), as_frag(q[t][0]), f32x16{});
        s = MFMA(as_frag(k1), as_frag(q[t][1]), s);
#pragma unroll
        for (int i = 0; i < 16; ++i) mx[t] = fmaxf(mx[t], s[i]);
      }
    }
#pragma unroll
    for (int t = 0; t < 4; ++t) {
      const float nm = -fmaxf(mx[t], swap_half(mx[t]));
      const unsigned hi = __float_as_uint(nm) & 0xffff0000u;
      const float r1 = nm - __uint_as_float(hi);
      const unsigned mid = __float_as_uint(r1) & 0xffff0000u;
      const float r2 = r1 - __uint_as_float(mid);
      if (h) { q[t][1][2] = (hi >> 16) | mid; q[t][1][3] = __float_as_uint(r2) >> 16; }
    }
  }
  STAMP(6);
  asm volatile("s_waitcnt lgkmcnt(0)" ::: "memory");
  lds_arrive(lds + L_CTRL + 20);
  (void)wait_lds_word(lds + L_CTRL + 20, 4);
  f32x16 o = acc[3];
#pragma unroll
  for (int u = 0; u < 3; ++u) add_tile(o, lds + L_CMB + ((wt + 3 - u) & 3) * 12288 + u * 4096 + lo);
  const auto lsw = __builtin_amdgcn_permlane32_swap(__float_as_uint(o[12]), __float_as_uint(o[12]), false, false);
  const float inv = __uint_as_float((unsigned)gv << 16) * __builtin_amdgcn_rcpf(__uint_as_float(lsw[1]));
  o *= inv;
  st16(rsWs, as_u4(pack8(o, 0)), o_off, 0);
  u32x4 o1 = as_u4(pack8(o, 1));
  o1[2] = h ? 0u : o1[2];
  o1[3] = h ? 0u : o1[3];
  st16(rsWs, o1, o_off, 1024);
  STAMP(7);
  drain();
  if (lane == 0) __hip_atomic_store((gu64*)(wsb + go_off), tag16, __ATOMIC_RELAXED, __HIP_MEMORY_SCOPE_AGENT);
  STAMP(10);
  STAMP_FLUSH(P, 0);
}

constexpr int W2S = 6;
constexpr int HPRE = 5;
#define LEA 5
#define LEB 11
#define LE0 16
#define LU0 3
#define F1LA 2
#define PACE 4
#define PACE2 2
constexpr int ffn2_ops(int f) { return (f + W2S < 14 ? 4 : 0) + 4; }
constexpr int ffn2_younger(int f, int upto) {
  int n = 4;
  for (int i = f - W2S + 1; i < upto; ++i) n += ffn2_ops(i);
  return n;
}

DI void role_f(const Params& P, LAS unsigned char* lds, const int wt) {
  u64 ep;
  const int lane = threadIdx.x & 63, r = lane & 31, h = lane >> 5;
  const int b = blockIdx.x, xcd = b & 7, idx = b >> 3;
  const int rb = (xcd & 1) * 8 + (idx & 7), j = (xcd >> 1) * 4 + (idx >> 3);
  const unsigned lo = lane * 16, lds0 = (unsigned)(uintptr_t)lds;
  const bool ffn = j < 14;
  STAMP_DECL;
  STAMP(0);
  f32x16 acc[4];
  u32x4 wl[28];
  const __amdgpu_buffer_rsrc_t rsL = mk_rsrc(ffn ? P.w1 : P.gw, ffn ? DM * FF * 2 : DM * 2);
  const unsigned lsrc = ffn ? (unsigned)((lane >> 2) * (FF * 2) + (j * 4 + wt) * 64 + (lane & 3) * 16) : (unsigned)(h * 16);
  const unsigned lstep = ffn ? 16 * FF * 2 : 32;
  const int rot = ROT1(rb, j);
  auto kc = [&](int c) { const int v = c + rot; return v >= 7 ? v - 7 : v; };
  auto L = [&](int u0, int u1) {
#pragma unroll
    for (int u = u0; u < u1; ++u) wl[u] = ld16(rsL, lsrc, (kc(u >> 2) * 4 + (u & 3)) * lstep);
    SBAR();
  };
  {
    const __amdgpu_buffer_rsrc_t rsX = mk_rsrc(P.x, SEQ * DM * 2);
    const __amdgpu_buffer_rsrc_t rsW = mk_rsrc(P.wqkv, DM * 3 * DM * 2);
    unsigned xsrc[2], wsrc[3];
#pragma unroll
    for (int i = 0; i < 2; ++i) {
      const int row = 32 * wt + 8 * i + (lane >> 3), ch = (lane & 7) ^ ((row >> 1) & 7);
      xsrc[i] = (unsigned)((rb * 128 + row) * (DM * 2) + ch * 16);
    }
#pragma unroll
    for (int i = 0; i < 3; ++i) {
      const int p = 64 * (3 * wt + i) + lane, kr = p / 12, ch = p % 12;
      wsrc[i] = (unsigned)(kr * (3 * DM * 2) + j * 168 + (ch < 11 ? ch : 10) * 16);
    }
    auto X = [&](int c) {
#pragma unroll
      for (int i = 0; i < 4; ++i) glds16(rsX, lds0 + L_X + c * 16384 + (4 * wt + i) * 1024, xsrc[i & 1], kc(c) * 128 + (i >> 1) * 16 * DM * 2);
    };
    auto W = [&](int c) {
#pragma unroll
      for (int i = 0; i < 3; ++i) glds16(rsW, lds0 + L_WQ + (c & 3) * 12288 + (3 * wt + i) * 1024, wsrc[i], kc(c) * 64 * 3 * DM * 2);
    };
    X(0); W(0); X(1); W(1); X(2);
    QSTAMP(1);
    wait_bar<11>();
    STAMP(1);
    W(2); X(3); W(3);
    QSTAMP(2);
    wait_bar<14>();
    X(4); W(4);
    QSTAMP(3);
    wait_bar<14>();
    X(5); W(5);
    QSTAMP(4);
    wait_bar<14>();
    X(6); W(6);
    QSTAMP(5);
    wait_bar<14>();
    if (wt == 0 && lane < 8) *(LAS unsigned*)(lds + L_CTRL + lane * 4) = 0u;
    L(0, LEA);
    QSTAMP(6);
    ep = wait_bar_epoch<7 + LEA>(P.ws + FL_OFF);
    L(LEA, LEB);
    QSTAMP(7);
    wait_bar<LEB>();
    L(LEB, LE0);
    QSTAMP(8);
    bar_lds();
    __builtin_amdgcn_s_sleep(FDELAY);
  }
  STAMP(2);
  const u64 tag = (ep + 1) << 16;
  unsigned char* const wsb = P.ws + SET_BASE + (((unsigned)ep + 1u) & 3u) * SET_STRIDE;
  const __amdgpu_buffer_rsrc_t rsWs = mk_rsrc(wsb, WS_END);
  unsigned xoff[4];
#pragma unroll
  for (int ks = 0; ks < 4; ++ks) xoff[ks] = (unsigned)(L_X + r * 128 + (((2 * ks + h) ^ ((r >> 1) & 7)) << 4));
  if (ffn) {
    LAS unsigned char* ring = lds + L_W1 + wt * 8192;
    const unsigned troff = (unsigned)((8 * h + ((lane & 15) >> 2)) * 64 + ((lane >> 4) & 1) * 32 + (lane & 3) * 8);
#pragma unroll
    for (int i = 0; i < 4; ++i) acc[i] = f32x16{};
    RSTAMP(1);
    constexpr int LA = F1LA;
    bf16x8 xf[LA + 1][4], wf[LA + 1];
    auto wr = [&](int u) { *(LAS u32x4*)(ring + (u & 7) * 1024 + lo) = wl[u]; };
    auto rdw = [&](int u) { wf[u % (LA + 1)] = tr_frag(ring + (u & 7) * 1024 + troff, 4 * 64); };
    auto rdx = [&](int u, int t) { xf[u % (LA + 1)][t] = *(const LAS bf16x8*)(lds + xoff[u & 3] + (u >> 2) * 16384 + t * 4096); };
#pragma unroll
    for (int u = 0; u < LA + 2; ++u) wr(u);
#pragma unroll
    for (int u = 0; u < LA; ++u) {
      rdw(u);
#pragma unroll
      for (int t = 0; t < 4; ++t) rdx(u, t);
    }
    SBAR();
#pragma unroll
    for (int u = 0; u < 28; ++u) {
      if ((u & 3) == 0) RSTAMP(2 + (u >> 2));
#pragma unroll
      for (int t = 0; t < 4; ++t) {
        if (u + LA < 28) {
          if (t == 1) rdw(u + LA);
          rdx(u + LA, t);
          if (t == 3 && u + LA + 2 < 28) wr(u + LA + 2);
        }
        SBAR();
        acc[t] = MFMA(wf[u % (LA + 1)], xf[u % (LA + 1)][t], acc[t]);
        SBAR();
        if (u >= LU0 && LE0 + (u - LU0) * 4 + t < 28) L(LE0 + (u - LU0) * 4 + t, LE0 + (u - LU0) * 4 + t + 1);
      }
    }
    lds_arrive(lds + L_CTRL);
    RSTAMP(9);
    STAMP(3);
    __builtin_amdgcn_s_setprio(3);
#pragma unroll
    for (int t = 0; t < 4; ++t) {
      f32x16 z = acc[t];
#pragma unroll
      for (int i = 0; i < 16; ++i) z[i] = z[i] * __builtin_amdgcn_rcpf(1.f + __builtin_amdgcn_exp2f(-LOG2E * z[i]));
      const unsigned so = HF_OFF + ((rb * 4 + t) * 56 + j * 4 + wt) * 2048;
      st16(rsWs, as_u4(pack8(z, 0)), lo, so);
      st16(rsWs, as_u4(pack8(z, 1)), lo + 1024, so);
    }
    drain();
    if (lane == 0) put_granule(wsb, GH_OFF + (rb * 16 + j) * 128 + wt * 8, tag, 0);
  } else {
    __builtin_amdgcn_s_setprio(3);
    if (j == 14) {
      L(LE0, 28);
      acc[0] = f32x16{};
#pragma unroll
      for (int u = 0; u < 28; ++u) {
        const bf16x8 xf = *(const LAS bf16x8*)(lds + xoff[u & 3] + (u >> 2) * 16384 + wt * 4096);
        acc[0] = MFMA(as_frag(wl[u]), xf, acc[0]);
      }
      const float g = __builtin_amdgcn_rcpf(1.f + __builtin_amdgcn_exp2f(-LOG2E * acc[0][0]));
      if (h == 0) put_granule(wsb, GG_OFF + ((rb * 4 + wt) * 32 + r) * 8, tag, cvtpk(g, 0.f));
    }
    lds_arrive(lds + L_CTRL);
    const __amdgpu_buffer_rsrc_t rsO = mk_rsrc(P.wo, DM * DM * 2);
    const int id0 = ((j - 14) * 16 + rb) * 4 + wt;
    for (int id = id0; id < 224; id += 128) {
      const int nt = id >> 4, hd = id & 15;
      unsigned rp[16];
#pragma unroll
      for (int e = 0; e < 16; ++e)
        rp[e] = __builtin_amdgcn_raw_buffer_load_b16(rsO, (unsigned)(((4 * h + 8 * (e >> 2) + (e & 3)) * DM + r) * 2), (unsigned)((DH * hd * DM + 32 * nt) * 2), 0);
      u32x4 a, c2;
#pragma unroll
      for (int i = 0; i < 4; ++i) {
        a[i] = (rp[2 * i] & 0xffffu) | (rp[2 * i + 1] << 16);
        c2[i] = (rp[8 + 2 * i] & 0xffffu) | (rp[8 + 2 * i + 1] << 16);
      }
      c2[2] = h ? 0u : c2[2];
      c2[3] = h ? 0u : c2[3];
      st16(rsWs, a, lo, WOF_OFF + id * 2048);
      st16(rsWs, c2, lo + 1024, WOF_OFF + id * 2048);
      drain();
      if (lane == 0) put_granule(wsb, GW_OFF + id * 8, tag, 0);
    }
  }
  STAMP(4);
  if (idx >= 28) {
    if (b == 255 && wt == 0) {
      __builtin_amdgcn_s_sleep(127);
#pragma unroll
      for (int i = 0; i < 4; ++i) (void)poll_granule<64>(wsb, GQ_OFF + (i * 64 + lane) * GQW, tag);
      if (lane == 0) __hip_atomic_store((gu64*)(P.ws + FL_OFF), ep + 1, __ATOMIC_RELAXED, __HIP_MEMORY_SCOPE_AGENT);
    }
    STAMP(10);
    STAMP_FLUSH(P, 0);
    return;
  }
  const int mb = 4 * xcd + (idx & 3), nb = idx >> 2;
  const __amdgpu_buffer_rsrc_t rsW2 = mk_rsrc(P.w2, FF * DM * 2), rsH = rsWs;
  const unsigned w2src = (unsigned)((wt * 448 + (lane >> 3)) * (DM * 2) + nb * 128 + ((((lane >> 2) & 1) ^ ((lane >> 4) & 1)) * 64) + (lane & 3) * 16);
  const unsigned ring0 = lds0 + L_W2 + wt * 24576;
  const int rot2 = ROT2;
  auto fr = [&](int f) { const int v = f + rot2; return v >= 14 ? v - 14 : v; };
  auto D = [&](int f) {
#pragma unroll
    for (int i = 0; i < 4; ++i) glds16(rsW2, ring0 + (f % W2S) * 4096 + i * 1024, w2src, (fr(f) * 32 + i * 8) * DM * 2);
  };
  (void)wait_lds_word(lds + L_CTRL, 4);
#pragma unroll
  for (int f = 0; f < W2S; ++f) { D(f); __builtin_amdgcn_s_sleep(PACE); }
  const int ftl = wt * 14 + ((lane & 15) < 14 ? (lane & 15) : 0);
  (void)poll_granule<8>(wsb, (lane & 16) ? GW_OFF + ((nb * 2 + ((lane >> 2) & 1)) * 16 + wt * 4 + (lane & 3)) * 8
                                        : GH_OFF + ((mb >> 1) * 16 + (ftl >> 2)) * 128 + (ftl & 3) * 8, tag);
  STAMP(5);
  u32x4 hb[HPRE][4], wo[16];
  const unsigned hbase = HF_OFF + (mb * 2 * 56 + wt * 14) * 2048;
  auto H = [&](int f) {
#pragma unroll
    for (int e = 0; e < 4; ++e) hb[f % HPRE][e] = ld16c(rsH, lo + (e & 1) * 1024, hbase + ((e >> 1) * 56 + fr(f)) * 2048);
  };
#pragma unroll
  for (int f = 0; f < HPRE; ++f) { H(f); SBAR(); }
  const float gl = __uint_as_float(poll_granule<8>(wsb, GG_OFF + (mb * 64 + lane) * 8, tag) << 16), gs = swap_half(gl);
  const float og0 = 1.f - (h ? gs : gl), og1 = 1.f - (h ? gl : gs);
  const int q4 = (lane & 15) >> 2;
  unsigned woff[2];
#pragma unroll
  for (int nt = 0; nt < 2; ++nt) woff[nt] = (unsigned)(L_W2 + wt * 24576 + (4 * h + q4) * 128 + ((nt ^ (q4 >> 1)) * 64) + ((lane >> 4) & 1) * 32 + (lane & 3) * 8);
#pragma unroll
  for (int i = 0; i < 4; ++i) acc[i] = f32x16{};
  STAMP(6);
  __builtin_amdgcn_s_setprio(0);
  bf16x8 wf[2][2][2];
  auto RW = [&](int f) {
#pragma unroll
    for (int s = 0; s < 2; ++s)
#pragma unroll
      for (int nt = 0; nt < 2; ++nt) wf[f & 1][s][nt] = tr_frag(lds + woff[nt] + (f % W2S) * 4096 + s * 2048, 1024);
  };
  RW(0);
  static_for<0, 14>([&](auto fc) {
    constexpr int f = decltype(fc)::value;
    asm volatile("s_waitcnt lgkmcnt(0)" ::: "memory");
    SBAR();
    if constexpr (f + 1 < 14) {
      if constexpr (f + 1 >= W2S) wait_vm<ffn2_younger(f + 1, f)>();
      RW(f + 1);
    }
    SBAR();
#pragma unroll
    for (int s = 0; s < 2; ++s)
#pragma unroll
      for (int nt = 0; nt < 2; ++nt)
#pragma unroll
        for (int mt = 0; mt < 2; ++mt) {
          acc[nt * 2 + mt] = MFMA(wf[f & 1][s][nt], as_frag(hb[f % HPRE][mt * 2 + s]), acc[nt * 2 + mt]);
        }
    SBAR();
    if constexpr (f + W2S < 14) D(f + W2S);
    if constexpr (f + HPRE < 14) H(f + HPRE);
    else if constexpr (f + HPRE < 18) {
      constexpr int e0 = (f + HPRE - 14) * 4;
#pragma unroll
      for (int e = e0; e < e0 + 4; ++e) wo[e] = ld16c(rsWs, lo, WOF_OFF + ((nb * 2 + (e >> 3)) * 16 + wt * 4) * 2048 + (e & 7) * 1024);
    }
    SBAR();
    __builtin_amdgcn_s_sleep(PACE2);
    SBAR();
  });
  STAMP(7);
  __builtin_amdgcn_s_setprio(3);
#pragma unroll
  for (int i = 0; i < 16; ++i) { acc[0][i] *= og0; acc[1][i] *= og1; acc[2][i] *= og0; acc[3][i] *= og1; }
  (void)wait_lds_word(lds + L_CTRL + 20, 1);
  __builtin_amdgcn_s_sleep(ODELAY);
  (void)poll_granule<POLL_A>(wsb, GO_OFF + ((mb * 2 + ((lane >> 2) & 1)) * 16 + wt * 4 + (lane & 3)) * 128, tag);
  STAMP(8);
  u32x4 of[16];
#pragma unroll
  for (int e = 0; e < 8; ++e)
#pragma unroll
    for (int mt = 0; mt < 2; ++mt) of[mt * 8 + e] = ld16c(rsWs, lo, OF_OFF + ((mb * 2 + mt) * 16 + wt * 4) * 2048 + e * 1024);
  SBAR();
#pragma unroll
  for (int e = 0; e < 8; ++e)
#pragma unroll
    for (int nt = 0; nt < 2; ++nt)
#pragma unroll
      for (int mt = 0; mt < 2; ++mt) acc[nt * 2 + mt] = MFMA(as_frag(wo[nt * 8 + e]), as_frag(of[mt * 8 + e]), acc[nt * 2 + mt]);
#pragma unroll
  for (int t = 0; t < 4; ++t) st_tile(lds + L_W2 + wt * 24576 + t * 4096 + lo, acc[t]);
  STAMP(9);
  bar_lds();
  f32x16 a = f32x16{};
#pragma unroll
  for (int f = 0; f < 4; ++f) add_tile(a, lds + L_W2 + f * 24576 + wt * 4096 + lo);
  unsigned short* dst = P.out + (size_t)((mb * 2 + (wt & 1)) * 32 + r) * DM + (nb * 2 + (wt >> 1)) * 32 + 8 * h;
#pragma unroll
  for (int k = 0; k < 2; ++k) {
    const auto x = __builtin_amdgcn_permlane32_swap(cvtpk(a[8 * k], a[8 * k + 1]), cvtpk(a[8 * k + 4], a[8 * k + 5]), false, false);
    const auto y = __builtin_amdgcn_permlane32_swap(cvtpk(a[8 * k + 2], a[8 * k + 3]), cvtpk(a[8 * k + 6], a[8 * k + 7]), false, false);
    *(u32x4*)(dst + 16 * k) = u32x4{x[0], y[0], x[1], y[1]};
  }
  STAMP(10);
  STAMP_FLUSH(P, 0);
}

extern __shared__ __attribute__((aligned(16))) unsigned char g_lds[];

__global__ __launch_bounds__(512, 2) void k_fused(Params P) {
  LAS unsigned char* lds = (LAS unsigned char*)g_lds;
  const int w = __builtin_amdgcn_readfirstlane(threadIdx.x >> 6);
  if (w < 4) role_a(P, lds, w);
  else role_f(P, lds, w - 4);
}

extern "C" void kernel_launch(void* const* d_in, const int* in_sizes, int n_in, void* d_out, int out_size, void* d_ws,
                              size_t ws_size, hipStream_t stream) {
  Params P;
  P.x = (const unsigned short*)d_in[0];
  P.wqkv = (const unsigned short*)d_in[1];
  P.wo = (const unsigned short*)d_in[2];
  P.w1 = (const unsigned short*)d_in[4];
  P.w2 = (const unsigned short*)d_in[5];
  P.gw = (const unsigned short*)d_in[6];
  P.out = (unsigned short*)d_out;
  P.ws = (unsigned char*)d_ws;
  (void)hipFuncSetAttribute((const void*)k_fused, hipFuncAttributeMaxDynamicSharedMemorySize, LDS_BYTES);
  k_fused<<<256, 512, LDS_BYTES, stream>>>(P);
}
```

```cpp
#define XSTAMP 99
#define XWAVE 4
#include <hip/hip_runtime.h>
#include <hip/hip_bf16.h>
#include <stdint.h>

typedef short bf16x8 __attribute__((ext_vector_type(8)));
typedef short s16x4 __attribute__((ext_vector_type(4)));
typedef float f32x16 __attribute__((ext_vector_type(16)));
typedef float f32x4 __attribute__((ext_vector_type(4)));
typedef unsigned u32x4 __attribute__((ext_vector_type(4)));
typedef unsigned u32x2 __attribute__((ext_vector_type(2)));
#define LAS __attribute__((address_space(3)))
#define DI __device__ __forceinline__
#define MFMA(a, b, c) __builtin_amdgcn_mfma_f32_32x32x16_bf16((a), (b), (c), 0, 0, 0)
#define SBAR() __builtin_amdgcn_sched_barrier(0)

constexpr int SEQ = 2048, DM = 448, NH = 16, DH = 28, FF = 1792;
constexpr unsigned FL_OFF = 0;
#define SETB 1u
constexpr unsigned SET_BASE = SETB << 20, SET_STRIDE = 24u << 20;
#define GQS 16u
constexpr unsigned GQW = GQS == 16u ? 128u : 4u * GQS;
constexpr unsigned GQ_OFF = 0;
constexpr unsigned GH_OFF = GQ_OFF + 256 * GQW;
constexpr unsigned GO_OFF = GH_OFF + 256 * 128;
constexpr unsigned GW_OFF = GO_OFF + 1024 * 128;
constexpr unsigned GG_OFF = GW_OFF + 256 * 8;
constexpr unsigned QF_OFF = 524288;
constexpr unsigned KV_OFF = QF_OFF + 16 * 64 * 2048;
constexpr unsigned HF_OFF = KV_OFF + 16 * 64 * 4096;
constexpr unsigned WOF_OFF = HF_OFF + 64 * 56 * 2048;
constexpr unsigned OF_OFF = WOF_OFF + 14 * 16 * 2048;
constexpr unsigned WS_END = OF_OFF + 64 * 16 * 2048;
static_assert(GG_OFF + 2048 * 8 <= QF_OFF && WS_END <= SET_STRIDE, "workspace map");

constexpr int L_X = 0;
constexpr int L_WQ = 114688;
constexpr int L_W1 = 114688;
constexpr int L_CTRL = 163776;
constexpr int L_W2 = 0;
constexpr int L_CMB = 98304;
constexpr int LDS_BYTES = 163840;

constexpr float QSCALE = 0.18898223650461362f * 1.4426950408889634f;
constexpr float LOG2E = 1.4426950408889634f;
constexpr float SAFE_BOUND = 60.0f;
constexpr unsigned SPIN_CAP = 1u << 18;
#define ROT1(rb, j) 0
#define KVD 4
#define ROTA 0
#define ROT2 0
#define PDELAY 16
#define ODELAY 28
#define FDELAY 18
#define POLL_A 4

struct Params {
  const unsigned short* x;
  const unsigned short* wqkv;
  const unsigned short* wo;
  const unsigned short* w1;
  const unsigned short* w2;
  const unsigned short* gw;
  unsigned short* out;
  unsigned char* ws;
};
typedef unsigned long long u64;
typedef __attribute__((address_space(1))) u64 gu64;

DI unsigned cvtpk(float lo, float hi) {
  typedef __bf16 bf2 __attribute__((ext_vector_type(2)));
  typedef float f2 __attribute__((ext_vector_type(2)));
  f2 v = {lo, hi};
  bf2 r = __builtin_convertvector(v, bf2);
  return __builtin_bit_cast(unsigned, r);
}
DI bf16x8 pack8(const f32x16& x, int s) {
  u32x4 p;
  p[0] = cvtpk(x[8 * s + 0], x[8 * s + 1]);
  p[1] = cvtpk(x[8 * s + 2], x[8 * s + 3]);
  p[2] = cvtpk(x[8 * s + 4], x[8 * s + 5]);
  p[3] = cvtpk(x[8 * s + 6], x[8 * s + 7]);
  return __builtin_bit_cast(bf16x8, p);
}
DI float swap_half(float v) {
  auto r = __builtin_amdgcn_permlane32_swap(__float_as_uint(v), __float_as_uint(v), false, false);
  return (threadIdx.x & 32) ? __uint_as_float(r[0]) : __uint_as_float(r[1]);
}
template <int CTRL> DI float dpp_mov(float v) {
  return __uint_as_float(__builtin_amdgcn_update_dpp(__float_as_uint(v), __float_as_uint(v), CTRL, 0xf, 0xf, false));
}
DI float row16_max(float v) {
  v = fmaxf(v, dpp_mov<0x128>(v));
  v = fmaxf(v, dpp_mov<0x124>(v));
  v = fmaxf(v, dpp_mov<0x122>(v));
  v = fmaxf(v, dpp_mov<0x121>(v));
  return v;
}
DI unsigned row16_umax(unsigned v) {
  v = max(v, (unsigned)__builtin_amdgcn_update_dpp(v, v, 0x128, 0xf, 0xf, false));
  v = max(v, (unsigned)__builtin_amdgcn_update_dpp(v, v, 0x124, 0xf, 0xf, false));
  v = max(v, (unsigned)__builtin_amdgcn_update_dpp(v, v, 0x122, 0xf, 0xf, false));
  v = max(v, (unsigned)__builtin_amdgcn_update_dpp(v, v, 0x121, 0xf, 0xf, false));
  return v;
}
DI float rdlane(float v, int l) { return __uint_as_float(__builtin_amdgcn_readlane(__float_as_uint(v), l)); }
DI s16x4 tr_read(const LAS unsigned char* p) {
  typedef short v4i16_t __attribute__((ext_vector_type(4)));
  return __builtin_bit_cast(s16x4, __builtin_amdgcn_ds_read_tr16_b64_v4i16((LAS v4i16_t*)p));
}
DI bf16x8 tr_frag(const LAS unsigned char* p, int row_stride4) {
  s16x4 lo = tr_read(p), hi = tr_read(p + row_stride4);
  return __builtin_shufflevector(lo, hi, 0, 1, 2, 3, 4, 5, 6, 7);
}
DI __amdgpu_buffer_rsrc_t mk_rsrc(const void* p, unsigned bytes) {
  return __builtin_amdgcn_make_buffer_rsrc((void*)p, (short)0, (int)bytes, 0x00020000);
}
DI void glds16(__amdgpu_buffer_rsrc_t rs, unsigned dst, unsigned voff, unsigned soff) {
  asm volatile("s_mov_b32 m0, %0\n\ts_nop 0\n\tbuffer_load_dwordx4 %1, %2, %3 offen lds" ::"s"(dst), "v"(voff), "s"(rs), "s"(soff) : "memory");
}
DI u32x4 ld16(__amdgpu_buffer_rsrc_t rs, unsigned voff, unsigned soff) { return __builtin_amdgcn_raw_buffer_load_b128(rs, voff, soff, 0); }
DI u32x4 ld16c(__amdgpu_buffer_rsrc_t rs, unsigned voff, unsigned soff) { return __builtin_amdgcn_raw_buffer_load_b128(rs, voff, soff, 16); }
DI void st16(__amdgpu_buffer_rsrc_t rs, u32x4 v, unsigned voff, unsigned soff) { __builtin_amdgcn_raw_buffer_store_b128(v, rs, voff + soff, 0, 16); }
DI bf16x8 as_frag(u32x4 v) { return __builtin_bit_cast(bf16x8, v); }
DI u32x4 as_u4(bf16x8 v) { return __builtin_bit_cast(u32x4, v); }
template <int N> DI void wait_bar() { asm volatile("s_waitcnt vmcnt(%0) lgkmcnt(0)\n\ts_barrier" ::"n"(N) : "memory"); }

template <int V> struct IC { static constexpr int value = V; };
template <int I, int N, class Fn> DI void static_for(Fn&& fn) {
  if constexpr (I < N) { fn(IC<I>{}); static_for<I + 1, N>(fn); }
}
DI void drain() { asm volatile("s_waitcnt vmcnt(0)" ::: "memory"); }
template <int N> DI void wait_vm() { asm volatile("s_waitcnt vmcnt(%0)" ::"n"(N) : "memory"); }
DI void put_granule(unsigned char* ws, unsigned off, u64 tag16, unsigned payload) {
  __hip_atomic_store((gu64*)(ws + off), tag16 | (u64)(payload & 0xffffu), __ATOMIC_RELAXED, __HIP_MEMORY_SCOPE_AGENT);
}
template <int SLEEP> DI unsigned poll_granule(unsigned char* ws, unsigned off, u64 tag16) {
  u64 v = ~tag16;
  for (unsigned spins = 0;; ++spins) {
    if (v - tag16 > 0xffffull) v = __hip_atomic_load((gu64*)(ws + off), __ATOMIC_RELAXED, __HIP_MEMORY_SCOPE_AGENT);
    if (__all(v - tag16 <= 0xffffull) || spins > SPIN_CAP) break;
    __builtin_amdgcn_s_sleep(SLEEP);
  }
  __builtin_amdgcn_fence(__ATOMIC_ACQUIRE, "wavefront");
  return (unsigned)v & 0xffffu;
}
DI unsigned wait_lds_word(LAS unsigned char* p, unsigned want) {
  unsigned v = 0;
#pragma nounroll
  for (unsigned spins = 0; spins < (1u << 24); ++spins) {
    v = __builtin_amdgcn_readfirstlane(__hip_atomic_load((LAS unsigned*)p, __ATOMIC_RELAXED, __HIP_MEMORY_SCOPE_WORKGROUP));
    if (v >= want) break;
    __builtin_amdgcn_s_sleep(1);
  }
  asm volatile("" ::: "memory");
  return v;
}
template <int N> DI u64 wait_bar_epoch(const unsigned char* p) {
  u64 ep;
  asm volatile("s_load_dwordx2 %0, %1, 0x0\n\ts_waitcnt vmcnt(%2) lgkmcnt(0)\n\ts_barrier" : "=&s"(ep) : "s"(p), "n"(N) : "memory");
  return ep;
}
DI void bar_lds() { asm volatile("s_waitcnt lgkmcnt(0)\n\ts_barrier" ::: "memory"); }
DI void st_tile(LAS unsigned char* p, const f32x16& a) {
#pragma unroll
  for (int g = 0; g < 4; ++g) *(LAS f32x4*)(p + g * 1024) = f32x4{a[4 * g], a[4 * g + 1], a[4 * g + 2], a[4 * g + 3]};
}
DI void add_tile(f32x16& a, const LAS unsigned char* p) {
#pragma unroll
  for (int g = 0; g < 4; ++g) {
    const f32x4 v = *(const LAS f32x4*)(p + g * 1024);
    a[4 * g] += v[0]; a[4 * g + 1] += v[1]; a[4 * g + 2] += v[2]; a[4 * g + 3] += v[3];
  }
}

#define STAMP_DECL unsigned long long rt0_ = rte, rtx_ = rt0_
#define STAMP(id) do { if ((id) == XSTAMP) { SBAR(); asm volatile("s_memrealtime %0\n\ts_waitcnt lgkmcnt(0)" : "=s"(rtx_) :: "memory"); SBAR(); } } while (0)
#define QSTAMP(id) do {} while (0)
#define RSTAMP(id) do {} while (0)
#define STAMP_FLUSH(P, ph) do { if (blockIdx.x == 9 && __builtin_amdgcn_readfirstlane(threadIdx.x >> 6) == XWAVE) { const unsigned long long until_ = rt0_ + 5000 + 4 * (rtx_ - rt0_); \
  while (__builtin_amdgcn_s_memrealtime() < until_) __builtin_amdgcn_s_sleep(2); } } while (0)

DI void lds_arrive(LAS unsigned char* p) {
  asm volatile("s_mov_b64 exec, 1\n\tds_add_u32 %0, %1\n\ts_mov_b64 exec, -1" ::"v"((unsigned)(uintptr_t)p), "v"(1u) : "memory");
}

#define PINV(x) asm volatile("" : "+v"(x))
#define PINS(x) asm volatile("" : "+s"(x))
DI float sumsq(const u32x4& a, const u32x4& b) {
  typedef __bf16 bf2 __attribute__((ext_vector_type(2)));
  float n0 = 0.f, n1 = 0.f;
#pragma unroll
  for (int i = 0; i < 4; ++i) {
    n0 = __builtin_amdgcn_fdot2_f32_bf16(__builtin_bit_cast(bf2, a[i]), __builtin_bit_cast(bf2, a[i]), n0, false);
    n1 = __builtin_amdgcn_fdot2_f32_bf16(__builtin_bit_cast(bf2, b[i]), __builtin_bit_cast(bf2, b[i]), n1, false);
  }
  return n0 + n1;
}

DI void role_a(const Params& P, LAS unsigned char* lds, const int wt, unsigned long long rte) {
  const int lane = threadIdx.x & 63, r = lane & 31, h = lane >> 5;
  const int b = blockIdx.x, xcd = b & 7, idx = b >> 3;
  const unsigned lo = lane * 16;
  STAMP_DECL;
  STAMP(0);
  __builtin_amdgcn_s_setprio(2);
  f32x16 acc[4];
  const int rb = (xcd & 1) * 8 + (idx & 7), j = (xcd >> 1) * 4 + (idx >> 3);
  const int head = xcd * 2 + (idx & 1), qb = idx >> 1;
  unsigned sto[3];
#pragma unroll
  for (int ct = 0; ct < 3; ++ct) {
    const int u = 3 * j + ct, part = u >> 4, t = (u & 15) * 64 + rb * 4 + wt;
    sto[ct] = lo + (part == 0 ? QF_OFF + t * 2048 : KV_OFF + t * 4096 + (part - 1) * 2048);
    PINV(sto[ct]);
  }
  unsigned padv = r == DH ? 0x3f803f80u : 0u;
  PINV(padv);
  const int gq = head / 3, gk = (16 + head) / 3, gvv = (32 + head) / 3;
  unsigned pub_off = GQ_OFF + (j * 16 + rb) * GQW + wt * GQS + (lane & 1) * 8;
  unsigned poll_off = GQ_OFF + (h ? gq * 16 + qb : (lane < 16 ? gk : gvv) * 16 + wt * 4 + ((lane >> 2) & 3)) * GQW + (lane & 3) * GQS + h * 8;
  unsigned chk_off = GQ_OFF + (gk * 16 + (lane >> 2)) * GQW + (lane & 3) * GQS;
  unsigned g_off = GG_OFF + ((qb * 4 + wt) * 32 + r) * 8;
  unsigned o_off = lo + OF_OFF + ((qb * 4 + wt) * 16 + head) * 2048;
  PINV(pub_off); PINV(poll_off); PINV(chk_off); PINV(g_off); PINV(o_off);
  unsigned kvbase = KV_OFF + (head * 64 + wt * 16) * 4096, go_off = GO_OFF + ((qb * 4 + wt) * 16 + head) * 128;
  unsigned qofs[4];
#pragma unroll
  for (int t = 0; t < 4; ++t) { qofs[t] = QF_OFF + (unsigned)(head * 64 + qb * 4 + ((wt + 1 + t) & 3)) * 2048; PINS(qofs[t]); }
  PINS(kvbase); PINS(go_off);
  unsigned xoff[4];
#pragma unroll
  for (int ks = 0; ks < 4; ++ks) xoff[ks] = (unsigned)(L_X + wt * 4096 + r * 128 + (((2 * ks + h) ^ ((r >> 1) & 7)) << 4));
  const unsigned wqoff = (unsigned)(L_WQ + (8 * h + ((lane & 15) >> 2)) * 192 + ((lane >> 4) & 1) * 32 + (lane & 3) * 8);
  u64 ep;
  asm volatile("s_load_dwordx2 %0, %1, 0x0\n\ts_waitcnt lgkmcnt(0)" : "=&s"(ep) : "s"(P.ws + FL_OFF) : "memory");
  u64 tag16 = (ep + 1) << 16;
  unsigned char* wsb = P.ws + SET_BASE + (((unsigned)ep + 1u) & 3u) * SET_STRIDE;
  PINS(tag16); PINS(wsb);
  const __amdgpu_buffer_rsrc_t rsWs = mk_rsrc(wsb, WS_END);
#pragma unroll
  for (int i = 0; i < 3; ++i) acc[i] = f32x16{};
  auto kloop = [&](auto nvc) {
    constexpr int NV = decltype(nvc)::value;
    bf16x8 xf[4], wf[4][3];
    auto mm = [&](int ks, int ct) { acc[ct] = ct >= 3 - NV ? MFMA(xf[ks], wf[ks][ct], acc[ct]) : MFMA(wf[ks][ct], xf[ks], acc[ct]); };
    auto step = [&](int c, int ks, bool domm) {
      const int km = (ks + 2) & 3;
#pragma unroll
      for (int ct = 0; ct < 3; ++ct) {
        if (domm) mm(km, ct);
        SBAR();
        if (ct == 0) xf[ks] = *(const LAS bf16x8*)(lds + xoff[ks] + c * 16384);
        wf[ks][ct] = tr_frag(lds + wqoff + (c & 3) * 12288 + ks * 16 * 192 + ct * 56, 4 * 192);
        SBAR();
      }
    };
#pragma unroll
    for (int c = 0; c < 7; ++c) {
      bar_lds();
      if (c == 0) STAMP(1);
      QSTAMP(1 + c);
      step(c, 0, c > 0);
      step(c, 1, c > 0);
      step(c, 2, true);
      step(c, 3, true);
    }
    bar_lds();
    QSTAMP(8);
#pragma unroll
    for (int ct = 0; ct < 3; ++ct) { mm(2, ct); mm(3, ct); SBAR(); }
  };
  auto epi = [&](auto nqc, auto nvc) {
    constexpr int NQ = decltype(nqc)::value, NV = decltype(nvc)::value;
    STAMP(2);
    u32x4 t0[3], t1[3];
#pragma unroll
    for (int ct = 0; ct < 3; ++ct) {
      if (ct < NQ) acc[ct] *= QSCALE;
      t0[ct] = as_u4(pack8(acc[ct], 0));
      if (ct >= 3 - NV) {
#pragma unroll
        for (int i = 0; i < 4; ++i) t0[ct][i] = r < DH ? t0[ct][i] : padv;
      }
      st16(rsWs, t0[ct], sto[ct], 0);
      t1[ct] = as_u4(pack8(acc[ct], 1));
      if (ct >= 3 - NV) {
#pragma unroll
        for (int i = 0; i < 4; ++i) t1[ct][i] = r < DH ? t1[ct][i] : padv;
      } else {
        t1[ct][2] = h ? (ct < NQ ? 0u : 0x3f803f80u) : t1[ct][2];
        t1[ct][3] = h ? (ct < NQ ? 0u : 0x00003f80u) : t1[ct][3];
      }
      st16(rsWs, t1[ct], sto[ct], 1024);
      SBAR();
    }
    STAMP(8);
    unsigned mq = 0, mk = 0;
#pragma unroll
    for (int ct = 0; ct < 3; ++ct) {
      const unsigned n = __float_as_uint(sumsq(t0[ct], t1[ct])) & 0x7fffffffu;
      if (ct < NQ) mq = max(mq, n); else mk = max(mk, n);
    }
    auto bound = [&](unsigned m) {
      m = row16_umax(m);
      const float lo2 = __uint_as_float(max((unsigned)__builtin_amdgcn_readlane(m, 0), (unsigned)__builtin_amdgcn_readlane(m, 16)));
      const float hi2 = __uint_as_float(max((unsigned)__builtin_amdgcn_readlane(m, 32), (unsigned)__builtin_amdgcn_readlane(m, 48)));
      return ((__float_as_uint(lo2 + hi2) >> 16) + 1u) & 0xffffu;
    };
    const unsigned pq = NQ > 0 ? bound(mq) : 0u, pk = NQ < 3 ? bound(mk) : 0u;
    u64 gran = tag16 | ((lane & 1) ? pq : pk);
    PINV(gran);
    drain();
    STAMP(9);
    if (lane < 2) __hip_atomic_store((gu64*)(wsb + pub_off), gran, __ATOMIC_RELAXED, __HIP_MEMORY_SCOPE_AGENT);
  };
  if (j < 10) {
    kloop(IC<0>{});
    if (j < 5) epi(IC<3>{}, IC<0>{});
    else if (j == 5) epi(IC<1>{}, IC<0>{});
    else epi(IC<0>{}, IC<0>{});
  } else if (j == 10) {
    kloop(IC<1>{});
    epi(IC<0>{}, IC<1>{});
  } else {
    kloop(IC<3>{});
    epi(IC<0>{}, IC<3>{});
  }
  STAMP(3);
  __builtin_amdgcn_s_sleep(PDELAY);
  const unsigned pay = poll_granule<POLL_A>(wsb, poll_off, tag16);
  STAMP(4);
  u32x4 q[4][2];
  LAS unsigned char* const cmb = lds + L_CMB + wt * 12288 + lo;
  u64 gv = 0;
  unsigned thr = 0;
#pragma nounroll
  for (int pass = 0;; ++pass) {
    u32x4 kv[KVD][4];
    kv[0][0] = ld16c(rsWs, lo, kvbase);
    SBAR();
    if (pass == 0) { q[0][0] = ld16c(rsWs, lo, qofs[0]); q[0][1] = ld16c(rsWs, lo + 1024, qofs[0]); }
    SBAR();
    kv[0][1] = ld16c(rsWs, lo + 1024, kvbase);
    SBAR();
    if (pass == 0) {
#pragma unroll
      for (int t = 1; t < 4; ++t) { q[t][0] = ld16c(rsWs, lo, qofs[t]); q[t][1] = ld16c(rsWs, lo + 1024, qofs[t]); SBAR(); }
    }
#pragma unroll
    for (int e = 2; e < 4 * (KVD - 1); ++e) { kv[e >> 2][e & 3] = ld16c(rsWs, lo + (e & 3) * 1024, kvbase + (e >> 2) * 4096); SBAR(); }
#pragma unroll
    for (int i = 0; i < 4; ++i) acc[i] = f32x16{};
    {
      const float q2 = __uint_as_float(__builtin_amdgcn_readlane(row16_umax(pay), 32) << 16);
      const float lim = SAFE_BOUND * SAFE_BOUND * __builtin_amdgcn_rcpf(q2);
      thr = lim < 1e30f ? __float_as_uint(lim) >> 16 : (lim >= 1e30f ? 0x7000u : 0u);
      PINV(thr);
    }
    __builtin_amdgcn_s_setprio(1);
    f32x16 sc = MFMA(as_frag(kv[0][0]), as_frag(q[0][0]), f32x16{});
    sc = MFMA(as_frag(kv[0][1]), as_frag(q[0][1]), sc);
    STAMP(5);
    u32x4 p0, p1 = {0, 0, 0, 0};
    float e0 = 0.f, e1 = 0.f, e2 = 0.f, e3 = 0.f;
    u64 ck = 0, cv = 0;
#define EXP4(i) do { e0 = __builtin_amdgcn_exp2f(sc[i]); e1 = __builtin_amdgcn_exp2f(sc[(i) + 1]); e2 = __builtin_amdgcn_exp2f(sc[(i) + 2]); e3 = __builtin_amdgcn_exp2f(sc[(i) + 3]); \
    asm volatile("" : "+v"(e0), "+v"(e1), "+v"(e2), "+v"(e3)); SBAR(); } while (0)
#define CVT2(P, i) do { P[i] = cvtpk(e0, e1); P[(i) + 1] = cvtpk(e2, e3); asm volatile("" : "+v"(P)); SBAR(); } while (0)
#define PART(t, g) do { *(LAS f32x4*)(cmb + (t) * 4096 + (g) * 1024) = f32x4{acc[t][4 * (g)], acc[t][4 * (g) + 1], acc[t][4 * (g) + 2], acc[t][4 * (g) + 3]}; SBAR(); } while (0)
    static_for<0, 64>([&](auto ic) {
      constexpr int i = decltype(ic)::value;
      constexpr int kt = i >> 2, t = i & 3, kn_ = (i + 1) >> 2, tn = (i + 1) & 3, kp = (i - 1) >> 2, tp = (i - 1) & 3;
      constexpr int kl = kt + KVD - 1;
      if (i == 24) STAMP(11);
      if (i == 56) (void)wait_lds_word(lds + L_CTRL, 4);
      f32x16 sn;
      SBAR();
      if (i < 63) sn = MFMA(as_frag(kv[kn_ % KVD][0]), as_frag(q[tn][0]), f32x16{});
      SBAR();
      if constexpr (i >= 62) PART(i - 62, 0);
      if (i > 0) CVT2(p1, 2);
      EXP4(0);
      if (i > 0) acc[tp] = MFMA(as_frag(kv[(kp + KVD) % KVD][3]), as_frag(p1), acc[tp]);
      SBAR();
      if (kl < 16) kv[kl % KVD][t] = ld16c(rsWs, lo + t * 1024, kvbase + kl * 4096);
      if (i == 52) gv = __hip_atomic_load((gu64*)(wsb + g_off), __ATOMIC_RELAXED, __HIP_MEMORY_SCOPE_AGENT);
      if (i == 53) ck = __hip_atomic_load((gu64*)(wsb + chk_off), __ATOMIC_RELAXED, __HIP_MEMORY_SCOPE_AGENT);
      if (i == 54) cv = __hip_atomic_load((gu64*)(wsb + (gvv - gk) * 16 * GQW + chk_off), __ATOMIC_RELAXED, __HIP_MEMORY_SCOPE_AGENT);
      SBAR();
      if constexpr (i >= 62) PART(i - 62, 1);
      CVT2(p0, 0);
      EXP4(4);
      if (i < 63) sn = MFMA(as_frag(kv[kn_ % KVD][1]), as_frag(q[tn][1]), sn);
      SBAR();
      if constexpr (i >= 62) PART(i - 62, 2);
      CVT2(p0, 2);
      EXP4(8);
      acc[t] = MFMA(as_frag(kv[kt % KVD][2]), as_frag(p0), acc[t]);
      SBAR();
      if constexpr (i >= 62) PART(i - 62, 3);
      CVT2(p1, 0);
      EXP4(12);
      if (i < 63) sc = sn;
    });
    CVT2(p1, 2);
    acc[3] = MFMA(as_frag(kv[15 % KVD][3]), as_frag(p1), acc[3]);
    SBAR();
    PART(2, 0); PART(2, 1); PART(2, 2); PART(2, 3);
#undef EXP4
#undef CVT2
#undef PART
    __builtin_amdgcn_s_setprio(2);
    if (__builtin_expect(__all(ck - tag16 <= (u64)thr && cv - tag16 <= 0x7000ull && gv - tag16 <= 0xffffull), 1)) break;
    gv = poll_granule<2>(wsb, g_off, tag16);
    if (pass) break;
    const unsigned pk = poll_granule<2>(wsb, chk_off, tag16), pv = poll_granule<2>(wsb, (gvv - gk) * 16 * GQW + chk_off, tag16);
    if (__all(pk <= thr && pv <= 0x7000u)) break;
    float mx[4];
#pragma unroll
    for (int t = 0; t < 4; ++t) mx[t] = -INFINITY;
    for (int kt = 0; kt < 64; ++kt) {
      const u32x4 k0 = ld16c(rsWs, lo, KV_OFF + (head * 64 + kt) * 4096), k1 = ld16c(rsWs, lo + 1024, KV_OFF + (head * 64 + kt) * 4096);
#pragma unroll
      for (int t = 0; t < 4; ++t) {
        f32x16 s = MFMA(as_frag(k0), as_frag(q[t][0]), f32x16{});
        s = MFMA(as_frag(k1), as_frag(q[t][1]), s);
#pragma unroll
        for (int i = 0; i < 16; ++i) mx[t] = fmaxf(mx[t], s[i]);
      }
    }
#pragma unroll
    for (int t = 0; t < 4; ++t) {
      const float nm = -fmaxf(mx[t], swap_half(mx[t]));
      const unsigned hi = __float_as_uint(nm) & 0xffff0000u;
      const float r1 = nm - __uint_as_float(hi);
      const unsigned mid = __float_as_uint(r1) & 0xffff0000u;
      const float r2 = r1 - __uint_as_float(mid);
      if (h) { q[t][1][2] = (hi >> 16) | mid; q[t][1][3] = __float_as_uint(r2) >> 16; }
    }
  }
  STAMP(6);
  asm volatile("s_waitcnt lgkmcnt(0)" ::: "memory");
  lds_arrive(lds + L_CTRL + 20);
  (void)wait_lds_word(lds + L_CTRL + 20, 4);
  f32x16 o = acc[3];
#pragma unroll
  for (int u = 0; u < 3; ++u) add_tile(o, lds + L_CMB + ((wt + 3 - u) & 3) * 12288 + u * 4096 + lo);
  const auto lsw = __builtin_amdgcn_permlane32_swap(__float_as_uint(o[12]), __float_as_uint(o[12]), false, false);
  const float inv = __uint_as_float((unsigned)gv << 16) * __builtin_amdgcn_rcpf(__uint_as_float(lsw[1]));
  o *= inv;
  st16(rsWs, as_u4(pack8(o, 0)), o_off, 0);
  u32x4 o1 = as_u4(pack8(o, 1));
  o1[2] = h ? 0u : o1[2];
  o1[3] = h ? 0u : o1[3];
  st16(rsWs, o1, o_off, 1024);
  STAMP(7);
  drain();
  if (lane == 0) __hip_atomic_store((gu64*)(wsb + go_off), tag16, __ATOMIC_RELAXED, __HIP_MEMORY_SCOPE_AGENT);
  STAMP(10);
  STAMP_FLUSH(P, 0);
}

constexpr int W2S = 6;
constexpr int HPRE = 5;
#define LEA 5
#define LEB 11
#define LE0 16
#define LU0 3
#define F1LA 2
#define PACE 4
#define PACE2 2
constexpr int ffn2_ops(int f) { return (f + W2S < 14 ? 4 : 0) + 4; }
constexpr int ffn2_younger(int f, int upto) {
  int n = 4;
  for (int i = f - W2S + 1; i < upto; ++i) n += ffn2_ops(i);
  return n;
}

DI void role_f(const Params& P, LAS unsigned char* lds, const int wt, unsigned long long rte) {
  u64 ep;
  const int lane = threadIdx.x & 63, r = lane & 31, h = lane >> 5;
  const int b = blockIdx.x, xcd = b & 7, idx = b >> 3;
  const int rb = (xcd & 1) * 8 + (idx & 7), j = (xcd >> 1) * 4 + (idx >> 3);
  const unsigned lo = lane * 16, lds0 = (unsigned)(uintptr_t)lds;
  const bool ffn = j < 14;
  STAMP_DECL;
  STAMP(0);
  f32x16 acc[4];
  u32x4 wl[28];
  const __amdgpu_buffer_rsrc_t rsL = mk_rsrc(ffn ? P.w1 : P.gw, ffn ? DM * FF * 2 : DM * 2);
  const unsigned lsrc = ffn ? (unsigned)((lane >> 2) * (FF * 2) + (j * 4 + wt) * 64 + (lane & 3) * 16) : (unsigned)(h * 16);
  const unsigned lstep = ffn ? 16 * FF * 2 : 32;
  const int rot = ROT1(rb, j);
  auto kc = [&](int c) { const int v = c + rot; return v >= 7 ? v - 7 : v; };
  auto L = [&](int u0, int u1) {
#pragma unroll
    for (int u = u0; u < u1; ++u) wl[u] = ld16(rsL, lsrc, (kc(u >> 2) * 4 + (u & 3)) * lstep);
    SBAR();
  };
  {
    const __amdgpu_buffer_rsrc_t rsX = mk_rsrc(P.x, SEQ * DM * 2);
    const __amdgpu_buffer_rsrc_t rsW = mk_rsrc(P.wqkv, DM * 3 * DM * 2);
    unsigned xsrc[2], wsrc[3];
#pragma unroll
    for (int i = 0; i < 2; ++i) {
      const int row = 32 * wt + 8 * i + (lane >> 3), ch = (lane & 7) ^ ((row >> 1) & 7);
      xsrc[i] = (unsigned)((rb * 128 + row) * (DM * 2) + ch * 16);
    }
#pragma unroll
    for (int i = 0; i < 3; ++i) {
      const int p = 64 * (3 * wt + i) + lane, kr = p / 12, ch = p % 12;
      wsrc[i] = (unsigned)(kr * (3 * DM * 2) + j * 168 + (ch < 11 ? ch : 10) * 16);
    }
    auto X = [&](int c) {
#pragma unroll
      for (int i = 0; i < 4; ++i) glds16(rsX, lds0 + L_X + c * 16384 + (4 * wt + i) * 1024, xsrc[i & 1], kc(c) * 128 + (i >> 1) * 16 * DM * 2);
    };
    auto W = [&](int c) {
#pragma unroll
      for (int i = 0; i < 3; ++i) glds16(rsW, lds0 + L_WQ + (c & 3) * 12288 + (3 * wt + i) * 1024, wsrc[i], kc(c) * 64 * 3 * DM * 2);
    };
    X(0); W(0); X(1); W(1); X(2);
    QSTAMP(1);
    wait_bar<11>();
    STAMP(1);
    W(2); X(3); W(3);
    QSTAMP(2);
    wait_bar<14>();
    X(4); W(4);
    QSTAMP(3);
    wait_bar<14>();
    X(5); W(5);
    QSTAMP(4);
    wait_bar<14>();
    X(6); W(6);
    QSTAMP(5);
    wait_bar<14>();
    if (wt == 0 && lane < 8) *(LAS unsigned*)(lds + L_CTRL + lane * 4) = 0u;
    L(0, LEA);
    QSTAMP(6);
    ep = wait_bar_epoch<7 + LEA>(P.ws + FL_OFF);
    L(LEA, LEB);
    QSTAMP(7);
    wait_bar<LEB>();
    L(LEB, LE0);
    QSTAMP(8);
    bar_lds();
    __builtin_amdgcn_s_sleep(FDELAY);
  }
  STAMP(2);
  const u64 tag = (ep + 1) << 16;
  unsigned char* const wsb = P.ws + SET_BASE + (((unsigned)ep + 1u) & 3u) * SET_STRIDE;
  const __amdgpu_buffer_rsrc_t rsWs = mk_rsrc(wsb, WS_END);
  unsigned xoff[4];
#pragma unroll
  for (int ks = 0; ks < 4; ++ks) xoff[ks] = (unsigned)(L_X + r * 128 + (((2 * ks + h) ^ ((r >> 1) & 7)) << 4));
  if (ffn) {
    LAS unsigned char* ring = lds + L_W1 + wt * 8192;
    const unsigned troff = (unsigned)((8 * h + ((lane & 15) >> 2)) * 64 + ((lane >> 4) & 1) * 32 + (lane & 3) * 8);
#pragma unroll
    for (int i = 0; i < 4; ++i) acc[i] = f32x16{};
    RSTAMP(1);
    constexpr int LA = F1LA;
    bf16x8 xf[LA + 1][4], wf[LA + 1];
    auto wr = [&](int u) { *(LAS u32x4*)(ring + (u & 7) * 1024 + lo) = wl[u]; };
    auto rdw = [&](int u) { wf[u % (LA + 1)] = tr_frag(ring + (u & 7) * 1024 + troff, 4 * 64); };
    auto rdx = [&](int u, int t) { xf[u % (LA + 1)][t] = *(const LAS bf16x8*)(lds + xoff[u & 3] + (u >> 2) * 16384 + t * 4096); };
#pragma unroll
    for (int u = 0; u < LA + 2; ++u) wr(u);
#pragma unroll
    for (int u = 0; u < LA; ++u) {
      rdw(u);
#pragma unroll
      for (int t = 0; t < 4; ++t) rdx(u, t);
    }
    SBAR();
#pragma unroll
    for (int u = 0; u < 28; ++u) {
      if ((u & 3) == 0) RSTAMP(2 + (u >> 2));
#pragma unroll
      for (int t = 0; t < 4; ++t) {
        if (u + LA < 28) {
          if (t == 1) rdw(u + LA);
          rdx(u + LA, t);
          if (t == 3 && u + LA + 2 < 28) wr(u + LA + 2);
        }
        SBAR();
        acc[t] = MFMA(wf[u % (LA + 1)], xf[u % (LA + 1)][t], acc[t]);
        SBAR();
        if (u >= LU0 && LE0 + (u - LU0) * 4 + t < 28) L(LE0 + (u - LU0) * 4 + t, LE0 + (u - LU0) * 4 + t + 1);
      }
    }
    lds_arrive(lds + L_CTRL);
    RSTAMP(9);
    STAMP(3);
    __builtin_amdgcn_s_setprio(3);
#pragma unroll
    for (int t = 0; t < 4; ++t) {
      f32x16 z = acc[t];
#pragma unroll
      for (int i = 0; i < 16; ++i) z[i] = z[i] * __builtin_amdgcn_rcpf(1.f + __builtin_amdgcn_exp2f(-LOG2E * z[i]));
      const unsigned so = HF_OFF + ((rb * 4 + t) * 56 + j * 4 + wt) * 2048;
      st16(rsWs, as_u4(pack8(z, 0)), lo, so);
      st16(rsWs, as_u4(pack8(z, 1)), lo + 1024, so);
    }
    drain();
    if (lane == 0) put_granule(wsb, GH_OFF + (rb * 16 + j) * 128 + wt * 8, tag, 0);
  } else {
    __builtin_amdgcn_s_setprio(3);
    if (j == 14) {
      L(LE0, 28);
      acc[0] = f32x16{};
#pragma unroll
      for (int u = 0; u < 28; ++u) {
        const bf16x8 xf = *(const LAS bf16x8*)(lds + xoff[u & 3] + (u >> 2) * 16384 + wt * 4096);
        acc[0] = MFMA(as_frag(wl[u]), xf, acc[0]);
      }
      const float g = __builtin_amdgcn_rcpf(1.f + __builtin_amdgcn_exp2f(-LOG2E * acc[0][0]));
      if (h == 0) put_granule(wsb, GG_OFF + ((rb * 4 + wt) * 32 + r) * 8, tag, cvtpk(g, 0.f));
    }
    lds_arrive(lds + L_CTRL);
    const __amdgpu_buffer_rsrc_t rsO = mk_rsrc(P.wo, DM * DM * 2);
    const int id0 = ((j - 14) * 16 + rb) * 4 + wt;
    for (int id = id0; id < 224; id += 128) {
      const int nt = id >> 4, hd = id & 15;
      unsigned rp[16];
#pragma unroll
      for (int e = 0; e < 16; ++e)
        rp[e] = __builtin_amdgcn_raw_buffer_load_b16(rsO, (unsigned)(((4 * h + 8 * (e >> 2) + (e & 3)) * DM + r) * 2), (unsigned)((DH * hd * DM + 32 * nt) * 2), 0);
      u32x4 a, c2;
#pragma unroll
      for (int i = 0; i < 4; ++i) {
        a[i] = (rp[2 * i] & 0xffffu) | (rp[2 * i + 1] << 16);
        c2[i] = (rp[8 + 2 * i] & 0xffffu) | (rp[8 + 2 * i + 1] << 16);
      }
      c2[2] = h ? 0u : c2[2];
      c2[3] = h ? 0u : c2[3];
      st16(rsWs, a, lo, WOF_OFF + id * 2048);
      st16(rsWs, c2, lo + 1024, WOF_OFF + id * 2048);
      drain();
      if (lane == 0) put_granule(wsb, GW_OFF + id * 8, tag, 0);
    }
  }
  STAMP(4);
  if (idx >= 28) {
    if (b == 255 && wt == 0) {
      __builtin_amdgcn_s_sleep(127);
#pragma unroll
      for (int i = 0; i < 4; ++i) (void)poll_granule<64>(wsb, GQ_OFF + (i * 64 + lane) * GQW, tag);
      if (lane == 0) __hip_atomic_store((gu64*)(P.ws + FL_OFF), ep + 1, __ATOMIC_RELAXED, __HIP_MEMORY_SCOPE_AGENT);
    }
    STAMP(10);
    STAMP_FLUSH(P, 0);
    return;
  }
  const int mb = 4 * xcd + (idx & 3), nb = idx >> 2;
  const __amdgpu_buffer_rsrc_t rsW2 = mk_rsrc(P.w2, FF * DM * 2), rsH = rsWs;
  const unsigned w2src = (unsigned)((wt * 448 + (lane >> 3)) * (DM * 2) + nb * 128 + ((((lane >> 2) & 1) ^ ((lane >> 4) & 1)) * 64) + (lane & 3) * 16);
  const unsigned ring0 = lds0 + L_W2 + wt * 24576;
  const int rot2 = ROT2;
  auto fr = [&](int f) { const int v = f + rot2; return v >= 14 ? v - 14 : v; };
  auto D = [&](int f) {
#pragma unroll
    for (int i = 0; i < 4; ++i) glds16(rsW2, ring0 + (f % W2S) * 4096 + i * 1024, w2src, (fr(f) * 32 + i * 8) * DM * 2);
  };
  (void)wait_lds_word(lds + L_CTRL, 4);
#pragma unroll
  for (int f = 0; f < W2S; ++f) { D(f); __builtin_amdgcn_s_sleep(PACE); }
  const int ftl = wt * 14 + ((lane & 15) < 14 ? (lane & 15) : 0);
  (void)poll_granule<8>(wsb, (lane & 16) ? GW_OFF + ((nb * 2 + ((lane >> 2) & 1)) * 16 + wt * 4 + (lane & 3)) * 8
                                        : GH_OFF + ((mb >> 1) * 16 + (ftl >> 2)) * 128 + (ftl & 3) * 8, tag);
  STAMP(5);
  u32x4 hb[HPRE][4], wo[16];
  const unsigned hbase = HF_OFF + (mb * 2 * 56 + wt * 14) * 2048;
  auto H = [&](int f) {
#pragma unroll
    for (int e = 0; e < 4; ++e) hb[f % HPRE][e] = ld16c(rsH, lo + (e & 1) * 1024, hbase + ((e >> 1) * 56 + fr(f)) * 2048);
  };
#pragma unroll
  for (int f = 0; f < HPRE; ++f) { H(f); SBAR(); }
  const float gl = __uint_as_float(poll_granule<8>(wsb, GG_OFF + (mb * 64 + lane) * 8, tag) << 16), gs = swap_half(gl);
  const float og0 = 1.f - (h ? gs : gl), og1 = 1.f - (h ? gl : gs);
  const int q4 = (lane & 15) >> 2;
  unsigned woff[2];
#pragma unroll
  for (int nt = 0; nt < 2; ++nt) woff[nt] = (unsigned)(L_W2 + wt * 24576 + (4 * h + q4) * 128 + ((nt ^ (q4 >> 1)) * 64) + ((lane >> 4) & 1) * 32 + (lane & 3) * 8);
#pragma unroll
  for (int i = 0; i < 4; ++i) acc[i] = f32x16{};
  STAMP(6);
  __builtin_amdgcn_s_setprio(0);
  bf16x8 wf[2][2][2];
  auto RW = [&](int f) {
#pragma unroll
    for (int s = 0; s < 2; ++s)
#pragma unroll
      for (int nt = 0; nt < 2; ++nt) wf[f & 1][s][nt] = tr_frag(lds + woff[nt] + (f % W2S) * 4096 + s * 2048, 1024);
  };
  RW(0);
  static_for<0, 14>([&](auto fc) {
    constexpr int f = decltype(fc)::value;
    asm volatile("s_waitcnt lgkmcnt(0)" ::: "memory");
    SBAR();
    if constexpr (f + 1 < 14) {
      if constexpr (f + 1 >= W2S) wait_vm<ffn2_younger(f + 1, f)>();
      RW(f + 1);
    }
    SBAR();
#pragma unroll
    for (int s = 0; s < 2; ++s)
#pragma unroll
      for (int nt = 0; nt < 2; ++nt)
#pragma unroll
        for (int mt = 0; mt < 2; ++mt) {
          acc[nt * 2 + mt] = MFMA(wf[f & 1][s][nt], as_frag(hb[f % HPRE][mt * 2 + s]), acc[nt * 2 + mt]);
        }
    SBAR();
    if constexpr (f + W2S < 14) D(f + W2S);
    if constexpr (f + HPRE < 14) H(f + HPRE);
    else if constexpr (f + HPRE < 18) {
      constexpr int e0 = (f + HPRE - 14) * 4;
#pragma unroll
      for (int e = e0; e < e0 + 4; ++e) wo[e] = ld16c(rsWs, lo, WOF_OFF + ((nb * 2 + (e >> 3)) * 16 + wt * 4) * 2048 + (e & 7) * 1024);
    }
    SBAR();
    __builtin_amdgcn_s_sleep(PACE2);
    SBAR();
  });
  STAMP(7);
  __builtin_amdgcn_s_setprio(3);
#pragma unroll
  for (int i = 0; i < 16; ++i) { acc[0][i] *= og0; acc[1][i] *= og1; acc[2][i] *= og0; acc[3][i] *= og1; }
  (void)wait_lds_word(lds + L_CTRL + 20, 1);
  __builtin_amdgcn_s_sleep(ODELAY);
  (void)poll_granule<POLL_A>(wsb, GO_OFF + ((mb * 2 + ((lane >> 2) & 1)) * 16 + wt * 4 + (lane & 3)) * 128, tag);
  STAMP(8);
  u32x4 of[16];
#pragma unroll
  for (int e = 0; e < 8; ++e)
#pragma unroll
    for (int mt = 0; mt < 2; ++mt) of[mt * 8 + e] = ld16c(rsWs, lo, OF_OFF + ((mb * 2 + mt) * 16 + wt * 4) * 2048 + e * 1024);
  SBAR();
#pragma unroll
  for (int e = 0; e < 8; ++e)
#pragma unroll
    for (int nt = 0; nt < 2; ++nt)
#pragma unroll
      for (int mt = 0; mt < 2; ++mt) acc[nt * 2 + mt] = MFMA(as_frag(wo[nt * 8 + e]), as_frag(of[mt * 8 + e]), acc[nt * 2 + mt]);
#pragma unroll
  for (int t = 0; t < 4; ++t) st_tile(lds + L_W2 + wt * 24576 + t * 4096 + lo, acc[t]);
  STAMP(9);
  bar_lds();
  f32x16 a = f32x16{};
#pragma unroll
  for (int f = 0; f < 4; ++f) add_tile(a, lds + L_W2 + f * 24576 + wt * 4096 + lo);
  unsigned short* dst = P.out + (size_t)((mb * 2 + (wt & 1)) * 32 + r) * DM + (nb * 2 + (wt >> 1)) * 32 + 8 * h;
#pragma unroll
  for (int k = 0; k < 2; ++k) {
    const auto x = __builtin_amdgcn_permlane32_swap(cvtpk(a[8 * k], a[8 * k + 1]), cvtpk(a[8 * k + 4], a[8 * k + 5]), false, false);
    const auto y = __builtin_amdgcn_permlane32_swap(cvtpk(a[8 * k + 2], a[8 * k + 3]), cvtpk(a[8 * k + 6], a[8 * k + 7]), false, false);
    *(u32x4*)(dst + 16 * k) = u32x4{x[0], y[0], x[1], y[1]};
  }
  STAMP(10);
  STAMP_FLUSH(P, 0);
}

extern __shared__ __attribute__((aligned(16))) unsigned char g_lds[];

__global__ __launch_bounds__(512, 2) void k_fused(Params P) {
  unsigned long long rte; asm volatile("s_memrealtime %0\n\ts_waitcnt lgkmcnt(0)" : "=s"(rte) :: "memory");
  LAS unsigned char* lds = (LAS unsigned char*)g_lds;
  const int w = __builtin_amdgcn_readfirstlane(threadIdx.x >> 6);
  if (w < 4) role_a(P, lds, w, rte);
  else role_f(P, lds, w - 4, rte);
}

extern "C" void kernel_launch(void* const* d_in, const int* in_sizes, int n_in, void* d_out, int out_size, void* d_ws,
                              size_t ws_size, hipStream_t stream) {
  Params P;
  P.x = (const unsigned short*)d_in[0];
  P.wqkv = (const unsigned short*)d_in[1];
  P.wo = (const unsigned short*)d_in[2];
  P.w1 = (const unsigned short*)d_in[4];
  P.w2 = (const unsigned short*)d_in[5];
  P.gw = (const unsigned short*)d_in[6];
  P.out = (unsigned short*)d_out;
  P.ws = (unsigned char*)d_ws;
  (void)hipFuncSetAttribute((const void*)k_fused, hipFuncAttributeMaxDynamicSharedMemorySize, LDS_BYTES);
  k_fused<<<256, 512, LDS_BYTES, stream>>>(P);
}
```

```cpp
#define MPF 0
#include <hip/hip_runtime.h>
#include <hip/hip_bf16.h>
#include <stdint.h>

typedef short bf16x8 __attribute__((ext_vector_type(8)));
typedef short s16x4 __attribute__((ext_vector_type(4)));
typedef float f32x16 __attribute__((ext_vector_type(16)));
typedef float f32x4 __attribute__((ext_vector_type(4)));
typedef unsigned u32x4 __attribute__((ext_vector_type(4)));
typedef unsigned u32x2 __attribute__((ext_vector_type(2)));
#define LAS __attribute__((address_space(3)))
#define DI __device__ __forceinline__
#define MFMA(a, b, c) __builtin_amdgcn_mfma_f32_32x32x16_bf16((a), (b), (c), 0, 0, 0)
#define SBAR() __builtin_amdgcn_sched_barrier(0)

constexpr int SEQ = 2048, DM = 448, NH = 16, DH = 28, FF = 1792;
constexpr unsigned FL_OFF = 0;
#define SETB 1u
constexpr unsigned SET_BASE = SETB << 20, SET_STRIDE = 24u << 20;
#define GQS 16u
constexpr unsigned GQW = GQS == 16u ? 128u : 4u * GQS;
constexpr unsigned GQ_OFF = 0;
constexpr unsigned GH_OFF = GQ_OFF + 256 * GQW;
constexpr unsigned GO_OFF = GH_OFF + 256 * 128;
constexpr unsigned GW_OFF = GO_OFF + 1024 * 128;
constexpr unsigned GG_OFF = GW_OFF + 256 * 8;
constexpr unsigned QF_OFF = 524288;
constexpr unsigned KV_OFF = QF_OFF + 16 * 64 * 2048;
constexpr unsigned HF_OFF = KV_OFF + 16 * 64 * 4096;
constexpr unsigned WOF_OFF = HF_OFF + 64 * 56 * 2048;
constexpr unsigned OF_OFF = WOF_OFF + 14 * 16 * 2048;
constexpr unsigned WS_END = OF_OFF + 64 * 16 * 2048;
static_assert(GG_OFF + 2048 * 8 <= QF_OFF && WS_END <= SET_STRIDE, "workspace map");

constexpr int L_X = 0;
constexpr int L_WQ = 114688;
constexpr int L_W1 = 114688;
constexpr int L_CTRL = 163776;
constexpr int L_W2 = 0;
constexpr int L_CMB = 98304;
constexpr int LDS_BYTES = 163840;

constexpr float QSCALE = 0.18898223650461362f * 1.4426950408889634f;
constexpr float LOG2E = 1.4426950408889634f;
constexpr float SAFE_BOUND = 60.0f;
constexpr unsigned SPIN_CAP = 1u << 18;
#define ROT1(rb, j) 0
#define KVD 4
#define ROTA 0
#define ROT2 0
#define PDELAY 16
#define ODELAY 28
#define FDELAY 18
#define CODE_PF 44032
#define POLL_A 4

struct Params {
  const unsigned short* x;
  const unsigned short* wqkv;
  const unsigned short* wo;
  const unsigned short* w1;
  const unsigned short* w2;
  const unsigned short* gw;
  unsigned short* out;
  unsigned char* ws;
};
typedef unsigned long long u64;
typedef __attribute__((address_space(1))) u64 gu64;

DI unsigned cvtpk(float lo, float hi) {
  typedef __bf16 bf2 __attribute__((ext_vector_type(2)));
  typedef float f2 __attribute__((ext_vector_type(2)));
  f2 v = {lo, hi};
  bf2 r = __builtin_convertvector(v, bf2);
  return __builtin_bit_cast(unsigned, r);
}
DI bf16x8 pack8(const f32x16& x, int s) {
  u32x4 p;
  p[0] = cvtpk(x[8 * s + 0], x[8 * s + 1]);
  p[1] = cvtpk(x[8 * s + 2], x[8 * s + 3]);
  p[2] = cvtpk(x[8 * s + 4], x[8 * s + 5]);
  p[3] = cvtpk(x[8 * s + 6], x[8 * s + 7]);
  return __builtin_bit_cast(bf16x8, p);
}
DI float swap_half(float v) {
  auto r = __builtin_amdgcn_permlane32_swap(__float_as_uint(v), __float_as_uint(v), false, false);
  return (threadIdx.x & 32) ? __uint_as_float(r[0]) : __uint_as_float(r[1]);
}
template <int CTRL> DI float dpp_mov(float v) {
  return __uint_as_float(__builtin_amdgcn_update_dpp(__float_as_uint(v), __float_as_uint(v), CTRL, 0xf, 0xf, false));
}
DI float row16_max(float v) {
  v = fmaxf(v, dpp_mov<0x128>(v));
  v = fmaxf(v, dpp_mov<0x124>(v));
  v = fmaxf(v, dpp_mov<0x122>(v));
  v = fmaxf(v, dpp_mov<0x121>(v));
  return v;
}
DI unsigned row16_umax(unsigned v) {
  v = max(v, (unsigned)__builtin_amdgcn_update_dpp(v, v, 0x128, 0xf, 0xf, false));
  v = max(v, (unsigned)__builtin_amdgcn_update_dpp(v, v, 0x124, 0xf, 0xf, false));
  v = max(v, (unsigned)__builtin_amdgcn_update_dpp(v, v, 0x122, 0xf, 0xf, false));
  v = max(v, (unsigned)__builtin_amdgcn_update_dpp(v, v, 0x121, 0xf, 0xf, false));
  return v;
}
DI float rdlane(float v, int l) { return __uint_as_float(__builtin_amdgcn_readlane(__float_as_uint(v), l)); }
DI s16x4 tr_read(const LAS unsigned char* p) {
  typedef short v4i16_t __attribute__((ext_vector_type(4)));
  return __builtin_bit_cast(s16x4, __builtin_amdgcn_ds_read_tr16_b64_v4i16((LAS v4i16_t*)p));
}
DI bf16x8 tr_frag(const LAS unsigned char* p, int row_stride4) {
  s16x4 lo = tr_read(p), hi = tr_read(p + row_stride4);
  return __builtin_shufflevector(lo, hi, 0, 1, 2, 3, 4, 5, 6, 7);
}
DI __amdgpu_buffer_rsrc_t mk_rsrc(const void* p, unsigned bytes) {
  return __builtin_amdgcn_make_buffer_rsrc((void*)p, (short)0, (int)bytes, 0x00020000);
}
DI void glds16(__amdgpu_buffer_rsrc_t rs, unsigned dst, unsigned voff, unsigned soff) {
  asm volatile("s_mov_b32 m0, %0\n\ts_nop 0\n\tbuffer_load_dwordx4 %1, %2, %3 offen lds" ::"s"(dst), "v"(voff), "s"(rs), "s"(soff) : "memory");
}
DI u32x4 ld16(__amdgpu_buffer_rsrc_t rs, unsigned voff, unsigned soff) { return __builtin_amdgcn_raw_buffer_load_b128(rs, voff, soff, 0); }
DI u32x4 ld16c(__amdgpu_buffer_rsrc_t rs, unsigned voff, unsigned soff) { return __builtin_amdgcn_raw_buffer_load_b128(rs, voff, soff, 16); }
DI void st16(__amdgpu_buffer_rsrc_t rs, u32x4 v, unsigned voff, unsigned soff) { __builtin_amdgcn_raw_buffer_store_b128(v, rs, voff + soff, 0, 16); }
DI bf16x8 as_frag(u32x4 v) { return __builtin_bit_cast(bf16x8, v); }
DI u32x4 as_u4(bf16x8 v) { return __builtin_bit_cast(u32x4, v); }
template <int N> DI void wait_bar() { asm volatile("s_waitcnt vmcnt(%0) lgkmcnt(0)\n\ts_barrier" ::"n"(N) : "memory"); }

template <int V> struct IC { static constexpr int value = V; };
template <int I, int N, class Fn> DI void static_for(Fn&& fn) {
  if constexpr (I < N) { fn(IC<I>{}); static_for<I + 1, N>(fn); }
}
DI void drain() { asm volatile("s_waitcnt vmcnt(0)" ::: "memory"); }
template <int N> DI void wait_vm() { asm volatile("s_waitcnt vmcnt(%0)" ::"n"(N) : "memory"); }
DI void put_granule(unsigned char* ws, unsigned off, u64 tag16, unsigned payload) {
  __hip_atomic_store((gu64*)(ws + off), tag16 | (u64)(payload & 0xffffu), __ATOMIC_RELAXED, __HIP_MEMORY_SCOPE_AGENT);
}
template <int SLEEP> DI unsigned poll_granule(unsigned char* ws, unsigned off, u64 tag16) {
  u64 v = ~tag16;
  for (unsigned spins = 0;; ++spins) {
    if (v - tag16 > 0xffffull) v = __hip_atomic_load((gu64*)(ws + off), __ATOMIC_RELAXED, __HIP_MEMORY_SCOPE_AGENT);
    if (__all(v - tag16 <= 0xffffull) || spins > SPIN_CAP) break;
    __builtin_amdgcn_s_sleep(SLEEP);
  }
  __builtin_amdgcn_fence(__ATOMIC_ACQUIRE, "wavefront");
  return (unsigned)v & 0xffffu;
}
DI unsigned wait_lds_word(LAS unsigned char* p, unsigned want) {
  unsigned v = 0;
#pragma nounroll
  for (unsigned spins = 0; spins < (1u << 24); ++spins) {
    v = __builtin_amdgcn_readfirstlane(__hip_atomic_load((LAS unsigned*)p, __ATOMIC_RELAXED, __HIP_MEMORY_SCOPE_WORKGROUP));
    if (v >= want) break;
    __builtin_amdgcn_s_sleep(1);
  }
  asm volatile("" ::: "memory");
  return v;
}
template <int N> DI u64 wait_bar_epoch(const unsigned char* p) {
  u64 ep;
  asm volatile("s_load_dwordx2 %0, %1, 0x0\n\ts_waitcnt vmcnt(%2) lgkmcnt(0)\n\ts_barrier" : "=&s"(ep) : "s"(p), "n"(N) : "memory");
  return ep;
}
DI void bar_lds() { asm volatile("s_waitcnt lgkmcnt(0)\n\ts_barrier" ::: "memory"); }
DI void st_tile(LAS unsigned char* p, const f32x16& a) {
#pragma unroll
  for (int g = 0; g < 4; ++g) *(LAS f32x4*)(p + g * 1024) = f32x4{a[4 * g], a[4 * g + 1], a[4 * g + 2], a[4 * g + 3]};
}
DI void add_tile(f32x16& a, const LAS unsigned char* p) {
#pragma unroll
  for (int g = 0; g < 4; ++g) {
    const f32x4 v = *(const LAS f32x4*)(p + g * 1024);
    a[4 * g] += v[0]; a[4 * g + 1] += v[1]; a[4 * g + 2] += v[2]; a[4 * g + 3] += v[3];
  }
}

#define STAMP_DECL do {} while (0)
#define STAMP(id) do {} while (0)
#define QSTAMP(id) do {} while (0)
#define RSTAMP(id) do {} while (0)
#define STAMP_FLUSH(P, ph) do {} while (0)

DI void lds_arrive(LAS unsigned char* p) {
  asm volatile("s_mov_b64 exec, 1\n\tds_add_u32 %0, %1\n\ts_mov_b64 exec, -1" ::"v"((unsigned)(uintptr_t)p), "v"(1u) : "memory");
}

#define PINV(x) asm volatile("" : "+v"(x))
#define PINS(x) asm volatile("" : "+s"(x))
DI float sumsq(const u32x4& a, const u32x4& b) {
  typedef __bf16 bf2 __attribute__((ext_vector_type(2)));
  float n0 = 0.f, n1 = 0.f;
#pragma unroll
  for (int i = 0; i < 4; ++i) {
    n0 = __builtin_amdgcn_fdot2_f32_bf16(__builtin_bit_cast(bf2, a[i]), __builtin_bit_cast(bf2, a[i]), n0, false);
    n1 = __builtin_amdgcn_fdot2_f32_bf16(__builtin_bit_cast(bf2, b[i]), __builtin_bit_cast(bf2, b[i]), n1, false);
  }
  return n0 + n1;
}

DI void role_a(const Params& P, LAS unsigned char* lds, const int wt, const unsigned code_word) {
  const int lane = threadIdx.x & 63, r = lane & 31, h = lane >> 5;
  const int b = blockIdx.x, xcd = b & 7, idx = b >> 3;
  const unsigned lo = lane * 16;
  STAMP_DECL;
  STAMP(0);
  __builtin_amdgcn_s_setprio(2);
  f32x16 acc[4];
  const int rb = (xcd & 1) * 8 + (idx & 7), j = (xcd >> 1) * 4 + (idx >> 3);
  const int head = xcd * 2 + (idx & 1), qb = idx >> 1;
  unsigned sto[3];
#pragma unroll
  for (int ct = 0; ct < 3; ++ct) {
    const int u = 3 * j + ct, part = u >> 4, t = (u & 15) * 64 + rb * 4 + wt;
    sto[ct] = lo + (part == 0 ? QF_OFF + t * 2048 : KV_OFF + t * 4096 + (part - 1) * 2048);
    PINV(sto[ct]);
  }
  unsigned padv = r == DH ? 0x3f803f80u : 0u;
  PINV(padv);
  const int gq = head / 3, gk = (16 + head) / 3, gvv = (32 + head) / 3;
  unsigned pub_off = GQ_OFF + (j * 16 + rb) * GQW + wt * GQS + (lane & 1) * 8;
  unsigned poll_off = GQ_OFF + (h ? gq * 16 + qb : (lane < 16 ? gk : gvv) * 16 + wt * 4 + ((lane >> 2) & 3)) * GQW + (lane & 3) * GQS + h * 8;
  unsigned chk_off = GQ_OFF + (gk * 16 + (lane >> 2)) * GQW + (lane & 3) * GQS;
  unsigned g_off = GG_OFF + ((qb * 4 + wt) * 32 + r) * 8;
  unsigned o_off = lo + OF_OFF + ((qb * 4 + wt) * 16 + head) * 2048;
  PINV(pub_off); PINV(poll_off); PINV(chk_off); PINV(g_off); PINV(o_off);
  unsigned kvbase = KV_OFF + (head * 64 + wt * 16) * 4096, go_off = GO_OFF + ((qb * 4 + wt) * 16 + head) * 128;
  unsigned qofs[4];
#pragma unroll
  for (int t = 0; t < 4; ++t) { qofs[t] = QF_OFF + (unsigned)(head * 64 + qb * 4 + ((wt + 1 + t) & 3)) * 2048; PINS(qofs[t]); }
  PINS(kvbase); PINS(go_off);
  unsigned xoff[4];
#pragma unroll
  for (int ks = 0; ks < 4; ++ks) xoff[ks] = (unsigned)(L_X + wt * 4096 + r * 128 + (((2 * ks + h) ^ ((r >> 1) & 7)) << 4));
  const unsigned wqoff = (unsigned)(L_WQ + (8 * h + ((lane & 15) >> 2)) * 192 + ((lane >> 4) & 1) * 32 + (lane & 3) * 8);
  u64 ep;
  asm volatile("s_load_dwordx2 %0, %1, 0x0\n\ts_waitcnt lgkmcnt(0)" : "=&s"(ep) : "s"(P.ws + FL_OFF) : "memory");
  u64 tag16 = (ep + 1) << 16;
  unsigned char* wsb = P.ws + SET_BASE + (((unsigned)ep + 1u) & 3u) * SET_STRIDE;
  PINS(tag16); PINS(wsb);
  const __amdgpu_buffer_rsrc_t rsWs = mk_rsrc(wsb, WS_END);
#pragma unroll
  for (int i = 0; i < 3; ++i) acc[i] = f32x16{};
  unsigned wtouch = 0;
  auto kloop = [&](auto nvc) {
    constexpr int NV = decltype(nvc)::value;
    bf16x8 xf[4], wf[4][3];
    auto mm = [&](int ks, int ct) { acc[ct] = ct >= 3 - NV ? MFMA(xf[ks], wf[ks][ct], acc[ct]) : MFMA(wf[ks][ct], xf[ks], acc[ct]); };
    auto step = [&](int c, int ks, bool domm) {
      const int km = (ks + 2) & 3;
#pragma unroll
      for (int ct = 0; ct < 3; ++ct) {
        if (domm) mm(km, ct);
        SBAR();
        if (ct == 0) xf[ks] = *(const LAS bf16x8*)(lds + xoff[ks] + c * 16384);
        wf[ks][ct] = tr_frag(lds + wqoff + (c & 3) * 12288 + ks * 16 * 192 + ct * 56, 4 * 192);
        SBAR();
      }
    };
#pragma unroll
    for (int c = 0; c < 7; ++c) {
      bar_lds();
      if (c == 0) STAMP(1);
      if (c == MPF) {
        const unsigned l = (unsigned)(b * 128 + (wt & 1) * 64 + lane) * 128;
        if (wt < 2) wtouch = l < DM * FF * 2 ? __builtin_amdgcn_raw_buffer_load_b32(mk_rsrc(P.w1, DM * FF * 2), l, 0, 0)
                           : l < 2 * DM * FF * 2 ? __builtin_amdgcn_raw_buffer_load_b32(mk_rsrc(P.w2, DM * FF * 2), l - DM * FF * 2, 0, 0)
                                                 : __builtin_amdgcn_raw_buffer_load_b32(mk_rsrc(P.wo, DM * DM * 2), l - 2 * DM * FF * 2, 0, 0);
        SBAR();
      }
      QSTAMP(1 + c);
      step(c, 0, c > 0);
      step(c, 1, c > 0);
      step(c, 2, true);
      step(c, 3, true);
    }
    bar_lds();
    QSTAMP(8);
#pragma unroll
    for (int ct = 0; ct < 3; ++ct) { mm(2, ct); mm(3, ct); SBAR(); }
  };
  auto epi = [&](auto nqc, auto nvc) {
    constexpr int NQ = decltype(nqc)::value, NV = decltype(nvc)::value;
    STAMP(2);
    asm volatile("" ::"v"(code_word), "v"(wtouch));
    u32x4 t0[3], t1[3];
#pragma unroll
    for (int ct = 0; ct < 3; ++ct) {
      if (ct < NQ) acc[ct] *= QSCALE;
      t0[ct] = as_u4(pack8(acc[ct], 0));
      if (ct >= 3 - NV) {
#pragma unroll
        for (int i = 0; i < 4; ++i) t0[ct][i] = r < DH ? t0[ct][i] : padv;
      }
      st16(rsWs, t0[ct], sto[ct], 0);
      t1[ct] = as_u4(pack8(acc[ct], 1));
      if (ct >= 3 - NV) {
#pragma unroll
        for (int i = 0; i < 4; ++i) t1[ct][i] = r < DH ? t1[ct][i] : padv;
      } else {
        t1[ct][2] = h ? (ct < NQ ? 0u : 0x3f803f80u) : t1[ct][2];
        t1[ct][3] = h ? (ct < NQ ? 0u : 0x00003f80u) : t1[ct][3];
      }
      st16(rsWs, t1[ct], sto[ct], 1024);
      SBAR();
    }
    STAMP(8);
    unsigned mq = 0, mk = 0;
#pragma unroll
    for (int ct = 0; ct < 3; ++ct) {
      const unsigned n = __float_as_uint(sumsq(t0[ct], t1[ct])) & 0x7fffffffu;
      if (ct < NQ) mq = max(mq, n); else mk = max(mk, n);
    }
    auto bound = [&](unsigned m) {
      m = row16_umax(m);
      const float lo2 = __uint_as_float(max((unsigned)__builtin_amdgcn_readlane(m, 0), (unsigned)__builtin_amdgcn_readlane(m, 16)));
      const float hi2 = __uint_as_float(max((unsigned)__builtin_amdgcn_readlane(m, 32), (unsigned)__builtin_amdgcn_readlane(m, 48)));
      return ((__float_as_uint(lo2 + hi2) >> 16) + 1u) & 0xffffu;
    };
    const unsigned pq = NQ > 0 ? bound(mq) : 0u, pk = NQ < 3 ? bound(mk) : 0u;
    u64 gran = tag16 | ((lane & 1) ? pq : pk);
    PINV(gran);
    drain();
    STAMP(9);
    if (lane < 2) __hip_atomic_store((gu64*)(wsb + pub_off), gran, __ATOMIC_RELAXED, __HIP_MEMORY_SCOPE_AGENT);
  };
  if (j < 10) {
    kloop(IC<0>{});
    if (j < 5) epi(IC<3>{}, IC<0>{});
    else if (j == 5) epi(IC<1>{}, IC<0>{});
    else epi(IC<0>{}, IC<0>{});
  } else if (j == 10) {
    kloop(IC<1>{});
    epi(IC<0>{}, IC<1>{});
  } else {
    kloop(IC<3>{});
    epi(IC<0>{}, IC<3>{});
  }
  STAMP(3);
  __builtin_amdgcn_s_sleep(PDELAY);
  const unsigned pay = poll_granule<POLL_A>(wsb, poll_off, tag16);
  STAMP(4);
  u32x4 q[4][2];
  LAS unsigned char* const cmb = lds + L_CMB + wt * 12288 + lo;
  u64 gv = 0;
  unsigned thr = 0;
#pragma nounroll
  for (int pass = 0;; ++pass) {
    u32x4 kv[KVD][4];
    kv[0][0] = ld16c(rsWs, lo, kvbase);
    SBAR();
    if (pass == 0) { q[0][0] = ld16c(rsWs, lo, qofs[0]); q[0][1] = ld16c(rsWs, lo + 1024, qofs[0]); }
    SBAR();
    kv[0][1] = ld16c(rsWs, lo + 1024, kvbase);
    SBAR();
    if (pass == 0) {
#pragma unroll
      for (int t = 1; t < 4; ++t) { q[t][0] = ld16c(rsWs, lo, qofs[t]); q[t][1] = ld16c(rsWs, lo + 1024, qofs[t]); SBAR(); }
    }
#pragma unroll
    for (int e = 2; e < 4 * (KVD - 1); ++e) { kv[e >> 2][e & 3] = ld16c(rsWs, lo + (e & 3) * 1024, kvbase + (e >> 2) * 4096); SBAR(); }
#pragma unroll
    for (int i = 0; i < 4; ++i) acc[i] = f32x16{};
    {
      const float q2 = __uint_as_float(__builtin_amdgcn_readlane(row16_umax(pay), 32) << 16);
      const float lim = SAFE_BOUND * SAFE_BOUND * __builtin_amdgcn_rcpf(q2);
      thr = lim < 1e30f ? __float_as_uint(lim) >> 16 : (lim >= 1e30f ? 0x7000u : 0u);
      PINV(thr);
    }
    __builtin_amdgcn_s_setprio(1);
    f32x16 sc = MFMA(as_frag(kv[0][0]), as_frag(q[0][0]), f32x16{});
    sc = MFMA(as_frag(kv[0][1]), as_frag(q[0][1]), sc);
    STAMP(5);
    u32x4 p0, p1 = {0, 0, 0, 0};
    float e0 = 0.f, e1 = 0.f, e2 = 0.f, e3 = 0.f;
    u64 ck = 0, cv = 0;
#define EXP4(i) do { e0 = __builtin_amdgcn_exp2f(sc[i]); e1 = __builtin_amdgcn_exp2f(sc[(i) + 1]); e2 = __builtin_amdgcn_exp2f(sc[(i) + 2]); e3 = __builtin_amdgcn_exp2f(sc[(i) + 3]); \
    asm volatile("" : "+v"(e0), "+v"(e1), "+v"(e2), "+v"(e3)); SBAR(); } while (0)
#define CVT2(P, i) do { P[i] = cvtpk(e0, e1); P[(i) + 1] = cvtpk(e2, e3); asm volatile("" : "+v"(P)); SBAR(); } while (0)
#define PART(t, g) do { *(LAS f32x4*)(cmb + (t) * 4096 + (g) * 1024) = f32x4{acc[t][4 * (g)], acc[t][4 * (g) + 1], acc[t][4 * (g) + 2], acc[t][4 * (g) + 3]}; SBAR(); } while (0)
    static_for<0, 64>([&](auto ic) {
      constexpr int i = decltype(ic)::value;
      constexpr int kt = i >> 2, t = i & 3, kn_ = (i + 1) >> 2, tn = (i + 1) & 3, kp = (i - 1) >> 2, tp = (i - 1) & 3;
      constexpr int kl = kt + KVD - 1;
      if (i == 24) STAMP(11);
      if (i == 56) (void)wait_lds_word(lds + L_CTRL, 4);
      f32x16 sn;
      SBAR();
      if (i < 63) sn = MFMA(as_frag(kv[kn_ % KVD][0]), as_frag(q[tn][0]), f32x16{});
      SBAR();
      if constexpr (i >= 62) PART(i - 62, 0);
      if (i > 0) CVT2(p1, 2);
      EXP4(0);
      if (i > 0) acc[tp] = MFMA(as_frag(kv[(kp + KVD) % KVD][3]), as_frag(p1), acc[tp]);
      SBAR();
      if (kl < 16) kv[kl % KVD][t] = ld16c(rsWs, lo + t * 1024, kvbase + kl * 4096);
      if (i == 52) gv = __hip_atomic_load((gu64*)(wsb + g_off), __ATOMIC_RELAXED, __HIP_MEMORY_SCOPE_AGENT);
      if (i == 53) ck = __hip_atomic_load((gu64*)(wsb + chk_off), __ATOMIC_RELAXED, __HIP_MEMORY_SCOPE_AGENT);
      if (i == 54) cv = __hip_atomic_load((gu64*)(wsb + (gvv - gk) * 16 * GQW + chk_off), __ATOMIC_RELAXED, __HIP_MEMORY_SCOPE_AGENT);
      SBAR();
      if constexpr (i >= 62) PART(i - 62, 1);
      CVT2(p0, 0);
      EXP4(4);
      if (i < 63) sn = MFMA(as_frag(kv[kn_ % KVD][1]), as_frag(q[tn][1]), sn);
      SBAR();
      if constexpr (i >= 62) PART(i - 62, 2);
      CVT2(p0, 2);
      EXP4(8);
      acc[t] = MFMA(as_frag(kv[kt % KVD][2]), as_frag(p0), acc[t]);
      SBAR();
      if constexpr (i >= 62) PART(i - 62, 3);
      CVT2(p1, 0);
      EXP4(12);
      if (i < 63) sc = sn;
    });
    CVT2(p1, 2);
    acc[3] = MFMA(as_frag(kv[15 % KVD][3]), as_frag(p1), acc[3]);
    SBAR();
    PART(2, 0); PART(2, 1); PART(2, 2); PART(2, 3);
#undef EXP4
#undef CVT2
#undef PART
    __builtin_amdgcn_s_setprio(2);
    if (__builtin_expect(__all(ck - tag16 <= (u64)thr && cv - tag16 <= 0x7000ull && gv - tag16 <= 0xffffull), 1)) break;
    gv = poll_granule<2>(wsb, g_off, tag16);
    if (pass) break;
    const unsigned pk = poll_granule<2>(wsb, chk_off, tag16), pv = poll_granule<2>(wsb, (gvv - gk) * 16 * GQW + chk_off, tag16);
    if (__all(pk <= thr && pv <= 0x7000u)) break;
    float mx[4];
#pragma unroll
    for (int t = 0; t < 4; ++t) mx[t] = -INFINITY;
    for (int kt = 0; kt < 64; ++kt) {
      const u32x4 k0 = ld16c(rsWs, lo, KV_OFF + (head * 64 + kt) * 4096), k1 = ld16c(rsWs, lo + 1024, KV_OFF + (head * 64 + kt) * 4096);
#pragma unroll
      for (int t = 0; t < 4; ++t) {
        f32x16 s = MFMA(as_frag(k0), as_frag(q[t][0]), f32x16{});
        s = MFMA(as_frag(k1), as_frag(q[t][1]), s);
#pragma unroll
        for (int i = 0; i < 16; ++i) mx[t] = fmaxf(mx[t], s[i]);
      }
    }
#pragma unroll
    for (int t = 0; t < 4; ++t) {
      const float nm = -fmaxf(mx[t], swap_half(mx[t]));
      const unsigned hi = __float_as_uint(nm) & 0xffff0000u;
      const float r1 = nm - __uint_as_float(hi);
      const unsigned mid = __float_as_uint(r1) & 0xffff0000u;
      const float r2 = r1 - __uint_as_float(mid);
      if (h) { q[t][1][2] = (hi >> 16) | mid; q[t][1][3] = __float_as_uint(r2) >> 16; }
    }
  }
  STAMP(6);
  asm volatile("s_waitcnt lgkmcnt(0)" ::: "memory");
  lds_arrive(lds + L_CTRL + 20);
  (void)wait_lds_word(lds + L_CTRL + 20, 4);
  f32x16 o = acc[3];
#pragma unroll
  for (int u = 0; u < 3; ++u) add_tile(o, lds + L_CMB + ((wt + 3 - u) & 3) * 12288 + u * 4096 + lo);
  const auto lsw = __builtin_amdgcn_permlane32_swap(__float_as_uint(o[12]), __float_as_uint(o[12]), false, false);
  const float inv = __uint_as_float((unsigned)gv << 16) * __builtin_amdgcn_rcpf(__uint_as_float(lsw[1]));
  o *= inv;
  st16(rsWs, as_u4(pack8(o, 0)), o_off, 0);
  u32x4 o1 = as_u4(pack8(o, 1));
  o1[2] = h ? 0u : o1[2];
  o1[3] = h ? 0u : o1[3];
  st16(rsWs, o1, o_off, 1024);
  STAMP(7);
  drain();
  if (lane == 0) __hip_atomic_store((gu64*)(wsb + go_off), tag16, __ATOMIC_RELAXED, __HIP_MEMORY_SCOPE_AGENT);
  STAMP(10);
  STAMP_FLUSH(P, 0);
}

constexpr int W2S = 6;
constexpr int HPRE = 5;
#define LEA 5
#define LEB 11
#define LE0 16
#define LU0 3
#define F1LA 2
#define PACE 4
#define PACE2 2
constexpr int ffn2_ops(int f) { return (f + W2S < 14 ? 4 : 0) + 4; }
constexpr int ffn2_younger(int f, int upto) {
  int n = 4;
  for (int i = f - W2S + 1; i < upto; ++i) n += ffn2_ops(i);
  return n;
}

DI void role_f(const Params& P, LAS unsigned char* lds, const int wt) {
  u64 ep;
  const int lane = threadIdx.x & 63, r = lane & 31, h = lane >> 5;
  const int b = blockIdx.x, xcd = b & 7, idx = b >> 3;
  const int rb = (xcd & 1) * 8 + (idx & 7), j = (xcd >> 1) * 4 + (idx >> 3);
  const unsigned lo = lane * 16, lds0 = (unsigned)(uintptr_t)lds;
  const bool ffn = j < 14;
  STAMP_DECL;
  STAMP(0);
  f32x16 acc[4];
  u32x4 wl[28];
  const __amdgpu_buffer_rsrc_t rsL = mk_rsrc(ffn ? P.w1 : P.gw, ffn ? DM * FF * 2 : DM * 2);
  const unsigned lsrc = ffn ? (unsigned)((lane >> 2) * (FF * 2) + (j * 4 + wt) * 64 + (lane & 3) * 16) : (unsigned)(h * 16);
  const unsigned lstep = ffn ? 16 * FF * 2 : 32;
  const int rot = ROT1(rb, j);
  auto kc = [&](int c) { const int v = c + rot; return v >= 7 ? v - 7 : v; };
  auto L = [&](int u0, int u1) {
#pragma unroll
    for (int u = u0; u < u1; ++u) wl[u] = ld16(rsL, lsrc, (kc(u >> 2) * 4 + (u & 3)) * lstep);
    SBAR();
  };
  {
    const __amdgpu_buffer_rsrc_t rsX = mk_rsrc(P.x, SEQ * DM * 2);
    const __amdgpu_buffer_rsrc_t rsW = mk_rsrc(P.wqkv, DM * 3 * DM * 2);
    unsigned xsrc[2], wsrc[3];
#pragma unroll
    for (int i = 0; i < 2; ++i) {
      const int row = 32 * wt + 8 * i + (lane >> 3), ch = (lane & 7) ^ ((row >> 1) & 7);
      xsrc[i] = (unsigned)((rb * 128 + row) * (DM * 2) + ch * 16);
    }
#pragma unroll
    for (int i = 0; i < 3; ++i) {
      const int p = 64 * (3 * wt + i) + lane, kr = p / 12, ch = p % 12;
      wsrc[i] = (unsigned)(kr * (3 * DM * 2) + j * 168 + (ch < 11 ? ch : 10) * 16);
    }
    auto X = [&](int c) {
#pragma unroll
      for (int i = 0; i < 4; ++i) glds16(rsX, lds0 + L_X + c * 16384 + (4 * wt + i) * 1024, xsrc[i & 1], kc(c) * 128 + (i >> 1) * 16 * DM * 2);
    };
    auto W = [&](int c) {
#pragma unroll
      for (int i = 0; i < 3; ++i) glds16(rsW, lds0 + L_WQ + (c & 3) * 12288 + (3 * wt + i) * 1024, wsrc[i], kc(c) * 64 * 3 * DM * 2);
    };
    X(0); W(0); X(1); W(1); X(2);
    QSTAMP(1);
    wait_bar<11>();
    STAMP(1);
    W(2); X(3); W(3);
    QSTAMP(2);
    wait_bar<14>();
    X(4); W(4);
    QSTAMP(3);
    wait_bar<14>();
    X(5); W(5);
    QSTAMP(4);
    wait_bar<14>();
    X(6); W(6);
    QSTAMP(5);
    wait_bar<14>();
    if (wt == 0 && lane < 8) *(LAS unsigned*)(lds + L_CTRL + lane * 4) = 0u;
    L(0, LEA);
    QSTAMP(6);
    ep = wait_bar_epoch<7 + LEA>(P.ws + FL_OFF);
    L(LEA, LEB);
    QSTAMP(7);
    wait_bar<LEB>();
    L(LEB, LE0);
    QSTAMP(8);
    bar_lds();
    __builtin_amdgcn_s_sleep(FDELAY);
  }
  STAMP(2);
  const u64 tag = (ep + 1) << 16;
  unsigned char* const wsb = P.ws + SET_BASE + (((unsigned)ep + 1u) & 3u) * SET_STRIDE;
  const __amdgpu_buffer_rsrc_t rsWs = mk_rsrc(wsb, WS_END);
  unsigned xoff[4];
#pragma unroll
  for (int ks = 0; ks < 4; ++ks) xoff[ks] = (unsigned)(L_X + r * 128 + (((2 * ks + h) ^ ((r >> 1) & 7)) << 4));
  if (ffn) {
    LAS unsigned char* ring = lds + L_W1 + wt * 8192;
    const unsigned troff = (unsigned)((8 * h + ((lane & 15) >> 2)) * 64 + ((lane >> 4) & 1) * 32 + (lane & 3) * 8);
#pragma unroll
    for (int i = 0; i < 4; ++i) acc[i] = f32x16{};
    RSTAMP(1);
    constexpr int LA = F1LA;
    bf16x8 xf[LA + 1][4], wf[LA + 1];
    auto wr = [&](int u) { *(LAS u32x4*)(ring + (u & 7) * 1024 + lo) = wl[u]; };
    auto rdw = [&](int u) { wf[u % (LA + 1)] = tr_frag(ring + (u & 7) * 1024 + troff, 4 * 64); };
    auto rdx = [&](int u, int t) { xf[u % (LA + 1)][t] = *(const LAS bf16x8*)(lds + xoff[u & 3] + (u >> 2) * 16384 + t * 4096); };
#pragma unroll
    for (int u = 0; u < LA + 2; ++u) wr(u);
#pragma unroll
    for (int u = 0; u < LA; ++u) {
      rdw(u);
#pragma unroll
      for (int t = 0; t < 4; ++t) rdx(u, t);
    }
    SBAR();
#pragma unroll
    for (int u = 0; u < 28; ++u) {
      if ((u & 3) == 0) RSTAMP(2 + (u >> 2));
#pragma unroll
      for (int t = 0; t < 4; ++t) {
        if (u + LA < 28) {
          if (t == 1) rdw(u + LA);
          rdx(u + LA, t);
          if (t == 3 && u + LA + 2 < 28) wr(u + LA + 2);
        }
        SBAR();
        acc[t] = MFMA(wf[u % (LA + 1)], xf[u % (LA + 1)][t], acc[t]);
        SBAR();
        if (u >= LU0 && LE0 + (u - LU0) * 4 + t < 28) L(LE0 + (u - LU0) * 4 + t, LE0 + (u - LU0) * 4 + t + 1);
      }
    }
    lds_arrive(lds + L_CTRL);
    RSTAMP(9);
    STAMP(3);
    __builtin_amdgcn_s_setprio(3);
#pragma unroll
    for (int t = 0; t < 4; ++t) {
      f32x16 z = acc[t];
#pragma unroll
      for (int i = 0; i < 16; ++i) z[i] = z[i] * __builtin_amdgcn_rcpf(1.f + __builtin_amdgcn_exp2f(-LOG2E * z[i]));
      const unsigned so = HF_OFF + ((rb * 4 + t) * 56 + j * 4 + wt) * 2048;
      st16(rsWs, as_u4(pack8(z, 0)), lo, so);
      st16(rsWs, as_u4(pack8(z, 1)), lo + 1024, so);
    }
    drain();
    if (lane == 0) put_granule(wsb, GH_OFF + (rb * 16 + j) * 128 + wt * 8, tag, 0);
  } else {
    __builtin_amdgcn_s_setprio(3);
    if (j == 14) {
      L(LE0, 28);
      acc[0] = f32x16{};
#pragma unroll
      for (int u = 0; u < 28; ++u) {
        const bf16x8 xf = *(const LAS bf16x8*)(lds + xoff[u & 3] + (u >> 2) * 16384 + wt * 4096);
        acc[0] = MFMA(as_frag(wl[u]), xf, acc[0]);
      }
      const float g = __builtin_amdgcn_rcpf(1.f + __builtin_amdgcn_exp2f(-LOG2E * acc[0][0]));
      if (h == 0) put_granule(wsb, GG_OFF + ((rb * 4 + wt) * 32 + r) * 8, tag, cvtpk(g, 0.f));
    }
    lds_arrive(lds + L_CTRL);
    const __amdgpu_buffer_rsrc_t rsO = mk_rsrc(P.wo, DM * DM * 2);
    const int id0 = ((j - 14) * 16 + rb) * 4 + wt;
    for (int id = id0; id < 224; id += 128) {
      const int nt = id >> 4, hd = id & 15;
      unsigned rp[16];
#pragma unroll
      for (int e = 0; e < 16; ++e)
        rp[e] = __builtin_amdgcn_raw_buffer_load_b16(rsO, (unsigned)(((4 * h + 8 * (e >> 2) + (e & 3)) * DM + r) * 2), (unsigned)((DH * hd * DM + 32 * nt) * 2), 0);
      u32x4 a, c2;
#pragma unroll
      for (int i = 0; i < 4; ++i) {
        a[i] = (rp[2 * i] & 0xffffu) | (rp[2 * i + 1] << 16);
        c2[i] = (rp[8 + 2 * i] & 0xffffu) | (rp[8 + 2 * i + 1] << 16);
      }
      c2[2] = h ? 0u : c2[2];
      c2[3] = h ? 0u : c2[3];
      st16(rsWs, a, lo, WOF_OFF + id * 2048);
      st16(rsWs, c2, lo + 1024, WOF_OFF + id * 2048);
      drain();
      if (lane == 0) put_granule(wsb, GW_OFF + id * 8, tag, 0);
    }
  }
  STAMP(4);
  if (idx >= 28) {
    if (b == 255 && wt == 0) {
      __builtin_amdgcn_s_sleep(127);
#pragma unroll
      for (int i = 0; i < 4; ++i) (void)poll_granule<64>(wsb, GQ_OFF + (i * 64 + lane) * GQW, tag);
      if (lane == 0) __hip_atomic_store((gu64*)(P.ws + FL_OFF), ep + 1, __ATOMIC_RELAXED, __HIP_MEMORY_SCOPE_AGENT);
    }
    STAMP(10);
    STAMP_FLUSH(P, 0);
    return;
  }
  const int mb = 4 * xcd + (idx & 3), nb = idx >> 2;
  const __amdgpu_buffer_rsrc_t rsW2 = mk_rsrc(P.w2, FF * DM * 2), rsH = rsWs;
  const unsigned w2src = (unsigned)((wt * 448 + (lane >> 3)) * (DM * 2) + nb * 128 + ((((lane >> 2) & 1) ^ ((lane >> 4) & 1)) * 64) + (lane & 3) * 16);
  const unsigned ring0 = lds0 + L_W2 + wt * 24576;
  const int rot2 = ROT2;
  auto fr = [&](int f) { const int v = f + rot2; return v >= 14 ? v - 14 : v; };
  auto D = [&](int f) {
#pragma unroll
    for (int i = 0; i < 4; ++i) glds16(rsW2, ring0 + (f % W2S) * 4096 + i * 1024, w2src, (fr(f) * 32 + i * 8) * DM * 2);
  };
  (void)wait_lds_word(lds + L_CTRL, 4);
#pragma unroll
  for (int f = 0; f < W2S; ++f) { D(f); __builtin_amdgcn_s_sleep(PACE); }
  const int ftl = wt * 14 + ((lane & 15) < 14 ? (lane & 15) : 0);
  (void)poll_granule<8>(wsb, (lane & 16) ? GW_OFF + ((nb * 2 + ((lane >> 2) & 1)) * 16 + wt * 4 + (lane & 3)) * 8
                                        : GH_OFF + ((mb >> 1) * 16 + (ftl >> 2)) * 128 + (ftl & 3) * 8, tag);
  STAMP(5);
  u32x4 hb[HPRE][4], wo[16];
  const unsigned hbase = HF_OFF + (mb * 2 * 56 + wt * 14) * 2048;
  auto H = [&](int f) {
#pragma unroll
    for (int e = 0; e < 4; ++e) hb[f % HPRE][e] = ld16c(rsH, lo + (e & 1) * 1024, hbase + ((e >> 1) * 56 + fr(f)) * 2048);
  };
#pragma unroll
  for (int f = 0; f < HPRE; ++f) { H(f); SBAR(); }
  const float gl = __uint_as_float(poll_granule<8>(wsb, GG_OFF + (mb * 64 + lane) * 8, tag) << 16), gs = swap_half(gl);
  const float og0 = 1.f - (h ? gs : gl), og1 = 1.f - (h ? gl : gs);
  const int q4 = (lane & 15) >> 2;
  unsigned woff[2];
#pragma unroll
  for (int nt = 0; nt < 2; ++nt) woff[nt] = (unsigned)(L_W2 + wt * 24576 + (4 * h + q4) * 128 + ((nt ^ (q4 >> 1)) * 64) + ((lane >> 4) & 1) * 32 + (lane & 3) * 8);
#pragma unroll
  for (int i = 0; i < 4; ++i) acc[i] = f32x16{};
  STAMP(6);
  __builtin_amdgcn_s_setprio(0);
  bf16x8 wf[2][2][2];
  auto RW = [&](int f) {
#pragma unroll
    for (int s = 0; s < 2; ++s)
#pragma unroll
      for (int nt = 0; nt < 2; ++nt) wf[f & 1][s][nt] = tr_frag(lds + woff[nt] + (f % W2S) * 4096 + s * 2048, 1024);
  };
  RW(0);
  static_for<0, 14>([&](auto fc) {
    constexpr int f = decltype(fc)::value;
    asm volatile("s_waitcnt lgkmcnt(0)" ::: "memory");
    SBAR();
    if constexpr (f + 1 < 14) {
      if constexpr (f + 1 >= W2S) wait_vm<ffn2_younger(f + 1, f)>();
      RW(f + 1);
    }
    SBAR();
#pragma unroll
    for (int s = 0; s < 2; ++s)
#pragma unroll
      for (int nt = 0; nt < 2; ++nt)
#pragma unroll
        for (int mt = 0; mt < 2; ++mt) {
          acc[nt * 2 + mt] = MFMA(wf[f & 1][s][nt], as_frag(hb[f % HPRE][mt * 2 + s]), acc[nt * 2 + mt]);
        }
    SBAR();
    if constexpr (f + W2S < 14) D(f + W2S);
    if constexpr (f + HPRE < 14) H(f + HPRE);
    else if constexpr (f + HPRE < 18) {
      constexpr int e0 = (f + HPRE - 14) * 4;
#pragma unroll
      for (int e = e0; e < e0 + 4; ++e) wo[e] = ld16c(rsWs, lo, WOF_OFF + ((nb * 2 + (e >> 3)) * 16 + wt * 4) * 2048 + (e & 7) * 1024);
    }
    SBAR();
    __builtin_amdgcn_s_sleep(PACE2);
    SBAR();
  });
  STAMP(7);
  __builtin_amdgcn_s_setprio(3);
#pragma unroll
  for (int i = 0; i < 16; ++i) { acc[0][i] *= og0; acc[1][i] *= og1; acc[2][i] *= og0; acc[3][i] *= og1; }
  (void)wait_lds_word(lds + L_CTRL + 20, 1);
  __builtin_amdgcn_s_sleep(ODELAY);
  (void)poll_granule<POLL_A>(wsb, GO_OFF + ((mb * 2 + ((lane >> 2) & 1)) * 16 + wt * 4 + (lane & 3)) * 128, tag);
  STAMP(8);
  u32x4 of[16];
#pragma unroll
  for (int e = 0; e < 8; ++e)
#pragma unroll
    for (int mt = 0; mt < 2; ++mt) of[mt * 8 + e] = ld16c(rsWs, lo, OF_OFF + ((mb * 2 + mt) * 16 + wt * 4) * 2048 + e * 1024);
  SBAR();
#pragma unroll
  for (int e = 0; e < 8; ++e)
#pragma unroll
    for (int nt = 0; nt < 2; ++nt)
#pragma unroll
      for (int mt = 0; mt < 2; ++mt) acc[nt * 2 + mt] = MFMA(as_frag(wo[nt * 8 + e]), as_frag(of[mt * 8 + e]), acc[nt * 2 + mt]);
#pragma unroll
  for (int t = 0; t < 4; ++t) st_tile(lds + L_W2 + wt * 24576 + t * 4096 + lo, acc[t]);
  STAMP(9);
  bar_lds();
  f32x16 a = f32x16{};
#pragma unroll
  for (int f = 0; f < 4; ++f) add_tile(a, lds + L_W2 + f * 24576 + wt * 4096 + lo);
  unsigned short* dst = P.out + (size_t)((mb * 2 + (wt & 1)) * 32 + r) * DM + (nb * 2 + (wt >> 1)) * 32 + 8 * h;
#pragma unroll
  for (int k = 0; k < 2; ++k) {
    const auto x = __builtin_amdgcn_permlane32_swap(cvtpk(a[8 * k], a[8 * k + 1]), cvtpk(a[8 * k + 4], a[8 * k + 5]), false, false);
    const auto y = __builtin_amdgcn_permlane32_swap(cvtpk(a[8 * k + 2], a[8 * k + 3]), cvtpk(a[8 * k + 6], a[8 * k + 7]), false, false);
    *(u32x4*)(dst + 16 * k) = u32x4{x[0], y[0], x[1], y[1]};
  }
  STAMP(10);
  STAMP_FLUSH(P, 0);
}

extern __shared__ __attribute__((aligned(16))) unsigned char g_lds[];

__global__ __launch_bounds__(512, 2) void k_fused(Params P) {
  u64 pc;
  asm volatile("s_getpc_b64 %0" : "=s"(pc));
  const unsigned code_word = __builtin_amdgcn_raw_buffer_load_b32(mk_rsrc((const void*)(pc & ~63ull), CODE_PF),
      (threadIdx.x & 256) * 4096 + (((blockIdx.x >> 3) * 32 + ((threadIdx.x >> 3) & 24) + (threadIdx.x & 7)) * 64), 0, 0);
  SBAR();
  LAS unsigned char* lds = (LAS unsigned char*)g_lds;
  const int w = __builtin_amdgcn_readfirstlane(threadIdx.x >> 6);
  if (w < 4) role_a(P, lds, w, code_word);
  else role_f(P, lds, w - 4);
}

extern "C" void kernel_launch(void* const* d_in, const int* in_sizes, int n_in, void* d_out, int out_size, void* d_ws,
                              size_t ws_size, hipStream_t stream) {
  Params P;
  P.x = (const unsigned short*)d_in[0];
  P.wqkv = (const unsigned short*)d_in[1];
  P.wo = (const unsigned short*)d_in[2];
  P.w1 = (const unsigned short*)d_in[4];
  P.w2 = (const unsigned short*)d_in[5];
  P.gw = (const unsigned short*)d_in[6];
  P.out = (unsigned short*)d_out;
  P.ws = (unsigned char*)d_ws;
  (void)hipFuncSetAttribute((const void*)k_fused, hipFuncAttributeMaxDynamicSharedMemorySize, LDS_BYTES);
  k_fused<<<256, 512, LDS_BYTES, stream>>>(P);
}
```

```cpp
#define XPF
#define ROT1(rb,j) ((((rb) + (j)) & 1) * 3)
#include <hip/hip_runtime.h>
#include <hip/hip_bf16.h>
#include <stdint.h>

typedef short bf16x8 __attribute__((ext_vector_type(8)));
typedef short s16x4 __attribute__((ext_vector_type(4)));
typedef float f32x16 __attribute__((ext_vector_type(16)));
typedef float f32x4 __attribute__((ext_vector_type(4)));
typedef unsigned u32x4 __attribute__((ext_vector_type(4)));
typedef unsigned u32x2 __attribute__((ext_vector_type(2)));
#define LAS __attribute__((address_space(3)))
#define DI __device__ __forceinline__
#define MFMA(a, b, c) __builtin_amdgcn_mfma_f32_32x32x16_bf16((a), (b), (c), 0, 0, 0)
#define SBAR() __builtin_amdgcn_sched_barrier(0)

constexpr int SEQ = 2048, DM = 448, NH = 16, DH = 28, FF = 1792;
constexpr unsigned FL_OFF = 0;
#define SETB 1u
constexpr unsigned SET_BASE = SETB << 20, SET_STRIDE = 24u << 20;
#define GQS 16u
constexpr unsigned GQW = GQS == 16u ? 128u : 4u * GQS;
constexpr unsigned GQ_OFF = 0;
constexpr unsigned GH_OFF = GQ_OFF + 256 * GQW;
constexpr unsigned GO_OFF = GH_OFF + 256 * 128;
constexpr unsigned GW_OFF = GO_OFF + 1024 * 128;
constexpr unsigned GG_OFF = GW_OFF + 256 * 8;
constexpr unsigned QF_OFF = 524288;
constexpr unsigned KV_OFF = QF_OFF + 16 * 64 * 2048;
constexpr unsigned HF_OFF = KV_OFF + 16 * 64 * 4096;
constexpr unsigned WOF_OFF = HF_OFF + 64 * 56 * 2048;
constexpr unsigned OF_OFF = WOF_OFF + 14 * 16 * 2048;
constexpr unsigned WS_END = OF_OFF + 64 * 16 * 2048;
static_assert(GG_OFF + 2048 * 8 <= QF_OFF && WS_END <= SET_STRIDE, "workspace map");

constexpr int L_X = 0;
constexpr int L_WQ = 114688;
constexpr int L_W1 = 114688;
constexpr int L_CTRL = 163776;
constexpr int L_W2 = 0;
constexpr int L_CMB = 98304;
constexpr int LDS_BYTES = 163840;

constexpr float QSCALE = 0.18898223650461362f * 1.4426950408889634f;
constexpr float LOG2E = 1.4426950408889634f;
constexpr float SAFE_BOUND = 60.0f;
constexpr unsigned SPIN_CAP = 1u << 18;
#define KVD 4
#define ROTA 0
#define ROT2 0
#define PDELAY 16
#define ODELAY 28
#define FDELAY 18
#define CODE_PF 44032
#define MPF 1
#define POLL_A 4

struct Params {
  const unsigned short* x;
  const unsigned short* wqkv;
  const unsigned short* wo;
  const unsigned short* w1;
  const unsigned short* w2;
  const unsigned short* gw;
  unsigned short* out;
  unsigned char* ws;
};
typedef unsigned long long u64;
typedef __attribute__((address_space(1))) u64 gu64;

DI unsigned cvtpk(float lo, float hi) {
  typedef __bf16 bf2 __attribute__((ext_vector_type(2)));
  typedef float f2 __attribute__((ext_vector_type(2)));
  f2 v = {lo, hi};
  bf2 r = __builtin_convertvector(v, bf2);
  return __builtin_bit_cast(unsigned, r);
}
DI bf16x8 pack8(const f32x16& x, int s) {
  u32x4 p;
  p[0] = cvtpk(x[8 * s + 0], x[8 * s + 1]);
  p[1] = cvtpk(x[8 * s + 2], x[8 * s + 3]);
  p[2] = cvtpk(x[8 * s + 4], x[8 * s + 5]);
  p[3] = cvtpk(x[8 * s + 6], x[8 * s + 7]);
  return __builtin_bit_cast(bf16x8, p);
}
DI float swap_half(float v) {
  auto r = __builtin_amdgcn_permlane32_swap(__float_as_uint(v), __float_as_uint(v), false, false);
  return (threadIdx.x & 32) ? __uint_as_float(r[0]) : __uint_as_float(r[1]);
}
template <int CTRL> DI float dpp_mov(float v) {
  return __uint_as_float(__builtin_amdgcn_update_dpp(__float_as_uint(v), __float_as_uint(v), CTRL, 0xf, 0xf, false));
}
DI float row16_max(float v) {
  v = fmaxf(v, dpp_mov<0x128>(v));
  v = fmaxf(v, dpp_mov<0x124>(v));
  v = fmaxf(v, dpp_mov<0x122>(v));
  v = fmaxf(v, dpp_mov<0x121>(v));
  return v;
}
DI unsigned row16_umax(unsigned v) {
  v = max(v, (unsigned)__builtin_amdgcn_update_dpp(v, v, 0x128, 0xf, 0xf, false));
  v = max(v, (unsigned)__builtin_amdgcn_update_dpp(v, v, 0x124, 0xf, 0xf, false));
  v = max(v, (unsigned)__builtin_amdgcn_update_dpp(v, v, 0x122, 0xf, 0xf, false));
  v = max(v, (unsigned)__builtin_amdgcn_update_dpp(v, v, 0x121, 0xf, 0xf, false));
  return v;
}
DI float rdlane(float v, int l) { return __uint_as_float(__builtin_amdgcn_readlane(__float_as_uint(v), l)); }
DI s16x4 tr_read(const LAS unsigned char* p) {
  typedef short v4i16_t __attribute__((ext_vector_type(4)));
  return __builtin_bit_cast(s16x4, __builtin_amdgcn_ds_read_tr16_b64_v4i16((LAS v4i16_t*)p));
}
DI bf16x8 tr_frag(const LAS unsigned char* p, int row_stride4) {
  s16x4 lo = tr_read(p), hi = tr_read(p + row_stride4);
  return __builtin_shufflevector(lo, hi, 0, 1, 2, 3, 4, 5, 6, 7);
}
DI __amdgpu_buffer_rsrc_t mk_rsrc(const void* p, unsigned bytes) {
  return __builtin_amdgcn_make_buffer_rsrc((void*)p, (short)0, (int)bytes, 0x00020000);
}
DI void glds16(__amdgpu_buffer_rsrc_t rs, unsigned dst, unsigned voff, unsigned soff) {
  asm volatile("s_mov_b32 m0, %0\n\ts_nop 0\n\tbuffer_load_dwordx4 %1, %2, %3 offen lds" ::"s"(dst), "v"(voff), "s"(rs), "s"(soff) : "memory");
}
DI u32x4 ld16(__amdgpu_buffer_rsrc_t rs, unsigned voff, unsigned soff) { return __builtin_amdgcn_raw_buffer_load_b128(rs, voff, soff, 0); }
DI u32x4 ld16c(__amdgpu_buffer_rsrc_t rs, unsigned voff, unsigned soff) { return __builtin_amdgcn_raw_buffer_load_b128(rs, voff, soff, 16); }
DI void st16(__amdgpu_buffer_rsrc_t rs, u32x4 v, unsigned voff, unsigned soff) { __builtin_amdgcn_raw_buffer_store_b128(v, rs, voff + soff, 0, 16); }
DI bf16x8 as_frag(u32x4 v) { return __builtin_bit_cast(bf16x8, v); }
DI u32x4 as_u4(bf16x8 v) { return __builtin_bit_cast(u32x4, v); }
template <int N> DI void wait_bar() { asm volatile("s_waitcnt vmcnt(%0) lgkmcnt(0)\n\ts_barrier" ::"n"(N) : "memory"); }

template <int V> struct IC { static constexpr int value = V; };
template <int I, int N, class Fn> DI void static_for(Fn&& fn) {
  if constexpr (I < N) { fn(IC<I>{}); static_for<I + 1, N>(fn); }
}
DI void drain() { asm volatile("s_waitcnt vmcnt(0)" ::: "memory"); }
template <int N> DI void wait_vm() { asm volatile("s_waitcnt vmcnt(%0)" ::"n"(N) : "memory"); }
DI void put_granule(unsigned char* ws, unsigned off, u64 tag16, unsigned payload) {
  __hip_atomic_store((gu64*)(ws + off), tag16 | (u64)(payload & 0xffffu), __ATOMIC_RELAXED, __HIP_MEMORY_SCOPE_AGENT);
}
template <int SLEEP> DI unsigned poll_granule(unsigned char* ws, unsigned off, u64 tag16) {
  u64 v = ~tag16;
  for (unsigned spins = 0;; ++spins) {
    if (v - tag16 > 0xffffull) v = __hip_atomic_load((gu64*)(ws + off), __ATOMIC_RELAXED, __HIP_MEMORY_SCOPE_AGENT);
    if (__all(v - tag16 <= 0xffffull) || spins > SPIN_CAP) break;
    __builtin_amdgcn_s_sleep(SLEEP);
  }
  __builtin_amdgcn_fence(__ATOMIC_ACQUIRE, "wavefront");
  return (unsigned)v & 0xffffu;
}
DI unsigned wait_lds_word(LAS unsigned char* p, unsigned want) {
  unsigned v = 0;
#pragma nounroll
  for (unsigned spins = 0; spins < (1u << 24); ++spins) {
    v = __builtin_amdgcn_readfirstlane(__hip_atomic_load((LAS unsigned*)p, __ATOMIC_RELAXED, __HIP_MEMORY_SCOPE_WORKGROUP));
    if (v >= want) break;
    __builtin_amdgcn_s_sleep(1);
  }
  asm volatile("" ::: "memory");
  return v;
}
template <int N> DI u64 wait_bar_epoch(const unsigned char* p) {
  u64 ep;
  asm volatile("s_load_dwordx2 %0, %1, 0x0\n\ts_waitcnt vmcnt(%2) lgkmcnt(0)\n\ts_barrier" : "=&s"(ep) : "s"(p), "n"(N) : "memory");
  return ep;
}
DI void bar_lds() { asm volatile("s_waitcnt lgkmcnt(0)\n\ts_barrier" ::: "memory"); }
DI void st_tile(LAS unsigned char* p, const f32x16& a) {
#pragma unroll
  for (int g = 0; g < 4; ++g) *(LAS f32x4*)(p + g * 1024) = f32x4{a[4 * g], a[4 * g + 1], a[4 * g + 2], a[4 * g + 3]};
}
DI void add_tile(f32x16& a, const LAS unsigned char* p) {
#pragma unroll
  for (int g = 0; g < 4; ++g) {
    const f32x4 v = *(const LAS f32x4*)(p + g * 1024);
    a[4 * g] += v[0]; a[4 * g + 1] += v[1]; a[4 * g + 2] += v[2]; a[4 * g + 3] += v[3];
  }
}

#define STAMP_DECL do {} while (0)
#define STAMP(id) do {} while (0)
#define QSTAMP(id) do {} while (0)
#define RSTAMP(id) do {} while (0)
#define STAMP_FLUSH(P, ph) do {} while (0)

DI void lds_arrive(LAS unsigned char* p) {
  asm volatile("s_mov_b64 exec, 1\n\tds_add_u32 %0, %1\n\ts_mov_b64 exec, -1" ::"v"((unsigned)(uintptr_t)p), "v"(1u) : "memory");
}

#define PINV(x) asm volatile("" : "+v"(x))
#define PINS(x) asm volatile("" : "+s"(x))
DI float sumsq(const u32x4& a, const u32x4& b) {
  typedef __bf16 bf2 __attribute__((ext_vector_type(2)));
  float n0 = 0.f, n1 = 0.f;
#pragma unroll
  for (int i = 0; i < 4; ++i) {
    n0 = __builtin_amdgcn_fdot2_f32_bf16(__builtin_bit_cast(bf2, a[i]), __builtin_bit_cast(bf2, a[i]), n0, false);
    n1 = __builtin_amdgcn_fdot2_f32_bf16(__builtin_bit_cast(bf2, b[i]), __builtin_bit_cast(bf2, b[i]), n1, false);
  }
  return n0 + n1;
}

DI void role_a(const Params& P, LAS unsigned char* lds, const int wt, const unsigned code_word) {
  const int lane = threadIdx.x & 63, r = lane & 31, h = lane >> 5;
  const int b = blockIdx.x, xcd = b & 7, idx = b >> 3;
  const unsigned lo = lane * 16;
  STAMP_DECL;
  STAMP(0);
  __builtin_amdgcn_s_setprio(2);
  unsigned xtouch = 0;
  if (wt < 2) xtouch = __builtin_amdgcn_raw_buffer_load_b32(wt ? mk_rsrc(P.wqkv, DM * 3 * DM * 2) : mk_rsrc(P.x, SEQ * DM * 2),
                                                            (unsigned)(wt ? b * 37 + (lane < 37 ? lane : 0) : b * 56 + (lane < 56 ? lane : 0)) * 128, 0, 0);
  SBAR();
  f32x16 acc[4];
  const int rb = (xcd & 1) * 8 + (idx & 7), j = (xcd >> 1) * 4 + (idx >> 3);
  const int head = xcd * 2 + (idx & 1), qb = idx >> 1;
  unsigned sto[3];
#pragma unroll
  for (int ct = 0; ct < 3; ++ct) {
    const int u = 3 * j + ct, part = u >> 4, t = (u & 15) * 64 + rb * 4 + wt;
    sto[ct] = lo + (part == 0 ? QF_OFF + t * 2048 : KV_OFF + t * 4096 + (part - 1) * 2048);
    PINV(sto[ct]);
  }
  unsigned padv = r == DH ? 0x3f803f80u : 0u;
  PINV(padv);
  const int gq = head / 3, gk = (16 + head) / 3, gvv = (32 + head) / 3;
  unsigned pub_off = GQ_OFF + (j * 16 + rb) * GQW + wt * GQS + (lane & 1) * 8;
  unsigned poll_off = GQ_OFF + (h ? gq * 16 + qb : (lane < 16 ? gk : gvv) * 16 + wt * 4 + ((lane >> 2) & 3)) * GQW + (lane & 3) * GQS + h * 8;
  unsigned chk_off = GQ_OFF + (gk * 16 + (lane >> 2)) * GQW + (lane & 3) * GQS;
  unsigned g_off = GG_OFF + ((qb * 4 + wt) * 32 + r) * 8;
  unsigned o_off = lo + OF_OFF + ((qb * 4 + wt) * 16 + head) * 2048;
  PINV(pub_off); PINV(poll_off); PINV(chk_off); PINV(g_off); PINV(o_off);
  unsigned kvbase = KV_OFF + (head * 64 + wt * 16) * 4096, go_off = GO_OFF + ((qb * 4 + wt) * 16 + head) * 128;
  unsigned qofs[4];
#pragma unroll
  for (int t = 0; t < 4; ++t) { qofs[t] = QF_OFF + (unsigned)(head * 64 + qb * 4 + ((wt + 1 + t) & 3)) * 2048; PINS(qofs[t]); }
  PINS(kvbase); PINS(go_off);
  unsigned xoff[4];
#pragma unroll
  for (int ks = 0; ks < 4; ++ks) xoff[ks] = (unsigned)(L_X + wt * 4096 + r * 128 + (((2 * ks + h) ^ ((r >> 1) & 7)) << 4));
  const unsigned wqoff = (unsigned)(L_WQ + (8 * h + ((lane & 15) >> 2)) * 192 + ((lane >> 4) & 1) * 32 + (lane & 3) * 8);
  u64 ep;
  asm volatile("s_load_dwordx2 %0, %1, 0x0\n\ts_waitcnt lgkmcnt(0)" : "=&s"(ep) : "s"(P.ws + FL_OFF) : "memory");
  u64 tag16 = (ep + 1) << 16;
  unsigned char* wsb = P.ws + SET_BASE + (((unsigned)ep + 1u) & 3u) * SET_STRIDE;
  PINS(tag16); PINS(wsb);
  const __amdgpu_buffer_rsrc_t rsWs = mk_rsrc(wsb, WS_END);
#pragma unroll
  for (int i = 0; i < 3; ++i) acc[i] = f32x16{};
  unsigned wtouch = 0;
  auto kloop = [&](auto nvc) {
    constexpr int NV = decltype(nvc)::value;
    bf16x8 xf[4], wf[4][3];
    auto mm = [&](int ks, int ct) { acc[ct] = ct >= 3 - NV ? MFMA(xf[ks], wf[ks][ct], acc[ct]) : MFMA(wf[ks][ct], xf[ks], acc[ct]); };
    auto step = [&](int c, int ks, bool domm) {
      const int km = (ks + 2) & 3;
#pragma unroll
      for (int ct = 0; ct < 3; ++ct) {
        if (domm) mm(km, ct);
        SBAR();
        if (ct == 0) xf[ks] = *(const LAS bf16x8*)(lds + xoff[ks] + c * 16384);
        wf[ks][ct] = tr_frag(lds + wqoff + (c & 3) * 12288 + ks * 16 * 192 + ct * 56, 4 * 192);
        SBAR();
      }
    };
#pragma unroll
    for (int c = 0; c < 7; ++c) {
      bar_lds();
      if (c == 0) STAMP(1);
      if (c == MPF) {
        const unsigned l = (unsigned)(b * 128 + (wt & 1) * 64 + lane) * 128;
        if (wt < 2) wtouch = l < DM * FF * 2 ? __builtin_amdgcn_raw_buffer_load_b32(mk_rsrc(P.w1, DM * FF * 2), l, 0, 0)
                           : l < 2 * DM * FF * 2 ? __builtin_amdgcn_raw_buffer_load_b32(mk_rsrc(P.w2, DM * FF * 2), l - DM * FF * 2, 0, 0)
                                                 : __builtin_amdgcn_raw_buffer_load_b32(mk_rsrc(P.wo, DM * DM * 2), l - 2 * DM * FF * 2, 0, 0);
        SBAR();
      }
      QSTAMP(1 + c);
      step(c, 0, c > 0);
      step(c, 1, c > 0);
      step(c, 2, true);
      step(c, 3, true);
    }
    bar_lds();
    QSTAMP(8);
#pragma unroll
    for (int ct = 0; ct < 3; ++ct) { mm(2, ct); mm(3, ct); SBAR(); }
  };
  auto epi = [&](auto nqc, auto nvc) {
    constexpr int NQ = decltype(nqc)::value, NV = decltype(nvc)::value;
    STAMP(2);
    asm volatile("" ::"v"(code_word), "v"(wtouch), "v"(xtouch));
    u32x4 t0[3], t1[3];
#pragma unroll
    for (int ct = 0; ct < 3; ++ct) {
      if (ct < NQ) acc[ct] *= QSCALE;
      t0[ct] = as_u4(pack8(acc[ct], 0));
      if (ct >= 3 - NV) {
#pragma unroll
        for (int i = 0; i < 4; ++i) t0[ct][i] = r < DH ? t0[ct][i] : padv;
      }
      st16(rsWs, t0[ct], sto[ct], 0);
      t1[ct] = as_u4(pack8(acc[ct], 1));
      if (ct >= 3 - NV) {
#pragma unroll
        for (int i = 0; i < 4; ++i) t1[ct][i] = r < DH ? t1[ct][i] : padv;
      } else {
        t1[ct][2] = h ? (ct < NQ ? 0u : 0x3f803f80u) : t1[ct][2];
        t1[ct][3] = h ? (ct < NQ ? 0u : 0x00003f80u) : t1[ct][3];
      }
      st16(rsWs, t1[ct], sto[ct], 1024);
      SBAR();
    }
    STAMP(8);
    unsigned mq = 0, mk = 0;
#pragma unroll
    for (int ct = 0; ct < 3; ++ct) {
      const unsigned n = __float_as_uint(sumsq(t0[ct], t1[ct])) & 0x7fffffffu;
      if (ct < NQ) mq = max(mq, n); else mk = max(mk, n);
    }
    auto bound = [&](unsigned m) {
      m = row16_umax(m);
      const float lo2 = __uint_as_float(max((unsigned)__builtin_amdgcn_readlane(m, 0), (unsigned)__builtin_amdgcn_readlane(m, 16)));
      const float hi2 = __uint_as_float(max((unsigned)__builtin_amdgcn_readlane(m, 32), (unsigned)__builtin_amdgcn_readlane(m, 48)));
      return ((__float_as_uint(lo2 + hi2) >> 16) + 1u) & 0xffffu;
    };
    const unsigned pq = NQ > 0 ? bound(mq) : 0u, pk = NQ < 3 ? bound(mk) : 0u;
    u64 gran = tag16 | ((lane & 1) ? pq : pk);
    PINV(gran);
    drain();
    STAMP(9);
    if (lane < 2) __hip_atomic_store((gu64*)(wsb + pub_off), gran, __ATOMIC_RELAXED, __HIP_MEMORY_SCOPE_AGENT);
  };
  if (j < 10) {
    kloop(IC<0>{});
    if (j < 5) epi(IC<3>{}, IC<0>{});
    else if (j == 5) epi(IC<1>{}, IC<0>{});
    else epi(IC<0>{}, IC<0>{});
  } else if (j == 10) {
    kloop(IC<1>{});
    epi(IC<0>{}, IC<1>{});
  } else {
    kloop(IC<3>{});
    epi(IC<0>{}, IC<3>{});
  }
  STAMP(3);
  __builtin_amdgcn_s_sleep(PDELAY);
  const unsigned pay = poll_granule<POLL_A>(wsb, poll_off, tag16);
  STAMP(4);
  u32x4 q[4][2];
  LAS unsigned char* const cmb = lds + L_CMB + wt * 12288 + lo;
  u64 gv = 0;
  unsigned thr = 0;
#pragma nounroll
  for (int pass = 0;; ++pass) {
    u32x4 kv[KVD][4];
    kv[0][0] = ld16c(rsWs, lo, kvbase);
    SBAR();
    if (pass == 0) { q[0][0] = ld16c(rsWs, lo, qofs[0]); q[0][1] = ld16c(rsWs, lo + 1024, qofs[0]); }
    SBAR();
    kv[0][1] = ld16c(rsWs, lo + 1024, kvbase);
    SBAR();
    if (pass == 0) {
#pragma unroll
      for (int t = 1; t < 4; ++t) { q[t][0] = ld16c(rsWs, lo, qofs[t]); q[t][1] = ld16c(rsWs, lo + 1024, qofs[t]); SBAR(); }
    }
#pragma unroll
    for (int e = 2; e < 4 * (KVD - 1); ++e) { kv[e >> 2][e & 3] = ld16c(rsWs, lo + (e & 3) * 1024, kvbase + (e >> 2) * 4096); SBAR(); }
#pragma unroll
    for (int i = 0; i < 4; ++i) acc[i] = f32x16{};
    {
      const float q2 = __uint_as_float(__builtin_amdgcn_readlane(row16_umax(pay), 32) << 16);
      const float lim = SAFE_BOUND * SAFE_BOUND * __builtin_amdgcn_rcpf(q2);
      thr = lim < 1e30f ? __float_as_uint(lim) >> 16 : (lim >= 1e30f ? 0x7000u : 0u);
      PINV(thr);
    }
    __builtin_amdgcn_s_setprio(1);
    f32x16 sc = MFMA(as_frag(kv[0][0]), as_frag(q[0][0]), f32x16{});
    sc = MFMA(as_frag(kv[0][1]), as_frag(q[0][1]), sc);
    STAMP(5);
    u32x4 p0, p1 = {0, 0, 0, 0};
    float e0 = 0.f, e1 = 0.f, e2 = 0.f, e3 = 0.f;
    u64 ck = 0, cv = 0;
#define EXP4(i) do { e0 = __builtin_amdgcn_exp2f(sc[i]); e1 = __builtin_amdgcn_exp2f(sc[(i) + 1]); e2 = __builtin_amdgcn_exp2f(sc[(i) + 2]); e3 = __builtin_amdgcn_exp2f(sc[(i) + 3]); \
    asm volatile("" : "+v"(e0), "+v"(e1), "+v"(e2), "+v"(e3)); SBAR(); } while (0)
#define CVT2(P, i) do { P[i] = cvtpk(e0, e1); P[(i) + 1] = cvtpk(e2, e3); asm volatile("" : "+v"(P)); SBAR(); } while (0)
#define PART(t, g) do { *(LAS f32x4*)(cmb + (t) * 4096 + (g) * 1024) = f32x4{acc[t][4 * (g)], acc[t][4 * (g) + 1], acc[t][4 * (g) + 2], acc[t][4 * (g) + 3]}; SBAR(); } while (0)
    static_for<0, 64>([&](auto ic) {
      constexpr int i = decltype(ic)::value;
      constexpr int kt = i >> 2, t = i & 3, kn_ = (i + 1) >> 2, tn = (i + 1) & 3, kp = (i - 1) >> 2, tp = (i - 1) & 3;
      constexpr int kl = kt + KVD - 1;
      if (i == 24) STAMP(11);
      if (i == 56) (void)wait_lds_word(lds + L_CTRL, 4);
      f32x16 sn;
      SBAR();
      if (i < 63) sn = MFMA(as_frag(kv[kn_ % KVD][0]), as_frag(q[tn][0]), f32x16{});
      SBAR();
      if constexpr (i >= 62) PART(i - 62, 0);
      if (i > 0) CVT2(p1, 2);
      EXP4(0);
      if (i > 0) acc[tp] = MFMA(as_frag(kv[(kp + KVD) % KVD][3]), as_frag(p1), acc[tp]);
      SBAR();
      if (kl < 16) kv[kl % KVD][t] = ld16c(rsWs, lo + t * 1024, kvbase + kl * 4096);
      if (i == 52) gv = __hip_atomic_load((gu64*)(wsb + g_off), __ATOMIC_RELAXED, __HIP_MEMORY_SCOPE_AGENT);
      if (i == 53) ck = __hip_atomic_load((gu64*)(wsb + chk_off), __ATOMIC_RELAXED, __HIP_MEMORY_SCOPE_AGENT);
      if (i == 54) cv = __hip_atomic_load((gu64*)(wsb + (gvv - gk) * 16 * GQW + chk_off), __ATOMIC_RELAXED, __HIP_MEMORY_SCOPE_AGENT);
      SBAR();
      if constexpr (i >= 62) PART(i - 62, 1);
      CVT2(p0, 0);
      EXP4(4);
      if (i < 63) sn = MFMA(as_frag(kv[kn_ % KVD][1]), as_frag(q[tn][1]), sn);
      SBAR();
      if constexpr (i >= 62) PART(i - 62, 2);
      CVT2(p0, 2);
      EXP4(8);
      acc[t] = MFMA(as_frag(kv[kt % KVD][2]), as_frag(p0), acc[t]);
      SBAR();
      if constexpr (i >= 62) PART(i - 62, 3);
      CVT2(p1, 0);
      EXP4(12);
      if (i < 63) sc = sn;
    });
    CVT2(p1, 2);
    acc[3] = MFMA(as_frag(kv[15 % KVD][3]), as_frag(p1), acc[3]);
    SBAR();
    PART(2, 0); PART(2, 1); PART(2, 2); PART(2, 3);
#undef EXP4
#undef CVT2
#undef PART
    __builtin_amdgcn_s_setprio(2);
    if (__builtin_expect(__all(ck - tag16 <= (u64)thr && cv - tag16 <= 0x7000ull && gv - tag16 <= 0xffffull), 1)) break;
    gv = poll_granule<2>(wsb, g_off, tag16);
    if (pass) break;
    const unsigned pk = poll_granule<2>(wsb, chk_off, tag16), pv = poll_granule<2>(wsb, (gvv - gk) * 16 * GQW + chk_off, tag16);
    if (__all(pk <= thr && pv <= 0x7000u)) break;
    float mx[4];
#pragma unroll
    for (int t = 0; t < 4; ++t) mx[t] = -INFINITY;
    for (int kt = 0; kt < 64; ++kt) {
      const u32x4 k0 = ld16c(rsWs, lo, KV_OFF + (head * 64 + kt) * 4096), k1 = ld16c(rsWs, lo + 1024, KV_OFF + (head * 64 + kt) * 4096);
#pragma unroll
      for (int t = 0; t < 4; ++t) {
        f32x16 s = MFMA(as_frag(k0), as_frag(q[t][0]), f32x16{});
        s = MFMA(as_frag(k1), as_frag(q[t][1]), s);
#pragma unroll
        for (int i = 0; i < 16; ++i) mx[t] = fmaxf(mx[t], s[i]);
      }
    }
#pragma unroll
    for (int t = 0; t < 4; ++t) {
      const float nm = -fmaxf(mx[t], swap_half(mx[t]));
      const unsigned hi = __float_as_uint(nm) & 0xffff0000u;
      const float r1 = nm - __uint_as_float(hi);
      const unsigned mid = __float_as_uint(r1) & 0xffff0000u;
      const float r2 = r1 - __uint_as_float(mid);
      if (h) { q[t][1][2] = (hi >> 16) | mid; q[t][1][3] = __float_as_uint(r2) >> 16; }
    }
  }
  STAMP(6);
  asm volatile("s_waitcnt lgkmcnt(0)" ::: "memory");
  lds_arrive(lds + L_CTRL + 20);
  (void)wait_lds_word(lds + L_CTRL + 20, 4);
  f32x16 o = acc[3];
#pragma unroll
  for (int u = 0; u < 3; ++u) add_tile(o, lds + L_CMB + ((wt + 3 - u) & 3) * 12288 + u * 4096 + lo);
  const auto lsw = __builtin_amdgcn_permlane32_swap(__float_as_uint(o[12]), __float_as_uint(o[12]), false, false);
  const float inv = __uint_as_float((unsigned)gv << 16) * __builtin_amdgcn_rcpf(__uint_as_float(lsw[1]));
  o *= inv;
  st16(rsWs, as_u4(pack8(o, 0)), o_off, 0);
  u32x4 o1 = as_u4(pack8(o, 1));
  o1[2] = h ? 0u : o1[2];
  o1[3] = h ? 0u : o1[3];
  st16(rsWs, o1, o_off, 1024);
  STAMP(7);
  drain();
  if (lane == 0) __hip_atomic_store((gu64*)(wsb + go_off), tag16, __ATOMIC_RELAXED, __HIP_MEMORY_SCOPE_AGENT);
  STAMP(10);
  STAMP_FLUSH(P, 0);
}

constexpr int W2S = 6;
constexpr int HPRE = 5;
#define LEA 5
#define LEB 11
#define LE0 16
#define LU0 3
#define F1LA 2
#define PACE 4
#define PACE2 2
constexpr int ffn2_ops(int f) { return (f + W2S < 14 ? 4 : 0) + 4; }
constexpr int ffn2_younger(int f, int upto) {
  int n = 4;
  for (int i = f - W2S + 1; i < upto; ++i) n += ffn2_ops(i);
  return n;
}

DI void role_f(const Params& P, LAS unsigned char* lds, const int wt) {
  u64 ep;
  const int lane = threadIdx.x & 63, r = lane & 31, h = lane >> 5;
  const int b = blockIdx.x, xcd = b & 7, idx = b >> 3;
  const int rb = (xcd & 1) * 8 + (idx & 7), j = (xcd >> 1) * 4 + (idx >> 3);
  const unsigned lo = lane * 16, lds0 = (unsigned)(uintptr_t)lds;
  const bool ffn = j < 14;
  STAMP_DECL;
  STAMP(0);
  f32x16 acc[4];
  u32x4 wl[28];
  const __amdgpu_buffer_rsrc_t rsL = mk_rsrc(ffn ? P.w1 : P.gw, ffn ? DM * FF * 2 : DM * 2);
  const unsigned lsrc = ffn ? (unsigned)((lane >> 2) * (FF * 2) + (j * 4 + wt) * 64 + (lane & 3) * 16) : (unsigned)(h * 16);
  const unsigned lstep = ffn ? 16 * FF * 2 : 32;
  const int rot = ROT1(rb, j);
  auto kc = [&](int c) { const int v = c + rot; return v >= 7 ? v - 7 : v; };
  auto L = [&](int u0, int u1) {
#pragma unroll
    for (int u = u0; u < u1; ++u) wl[u] = ld16(rsL, lsrc, (kc(u >> 2) * 4 + (u & 3)) * lstep);
    SBAR();
  };
  {
    const __amdgpu_buffer_rsrc_t rsX = mk_rsrc(P.x, SEQ * DM * 2);
    const __amdgpu_buffer_rsrc_t rsW = mk_rsrc(P.wqkv, DM * 3 * DM * 2);
    unsigned xsrc[2], wsrc[3];
#pragma unroll
    for (int i = 0; i < 2; ++i) {
      const int row = 32 * wt + 8 * i + (lane >> 3), ch = (lane & 7) ^ ((row >> 1) & 7);
      xsrc[i] = (unsigned)((rb * 128 + row) * (DM * 2) + ch * 16);
    }
#pragma unroll
    for (int i = 0; i < 3; ++i) {
      const int p = 64 * (3 * wt + i) + lane, kr = p / 12, ch = p % 12;
      wsrc[i] = (unsigned)(kr * (3 * DM * 2) + j * 168 + (ch < 11 ? ch : 10) * 16);
    }
    auto X = [&](int c) {
#pragma unroll
      for (int i = 0; i < 4; ++i) glds16(rsX, lds0 + L_X + c * 16384 + (4 * wt + i) * 1024, xsrc[i & 1], kc(c) * 128 + (i >> 1) * 16 * DM * 2);
    };
    auto W = [&](int c) {
#pragma unroll
      for (int i = 0; i < 3; ++i) glds16(rsW, lds0 + L_WQ + (c & 3) * 12288 + (3 * wt + i) * 1024, wsrc[i], kc(c) * 64 * 3 * DM * 2);
    };
    X(0); W(0); X(1); W(1); X(2);
    QSTAMP(1);
    wait_bar<11>();
    STAMP(1);
    W(2); X(3); W(3);
    QSTAMP(2);
    wait_bar<14>();
    X(4); W(4);
    QSTAMP(3);
    wait_bar<14>();
    X(5); W(5);
    QSTAMP(4);
    wait_bar<14>();
    X(6); W(6);
    QSTAMP(5);
    wait_bar<14>();
    if (wt == 0 && lane < 8) *(LAS unsigned*)(lds + L_CTRL + lane * 4) = 0u;
    L(0, LEA);
    QSTAMP(6);
    ep = wait_bar_epoch<7 + LEA>(P.ws + FL_OFF);
    L(LEA, LEB);
    QSTAMP(7);
    wait_bar<LEB>();
    L(LEB, LE0);
    QSTAMP(8);
    bar_lds();
    __builtin_amdgcn_s_sleep(FDELAY);
  }
  STAMP(2);
  const u64 tag = (ep + 1) << 16;
  unsigned char* const wsb = P.ws + SET_BASE + (((unsigned)ep + 1u) & 3u) * SET_STRIDE;
  const __amdgpu_buffer_rsrc_t rsWs = mk_rsrc(wsb, WS_END);
  unsigned xoff[4];
#pragma unroll
  for (int ks = 0; ks < 4; ++ks) xoff[ks] = (unsigned)(L_X + r * 128 + (((2 * ks + h) ^ ((r >> 1) & 7)) << 4));
  if (ffn) {
    LAS unsigned char* ring = lds + L_W1 + wt * 8192;
    const unsigned troff = (unsigned)((8 * h + ((lane & 15) >> 2)) * 64 + ((lane >> 4) & 1) * 32 + (lane & 3) * 8);
#pragma unroll
    for (int i = 0; i < 4; ++i) acc[i] = f32x16{};
    RSTAMP(1);
    constexpr int LA = F1LA;
    bf16x8 xf[LA + 1][4], wf[LA + 1];
    auto wr = [&](int u) { *(LAS u32x4*)(ring + (u & 7) * 1024 + lo) = wl[u]; };
    auto rdw = [&](int u) { wf[u % (LA + 1)] = tr_frag(ring + (u & 7) * 1024 + troff, 4 * 64); };
    auto rdx = [&](int u, int t) { xf[u % (LA + 1)][t] = *(const LAS bf16x8*)(lds + xoff[u & 3] + (u >> 2) * 16384 + t * 4096); };
#pragma unroll
    for (int u = 0; u < LA + 2; ++u) wr(u);
#pragma unroll
    for (int u = 0; u < LA; ++u) {
      rdw(u);
#pragma unroll
      for (int t = 0; t < 4; ++t) rdx(u, t);
    }
    SBAR();
#pragma unroll
    for (int u = 0; u < 28; ++u) {
      if ((u & 3) == 0) RSTAMP(2 + (u >> 2));
#pragma unroll
      for (int t = 0; t < 4; ++t) {
        if (u + LA < 28) {
          if (t == 1) rdw(u + LA);
          rdx(u + LA, t);
          if (t == 3 && u + LA + 2 < 28) wr(u + LA + 2);
        }
        SBAR();
        acc[t] = MFMA(wf[u % (LA + 1)], xf[u % (LA + 1)][t], acc[t]);
        SBAR();
        if (u >= LU0 && LE0 + (u - LU0) * 4 + t < 28) L(LE0 + (u - LU0) * 4 + t, LE0 + (u - LU0) * 4 + t + 1);
      }
    }
    lds_arrive(lds + L_CTRL);
    RSTAMP(9);
    STAMP(3);
    __builtin_amdgcn_s_setprio(3);
#pragma unroll
    for (int t = 0; t < 4; ++t) {
      f32x16 z = acc[t];
#pragma unroll
      for (int i = 0; i < 16; ++i) z[i] = z[i] * __builtin_amdgcn_rcpf(1.f + __builtin_amdgcn_exp2f(-LOG2E * z[i]));
      const unsigned so = HF_OFF + ((rb * 4 + t) * 56 + j * 4 + wt) * 2048;
      st16(rsWs, as_u4(pack8(z, 0)), lo, so);
      st16(rsWs, as_u4(pack8(z, 1)), lo + 1024, so);
    }
    drain();
    if (lane == 0) put_granule(wsb, GH_OFF + (rb * 16 + j) * 128 + wt * 8, tag, 0);
  } else {
    __builtin_amdgcn_s_setprio(3);
    if (j == 14) {
      L(LE0, 28);
      acc[0] = f32x16{};
#pragma unroll
      for (int u = 0; u < 28; ++u) {
        const bf16x8 xf = *(const LAS bf16x8*)(lds + xoff[u & 3] + (u >> 2) * 16384 + wt * 4096);
        acc[0] = MFMA(as_frag(wl[u]), xf, acc[0]);
      }
      const float g = __builtin_amdgcn_rcpf(1.f + __builtin_amdgcn_exp2f(-LOG2E * acc[0][0]));
      if (h == 0) put_granule(wsb, GG_OFF + ((rb * 4 + wt) * 32 + r) * 8, tag, cvtpk(g, 0.f));
    }
    lds_arrive(lds + L_CTRL);
    const __amdgpu_buffer_rsrc_t rsO = mk_rsrc(P.wo, DM * DM * 2);
    const int id0 = ((j - 14) * 16 + rb) * 4 + wt;
    for (int id = id0; id < 224; id += 128) {
      const int nt = id >> 4, hd = id & 15;
      unsigned rp[16];
#pragma unroll
      for (int e = 0; e < 16; ++e)
        rp[e] = __builtin_amdgcn_raw_buffer_load_b16(rsO, (unsigned)(((4 * h + 8 * (e >> 2) + (e & 3)) * DM + r) * 2), (unsigned)((DH * hd * DM + 32 * nt) * 2), 0);
      u32x4 a, c2;
#pragma unroll
      for (int i = 0; i < 4; ++i) {
        a[i] = (rp[2 * i] & 0xffffu) | (rp[2 * i + 1] << 16);
        c2[i] = (rp[8 + 2 * i] & 0xffffu) | (rp[8 + 2 * i + 1] << 16);
      }
      c2[2] = h ? 0u : c2[2];
      c2[3] = h ? 0u : c2[3];
      st16(rsWs, a, lo, WOF_OFF + id * 2048);
      st16(rsWs, c2, lo + 1024, WOF_OFF + id * 2048);
      drain();
      if (lane == 0) put_granule(wsb, GW_OFF + id * 8, tag, 0);
    }
  }
  STAMP(4);
  if (idx >= 28) {
    if (b == 255 && wt == 0) {
      __builtin_amdgcn_s_sleep(127);
#pragma unroll
      for (int i = 0; i < 4; ++i) (void)poll_granule<64>(wsb, GQ_OFF + (i * 64 + lane) * GQW, tag);
      if (lane == 0) __hip_atomic_store((gu64*)(P.ws + FL_OFF), ep + 1, __ATOMIC_RELAXED, __HIP_MEMORY_SCOPE_AGENT);
    }
    STAMP(10);
    STAMP_FLUSH(P, 0);
    return;
  }
  const int mb = 4 * xcd + (idx & 3), nb = idx >> 2;
  const __amdgpu_buffer_rsrc_t rsW2 = mk_rsrc(P.w2, FF * DM * 2), rsH = rsWs;
  const unsigned w2src = (unsigned)((wt * 448 + (lane >> 3)) * (DM * 2) + nb * 128 + ((((lane >> 2) & 1) ^ ((lane >> 4) & 1)) * 64) + (lane & 3) * 16);
  const unsigned ring0 = lds0 + L_W2 + wt * 24576;
  const int rot2 = ROT2;
  auto fr = [&](int f) { const int v = f + rot2; return v >= 14 ? v - 14 : v; };
  auto D = [&](int f) {
#pragma unroll
    for (int i = 0; i < 4; ++i) glds16(rsW2, ring0 + (f % W2S) * 4096 + i * 1024, w2src, (fr(f) * 32 + i * 8) * DM * 2);
  };
  (void)wait_lds_word(lds + L_CTRL, 4);
#pragma unroll
  for (int f = 0; f < W2S; ++f) { D(f); __builtin_amdgcn_s_sleep(PACE); }
  const int ftl = wt * 14 + ((lane & 15) < 14 ? (lane & 15) : 0);
  (void)poll_granule<8>(wsb, (lane & 16) ? GW_OFF + ((nb * 2 + ((lane >> 2) & 1)) * 16 + wt * 4 + (lane & 3)) * 8
                                        : GH_OFF + ((mb >> 1) * 16 + (ftl >> 2)) * 128 + (ftl & 3) * 8, tag);
  STAMP(5);
  u32x4 hb[HPRE][4], wo[16];
  const unsigned hbase = HF_OFF + (mb * 2 * 56 + wt * 14) * 2048;
  auto H = [&](int f) {
#pragma unroll
    for (int e = 0; e < 4; ++e) hb[f % HPRE][e] = ld16c(rsH, lo + (e & 1) * 1024, hbase + ((e >> 1) * 56 + fr(f)) * 2048);
  };
#pragma unroll
  for (int f = 0; f < HPRE; ++f) { H(f); SBAR(); }
  const float gl = __uint_as_float(poll_granule<8>(wsb, GG_OFF + (mb * 64 + lane) * 8, tag) << 16), gs = swap_half(gl);
  const float og0 = 1.f - (h ? gs : gl), og1 = 1.f - (h ? gl : gs);
  const int q4 = (lane & 15) >> 2;
  unsigned woff[2];
#pragma unroll
  for (int nt = 0; nt < 2; ++nt) woff[nt] = (unsigned)(L_W2 + wt * 24576 + (4 * h + q4) * 128 + ((nt ^ (q4 >> 1)) * 64) + ((lane >> 4) & 1) * 32 + (lane & 3) * 8);
#pragma unroll
  for (int i = 0; i < 4; ++i) acc[i] = f32x16{};
  STAMP(6);
  __builtin_amdgcn_s_setprio(0);
  bf16x8 wf[2][2][2];
  auto RW = [&](int f) {
#pragma unroll
    for (int s = 0; s < 2; ++s)
#pragma unroll
      for (int nt = 0; nt < 2; ++nt) wf[f & 1][s][nt] = tr_frag(lds + woff[nt] + (f % W2S) * 4096 + s * 2048, 1024);
  };
  RW(0);
  static_for<0, 14>([&](auto fc) {
    constexpr int f = decltype(fc)::value;
    asm volatile("s_waitcnt lgkmcnt(0)" ::: "memory");
    SBAR();
    if constexpr (f + 1 < 14) {
      if constexpr (f + 1 >= W2S) wait_vm<ffn2_younger(f + 1, f)>();
      RW(f + 1);
    }
    SBAR();
#pragma unroll
    for (int s = 0; s < 2; ++s)
#pragma unroll
      for (int nt = 0; nt < 2; ++nt)
#pragma unroll
        for (int mt = 0; mt < 2; ++mt) {
          acc[nt * 2 + mt] = MFMA(wf[f & 1][s][nt], as_frag(hb[f % HPRE][mt * 2 + s]), acc[nt * 2 + mt]);
        }
    SBAR();
    if constexpr (f + W2S < 14) D(f + W2S);
    if constexpr (f + HPRE < 14) H(f + HPRE);
    else if constexpr (f + HPRE < 18) {
      constexpr int e0 = (f + HPRE - 14) * 4;
#pragma unroll
      for (int e = e0; e < e0 + 4; ++e) wo[e] = ld16c(rsWs, lo, WOF_OFF + ((nb * 2 + (e >> 3)) * 16 + wt * 4) * 2048 + (e & 7) * 1024);
    }
    SBAR();
    __builtin_amdgcn_s_sleep(PACE2);
    SBAR();
  });
  STAMP(7);
  __builtin_amdgcn_s_setprio(3);
#pragma unroll
  for (int i = 0; i < 16; ++i) { acc[0][i] *= og0; acc[1][i] *= og1; acc[2][i] *= og0; acc[3][i] *= og1; }
  (void)wait_lds_word(lds + L_CTRL + 20, 1);
  __builtin_amdgcn_s_sleep(ODELAY);
  (void)poll_granule<POLL_A>(wsb, GO_OFF + ((mb * 2 + ((lane >> 2) & 1)) * 16 + wt * 4 + (lane & 3)) * 128, tag);
  STAMP(8);
  u32x4 of[16];
#pragma unroll
  for (int e = 0; e < 8; ++e)
#pragma unroll
    for (int mt = 0; mt < 2; ++mt) of[mt * 8 + e] = ld16c(rsWs, lo, OF_OFF + ((mb * 2 + mt) * 16 + wt * 4) * 2048 + e * 1024);
  SBAR();
#pragma unroll
  for (int e = 0; e < 8; ++e)
#pragma unroll
    for (int nt = 0; nt < 2; ++nt)
#pragma unroll
      for (int mt = 0; mt < 2; ++mt) acc[nt * 2 + mt] = MFMA(as_frag(wo[nt * 8 + e]), as_frag(of[mt * 8 + e]), acc[nt * 2 + mt]);
#pragma unroll
  for (int t = 0; t < 4; ++t) st_tile(lds + L_W2 + wt * 24576 + t * 4096 + lo, acc[t]);
  STAMP(9);
  bar_lds();
  f32x16 a = f32x16{};
#pragma unroll
  for (int f = 0; f < 4; ++f) add_tile(a, lds + L_W2 + f * 24576 + wt * 4096 + lo);
  unsigned short* dst = P.out + (size_t)((mb * 2 + (wt & 1)) * 32 + r) * DM + (nb * 2 + (wt >> 1)) * 32 + 8 * h;
#pragma unroll
  for (int k = 0; k < 2; ++k) {
    const auto x = __builtin_amdgcn_permlane32_swap(cvtpk(a[8 * k], a[8 * k + 1]), cvtpk(a[8 * k + 4], a[8 * k + 5]), false, false);
    const auto y = __builtin_amdgcn_permlane32_swap(cvtpk(a[8 * k + 2], a[8 * k + 3]), cvtpk(a[8 * k + 6], a[8 * k + 7]), false, false);
    *(u32x4*)(dst + 16 * k) = u32x4{x[0], y[0], x[1], y[1]};
  }
  STAMP(10);
  STAMP_FLUSH(P, 0);
}

extern __shared__ __attribute__((aligned(16))) unsigned char g_lds[];

__global__ __launch_bounds__(512, 2) void k_fused(Params P) {
  u64 pc;
  asm volatile("s_getpc_b64 %0" : "=s"(pc));
  const unsigned code_word = __builtin_amdgcn_raw_buffer_load_b32(mk_rsrc((const void*)(pc & ~63ull), CODE_PF),
      (threadIdx.x & 256) * 4096 + (((blockIdx.x >> 3) * 32 + ((threadIdx.x >> 3) & 24) + (threadIdx.x & 7)) * 64), 0, 0);
  SBAR();
  LAS unsigned char* lds = (LAS unsigned char*)g_lds;
  const int w = __builtin_amdgcn_readfirstlane(threadIdx.x >> 6);
  if (w < 4) role_a(P, lds, w, code_word);
  else role_f(P, lds, w - 4);
}

extern "C" void kernel_launch(void* const* d_in, const int* in_sizes, int n_in, void* d_out, int out_size, void* d_ws,
                              size_t ws_size, hipStream_t stream) {
  Params P;
  P.x = (const unsigned short*)d_in[0];
  P.wqkv = (const unsigned short*)d_in[1];
  P.wo = (const unsigned short*)d_in[2];
  P.w1 = (const unsigned short*)d_in[4];
  P.w2 = (const unsigned short*)d_in[5];
  P.gw = (const unsigned short*)d_in[6];
  P.out = (unsigned short*)d_out;
  P.ws = (unsigned char*)d_ws;
  (void)hipFuncSetAttribute((const void*)k_fused, hipFuncAttributeMaxDynamicSharedMemorySize, LDS_BYTES);
  k_fused<<<256, 512, LDS_BYTES, stream>>>(P);
}
```

```cpp
#define XSTAMP 0
#define XWAVE 1
#define XBLK 9
#include <hip/hip_runtime.h>
#include <hip/hip_bf16.h>
#include <stdint.h>

typedef short bf16x8 __attribute__((ext_vector_type(8)));
typedef short s16x4 __attribute__((ext_vector_type(4)));
typedef float f32x16 __attribute__((ext_vector_type(16)));
typedef float f32x4 __attribute__((ext_vector_type(4)));
typedef unsigned u32x4 __attribute__((ext_vector_type(4)));
typedef unsigned u32x2 __attribute__((ext_vector_type(2)));
#define LAS __attribute__((address_space(3)))
#define DI __device__ __forceinline__
#define MFMA(a, b, c) __builtin_amdgcn_mfma_f32_32x32x16_bf16((a), (b), (c), 0, 0, 0)
#define SBAR() __builtin_amdgcn_sched_barrier(0)

constexpr int SEQ = 2048, DM = 448, NH = 16, DH = 28, FF = 1792;
constexpr unsigned FL_OFF = 0;
#define SETB 1u
constexpr unsigned SET_BASE = SETB << 20, SET_STRIDE = 24u << 20;
#define GQS 16u
constexpr unsigned GQW = GQS == 16u ? 128u : 4u * GQS;
constexpr unsigned GQ_OFF = 0;
constexpr unsigned GH_OFF = GQ_OFF + 256 * GQW;
constexpr unsigned GO_OFF = GH_OFF + 256 * 128;
constexpr unsigned GW_OFF = GO_OFF + 1024 * 128;
constexpr unsigned GG_OFF = GW_OFF + 256 * 8;
constexpr unsigned QF_OFF = 524288;
constexpr unsigned KV_OFF = QF_OFF + 16 * 64 * 2048;
constexpr unsigned HF_OFF = KV_OFF + 16 * 64 * 4096;
constexpr unsigned WOF_OFF = HF_OFF + 64 * 56 * 2048;
constexpr unsigned OF_OFF = WOF_OFF + 14 * 16 * 2048;
constexpr unsigned WS_END = OF_OFF + 64 * 16 * 2048;
static_assert(GG_OFF + 2048 * 8 <= QF_OFF && WS_END <= SET_STRIDE, "workspace map");

constexpr int L_X = 0;
constexpr int L_WQ = 114688;
constexpr int L_W1 = 114688;
constexpr int L_CTRL = 163776;
constexpr int L_W2 = 0;
constexpr int L_CMB = 98304;
constexpr int LDS_BYTES = 163840;

constexpr float QSCALE = 0.18898223650461362f * 1.4426950408889634f;
constexpr float LOG2E = 1.4426950408889634f;
constexpr float SAFE_BOUND = 60.0f;
constexpr unsigned SPIN_CAP = 1u << 18;
#define ROT1(rb, j) ((((rb) + (j)) & 1) * 3)
#define KVD 4
#define ROTA 0
#define ROT2 0
#define PDELAY 16
#define ODELAY 28
#define FDELAY 18
#define CODE_PF 44032
#define MPF 1
#define POLL_A 4

struct Params {
  const unsigned short* x;
  const unsigned short* wqkv;
  const unsigned short* wo;
  const unsigned short* w1;
  const unsigned short* w2;
  const unsigned short* gw;
  unsigned short* out;
  unsigned char* ws;
};
typedef unsigned long long u64;
typedef __attribute__((address_space(1))) u64 gu64;

DI unsigned cvtpk(float lo, float hi) {
  typedef __bf16 bf2 __attribute__((ext_vector_type(2)));
  typedef float f2 __attribute__((ext_vector_type(2)));
  f2 v = {lo, hi};
  bf2 r = __builtin_convertvector(v, bf2);
  return __builtin_bit_cast(unsigned, r);
}
DI bf16x8 pack8(const f32x16& x, int s) {
  u32x4 p;
  p[0] = cvtpk(x[8 * s + 0], x[8 * s + 1]);
  p[1] = cvtpk(x[8 * s + 2], x[8 * s + 3]);
  p[2] = cvtpk(x[8 * s + 4], x[8 * s + 5]);
  p[3] = cvtpk(x[8 * s + 6], x[8 * s + 7]);
  return __builtin_bit_cast(bf16x8, p);
}
DI float swap_half(float v) {
  auto r = __builtin_amdgcn_permlane32_swap(__float_as_uint(v), __float_as_uint(v), false, false);
  return (threadIdx.x & 32) ? __uint_as_float(r[0]) : __uint_as_float(r[1]);
}
template <int CTRL> DI float dpp_mov(float v) {
  return __uint_as_float(__builtin_amdgcn_update_dpp(__float_as_uint(v), __float_as_uint(v), CTRL, 0xf, 0xf, false));
}
DI float row16_max(float v) {
  v = fmaxf(v, dpp_mov<0x128>(v));
  v = fmaxf(v, dpp_mov<0x124>(v));
  v = fmaxf(v, dpp_mov<0x122>(v));
  v = fmaxf(v, dpp_mov<0x121>(v));
  return v;
}
DI unsigned row16_umax(unsigned v) {
  v = max(v, (unsigned)__builtin_amdgcn_update_dpp(v, v, 0x128, 0xf, 0xf, false));
  v = max(v, (unsigned)__builtin_amdgcn_update_dpp(v, v, 0x124, 0xf, 0xf, false));
  v = max(v, (unsigned)__builtin_amdgcn_update_dpp(v, v, 0x122, 0xf, 0xf, false));
  v = max(v, (unsigned)__builtin_amdgcn_update_dpp(v, v, 0x121, 0xf, 0xf, false));
  return v;
}
DI float rdlane(float v, int l) { return __uint_as_float(__builtin_amdgcn_readlane(__float_as_uint(v), l)); }
DI s16x4 tr_read(const LAS unsigned char* p) {
  typedef short v4i16_t __attribute__((ext_vector_type(4)));
  return __builtin_bit_cast(s16x4, __builtin_amdgcn_ds_read_tr16_b64_v4i16((LAS v4i16_t*)p));
}
DI bf16x8 tr_frag(const LAS unsigned char* p, int row_stride4) {
  s16x4 lo = tr_read(p), hi = tr_read(p + row_stride4);
  return __builtin_shufflevector(lo, hi, 0, 1, 2, 3, 4, 5, 6, 7);
}
DI __amdgpu_buffer_rsrc_t mk_rsrc(const void* p, unsigned bytes) {
  return __builtin_amdgcn_make_buffer_rsrc((void*)p, (short)0, (int)bytes, 0x00020000);
}
DI void glds16(__amdgpu_buffer_rsrc_t rs, unsigned dst, unsigned voff, unsigned soff) {
  asm volatile("s_mov_b32 m0, %0\n\ts_nop 0\n\tbuffer_load_dwordx4 %1, %2, %3 offen lds" ::"s"(dst), "v"(voff), "s"(rs), "s"(soff) : "memory");
}
DI u32x4 ld16(__amdgpu_buffer_rsrc_t rs, unsigned voff, unsigned soff) { return __builtin_amdgcn_raw_buffer_load_b128(rs, voff, soff, 0); }
DI u32x4 ld16c(__amdgpu_buffer_rsrc_t rs, unsigned voff, unsigned soff) { return __builtin_amdgcn_raw_buffer_load_b128(rs, voff, soff, 16); }
DI void st16(__amdgpu_buffer_rsrc_t rs, u32x4 v, unsigned voff, unsigned soff) { __builtin_amdgcn_raw_buffer_store_b128(v, rs, voff + soff, 0, 16); }
DI bf16x8 as_frag(u32x4 v) { return __builtin_bit_cast(bf16x8, v); }
DI u32x4 as_u4(bf16x8 v) { return __builtin_bit_cast(u32x4, v); }
template <int N> DI void wait_bar() { asm volatile("s_waitcnt vmcnt(%0) lgkmcnt(0)\n\ts_barrier" ::"n"(N) : "memory"); }

template <int V> struct IC { static constexpr int value = V; };
template <int I, int N, class Fn> DI void static_for(Fn&& fn) {
  if constexpr (I < N) { fn(IC<I>{}); static_for<I + 1, N>(fn); }
}
DI void drain() { asm volatile("s_waitcnt vmcnt(0)" ::: "memory"); }
template <int N> DI void wait_vm() { asm volatile("s_waitcnt vmcnt(%0)" ::"n"(N) : "memory"); }
DI void put_granule(unsigned char* ws, unsigned off, u64 tag16, unsigned payload) {
  __hip_atomic_store((gu64*)(ws + off), tag16 | (u64)(payload & 0xffffu), __ATOMIC_RELAXED, __HIP_MEMORY_SCOPE_AGENT);
}
template <int SLEEP> DI unsigned poll_granule(unsigned char* ws, unsigned off, u64 tag16) {
  u64 v = ~tag16;
  for (unsigned spins = 0;; ++spins) {
    if (v - tag16 > 0xffffull) v = __hip_atomic_load((gu64*)(ws + off), __ATOMIC_RELAXED, __HIP_MEMORY_SCOPE_AGENT);
    if (__all(v - tag16 <= 0xffffull) || spins > SPIN_CAP) break;
    __builtin_amdgcn_s_sleep(SLEEP);
  }
  __builtin_amdgcn_fence(__ATOMIC_ACQUIRE, "wavefront");
  return (unsigned)v & 0xffffu;
}
DI unsigned wait_lds_word(LAS unsigned char* p, unsigned want) {
  unsigned v = 0;
#pragma nounroll
  for (unsigned spins = 0; spins < (1u << 24); ++spins) {
    v = __builtin_amdgcn_readfirstlane(__hip_atomic_load((LAS unsigned*)p, __ATOMIC_RELAXED, __HIP_MEMORY_SCOPE_WORKGROUP));
    if (v >= want) break;
    __builtin_amdgcn_s_sleep(1);
  }
  asm volatile("" ::: "memory");
  return v;
}
template <int N> DI u64 wait_bar_epoch(const unsigned char* p) {
  u64 ep;
  asm volatile("s_load_dwordx2 %0, %1, 0x0\n\ts_waitcnt vmcnt(%2) lgkmcnt(0)\n\ts_barrier" : "=&s"(ep) : "s"(p), "n"(N) : "memory");
  return ep;
}
DI void bar_lds() { asm volatile("s_waitcnt lgkmcnt(0)\n\ts_barrier" ::: "memory"); }
DI void st_tile(LAS unsigned char* p, const f32x16& a) {
#pragma unroll
  for (int g = 0; g < 4; ++g) *(LAS f32x4*)(p + g * 1024) = f32x4{a[4 * g], a[4 * g + 1], a[4 * g + 2], a[4 * g + 3]};
}
DI void add_tile(f32x16& a, const LAS unsigned char* p) {
#pragma unroll
  for (int g = 0; g < 4; ++g) {
    const f32x4 v = *(const LAS f32x4*)(p + g * 1024);
    a[4 * g] += v[0]; a[4 * g + 1] += v[1]; a[4 * g + 2] += v[2]; a[4 * g + 3] += v[3];
  }
}

#define STAMP_DECL unsigned long long rt0_ = __builtin_amdgcn_s_memrealtime(), rtx_ = rt0_
#define STAMP(id) do { if ((id) == XSTAMP) { SBAR(); asm volatile("s_memrealtime %0\n\ts_waitcnt lgkmcnt(0)" : "=s"(rtx_) :: "memory"); SBAR(); } } while (0)
#define QSTAMP(id) do {} while (0)
#define RSTAMP(id) do {} while (0)
#define STAMP_FLUSH(P, ph) do { if (blockIdx.x == XBLK && __builtin_amdgcn_readfirstlane(threadIdx.x >> 6) == XWAVE) { const unsigned long long until_ = rt0_ + 5000 + 4 * (rtx_ - rt0_); \
  while (__builtin_amdgcn_s_memrealtime() < until_) __builtin_amdgcn_s_sleep(2); } } while (0)

DI void lds_arrive(LAS unsigned char* p) {
  asm volatile("s_mov_b64 exec, 1\n\tds_add_u32 %0, %1\n\ts_mov_b64 exec, -1" ::"v"((unsigned)(uintptr_t)p), "v"(1u) : "memory");
}

#define PINV(x) asm volatile("" : "+v"(x))
#define PINS(x) asm volatile("" : "+s"(x))
DI float sumsq(const u32x4& a, const u32x4& b) {
  typedef __bf16 bf2 __attribute__((ext_vector_type(2)));
  float n0 = 0.f, n1 = 0.f;
#pragma unroll
  for (int i = 0; i < 4; ++i) {
    n0 = __builtin_amdgcn_fdot2_f32_bf16(__builtin_bit_cast(bf2, a[i]), __builtin_bit_cast(bf2, a[i]), n0, false);
    n1 = __builtin_amdgcn_fdot2_f32_bf16(__builtin_bit_cast(bf2, b[i]), __builtin_bit_cast(bf2, b[i]), n1, false);
  }
  return n0 + n1;
}

DI void role_a(const Params& P, LAS unsigned char* lds, const int wt, const unsigned code_word) {
  const int lane = threadIdx.x & 63, r = lane & 31, h = lane >> 5;
  const int b = blockIdx.x, xcd = b & 7, idx = b >> 3;
  const unsigned lo = lane * 16;
  STAMP_DECL;
  STAMP(0);
  __builtin_amdgcn_s_setprio(2);
  unsigned xtouch = 0;
  if (wt < 2) xtouch = __builtin_amdgcn_raw_buffer_load_b32(wt ? mk_rsrc(P.wqkv, DM * 3 * DM * 2) : mk_rsrc(P.x, SEQ * DM * 2),
                                                            (unsigned)(wt ? b * 37 + (lane < 37 ? lane : 0) : b * 56 + (lane < 56 ? lane : 0)) * 128, 0, 0);
  SBAR();
  f32x16 acc[4];
  const int rb = (xcd & 1) * 8 + (idx & 7), j = (xcd >> 1) * 4 + (idx >> 3);
  const int head = xcd * 2 + (idx & 1), qb = idx >> 1;
  unsigned sto[3];
#pragma unroll
  for (int ct = 0; ct < 3; ++ct) {
    const int u = 3 * j + ct, part = u >> 4, t = (u & 15) * 64 + rb * 4 + wt;
    sto[ct] = lo + (part == 0 ? QF_OFF + t * 2048 : KV_OFF + t * 4096 + (part - 1) * 2048);
    PINV(sto[ct]);
  }
  unsigned padv = r == DH ? 0x3f803f80u : 0u;
  PINV(padv);
  const int gq = head / 3, gk = (16 + head) / 3, gvv = (32 + head) / 3;
  unsigned pub_off = GQ_OFF + (j * 16 + rb) * GQW + wt * GQS + (lane & 1) * 8;
  unsigned poll_off = GQ_OFF + (h ? gq * 16 + qb : (lane < 16 ? gk : gvv) * 16 + wt * 4 + ((lane >> 2) & 3)) * GQW + (lane & 3) * GQS + h * 8;
  unsigned chk_off = GQ_OFF + (gk * 16 + (lane >> 2)) * GQW + (lane & 3) * GQS;
  unsigned g_off = GG_OFF + ((qb * 4 + wt) * 32 + r) * 8;
  unsigned o_off = lo + OF_OFF + ((qb * 4 + wt) * 16 + head) * 2048;
  PINV(pub_off); PINV(poll_off); PINV(chk_off); PINV(g_off); PINV(o_off);
  unsigned kvbase = KV_OFF + (head * 64 + wt * 16) * 4096, go_off = GO_OFF + ((qb * 4 + wt) * 16 + head) * 128;
  unsigned qofs[4];
#pragma unroll
  for (int t = 0; t < 4; ++t) { qofs[t] = QF_OFF + (unsigned)(head * 64 + qb * 4 + ((wt + 1 + t) & 3)) * 2048; PINS(qofs[t]); }
  PINS(kvbase); PINS(go_off);
  unsigned xoff[4];
#pragma unroll
  for (int ks = 0; ks < 4; ++ks) xoff[ks] = (unsigned)(L_X + wt * 4096 + r * 128 + (((2 * ks + h) ^ ((r >> 1) & 7)) << 4));
  const unsigned wqoff = (unsigned)(L_WQ + (8 * h + ((lane & 15) >> 2)) * 192 + ((lane >> 4) & 1) * 32 + (lane & 3) * 8);
  u64 ep;
  asm volatile("s_load_dwordx2 %0, %1, 0x0\n\ts_waitcnt lgkmcnt(0)" : "=&s"(ep) : "s"(P.ws + FL_OFF) : "memory");
  u64 tag16 = (ep + 1) << 16;
  unsigned char* wsb = P.ws + SET_BASE + (((unsigned)ep + 1u) & 3u) * SET_STRIDE;
  PINS(tag16); PINS(wsb);
  const __amdgpu_buffer_rsrc_t rsWs = mk_rsrc(wsb, WS_END);
#pragma unroll
  for (int i = 0; i < 3; ++i) acc[i] = f32x16{};
  unsigned wtouch = 0;
  auto kloop = [&](auto nvc) {
    constexpr int NV = decltype(nvc)::value;
    bf16x8 xf[4], wf[4][3];
    auto mm = [&](int ks, int ct) { acc[ct] = ct >= 3 - NV ? MFMA(xf[ks], wf[ks][ct], acc[ct]) : MFMA(wf[ks][ct], xf[ks], acc[ct]); };
    auto step = [&](int c, int ks, bool domm) {
      const int km = (ks + 2) & 3;
#pragma unroll
      for (int ct = 0; ct < 3; ++ct) {
        if (domm) mm(km, ct);
        SBAR();
        if (ct == 0) xf[ks] = *(const LAS bf16x8*)(lds + xoff[ks] + c * 16384);
        wf[ks][ct] = tr_frag(lds + wqoff + (c & 3) * 12288 + ks * 16 * 192 + ct * 56, 4 * 192);
        SBAR();
      }
    };
#pragma unroll
    for (int c = 0; c < 7; ++c) {
      bar_lds();
      if (c == 0) STAMP(1);
      if (c == MPF) {
        const unsigned l = (unsigned)(b * 128 + (wt & 1) * 64 + lane) * 128;
        if (wt < 2) wtouch = l < DM * FF * 2 ? __builtin_amdgcn_raw_buffer_load_b32(mk_rsrc(P.w1, DM * FF * 2), l, 0, 0)
                           : l < 2 * DM * FF * 2 ? __builtin_amdgcn_raw_buffer_load_b32(mk_rsrc(P.w2, DM * FF * 2), l - DM * FF * 2, 0, 0)
                                                 : __builtin_amdgcn_raw_buffer_load_b32(mk_rsrc(P.wo, DM * DM * 2), l - 2 * DM * FF * 2, 0, 0);
        SBAR();
      }
      QSTAMP(1 + c);
      step(c, 0, c > 0);
      step(c, 1, c > 0);
      step(c, 2, true);
      step(c, 3, true);
    }
    bar_lds();
    QSTAMP(8);
#pragma unroll
    for (int ct = 0; ct < 3; ++ct) { mm(2, ct); mm(3, ct); SBAR(); }
  };
  auto epi = [&](auto nqc, auto nvc) {
    constexpr int NQ = decltype(nqc)::value, NV = decltype(nvc)::value;
    STAMP(2);
    asm volatile("" ::"v"(code_word), "v"(wtouch), "v"(xtouch));
    u32x4 t0[3], t1[3];
#pragma unroll
    for (int ct = 0; ct < 3; ++ct) {
      if (ct < NQ) acc[ct] *= QSCALE;
      t0[ct] = as_u4(pack8(acc[ct], 0));
      if (ct >= 3 - NV) {
#pragma unroll
        for (int i = 0; i < 4; ++i) t0[ct][i] = r < DH ? t0[ct][i] : padv;
      }
      st16(rsWs, t0[ct], sto[ct], 0);
      t1[ct] = as_u4(pack8(acc[ct], 1));
      if (ct >= 3 - NV) {
#pragma unroll
        for (int i = 0; i < 4; ++i) t1[ct][i] = r < DH ? t1[ct][i] : padv;
      } else {
        t1[ct][2] = h ? (ct < NQ ? 0u : 0x3f803f80u) : t1[ct][2];
        t1[ct][3] = h ? (ct < NQ ? 0u : 0x00003f80u) : t1[ct][3];
      }
      st16(rsWs, t1[ct], sto[ct], 1024);
      SBAR();
    }
    STAMP(8);
    unsigned mq = 0, mk = 0;
#pragma unroll
    for (int ct = 0; ct < 3; ++ct) {
      const unsigned n = __float_as_uint(sumsq(t0[ct], t1[ct])) & 0x7fffffffu;
      if (ct < NQ) mq = max(mq, n); else mk = max(mk, n);
    }
    auto bound = [&](unsigned m) {
      m = row16_umax(m);
      const float lo2 = __uint_as_float(max((unsigned)__builtin_amdgcn_readlane(m, 0), (unsigned)__builtin_amdgcn_readlane(m, 16)));
      const float hi2 = __uint_as_float(max((unsigned)__builtin_amdgcn_readlane(m, 32), (unsigned)__builtin_amdgcn_readlane(m, 48)));
      return ((__float_as_uint(lo2 + hi2) >> 16) + 1u) & 0xffffu;
    };
    const unsigned pq = NQ > 0 ? bound(mq) : 0u, pk = NQ < 3 ? bound(mk) : 0u;
    u64 gran = tag16 | ((lane & 1) ? pq : pk);
    PINV(gran);
    drain();
    STAMP(9);
    if (lane < 2) __hip_atomic_store((gu64*)(wsb + pub_off), gran, __ATOMIC_RELAXED, __HIP_MEMORY_SCOPE_AGENT);
  };
  if (j < 10) {
    kloop(IC<0>{});
    if (j < 5) epi(IC<3>{}, IC<0>{});
    else if (j == 5) epi(IC<1>{}, IC<0>{});
    else epi(IC<0>{}, IC<0>{});
  } else if (j == 10) {
    kloop(IC<1>{});
    epi(IC<0>{}, IC<1>{});
  } else {
    kloop(IC<3>{});
    epi(IC<0>{}, IC<3>{});
  }
  STAMP(3);
  __builtin_amdgcn_s_sleep(PDELAY);
  const unsigned pay = poll_granule<POLL_A>(wsb, poll_off, tag16);
  STAMP(4);
  u32x4 q[4][2];
  LAS unsigned char* const cmb = lds + L_CMB + wt * 12288 + lo;
  u64 gv = 0;
  unsigned thr = 0;
#pragma nounroll
  for (int pass = 0;; ++pass) {
    u32x4 kv[KVD][4];
    kv[0][0] = ld16c(rsWs, lo, kvbase);
    SBAR();
    if (pass == 0) { q[0][0] = ld16c(rsWs, lo, qofs[0]); q[0][1] = ld16c(rsWs, lo + 1024, qofs[0]); }
    SBAR();
    kv[0][1] = ld16c(rsWs, lo + 1024, kvbase);
    SBAR();
    if (pass == 0) {
#pragma unroll
      for (int t = 1; t < 4; ++t) { q[t][0] = ld16c(rsWs, lo, qofs[t]); q[t][1] = ld16c(rsWs, lo + 1024, qofs[t]); SBAR(); }
    }
#pragma unroll
    for (int e = 2; e < 4 * (KVD - 1); ++e) { kv[e >> 2][e & 3] = ld16c(rsWs, lo + (e & 3) * 1024, kvbase + (e >> 2) * 4096); SBAR(); }
#pragma unroll
    for (int i = 0; i < 4; ++i) acc[i] = f32x16{};
    {
      const float q2 = __uint_as_float(__builtin_amdgcn_readlane(row16_umax(pay), 32) << 16);
      const float lim = SAFE_BOUND * SAFE_BOUND * __builtin_amdgcn_rcpf(q2);
      thr = lim < 1e30f ? __float_as_uint(lim) >> 16 : (lim >= 1e30f ? 0x7000u : 0u);
      PINV(thr);
    }
    __builtin_amdgcn_s_setprio(1);
    f32x16 sc = MFMA(as_frag(kv[0][0]), as_frag(q[0][0]), f32x16{});
    sc = MFMA(as_frag(kv[0][1]), as_frag(q[0][1]), sc);
    STAMP(5);
    u32x4 p0, p1 = {0, 0, 0, 0};
    float e0 = 0.f, e1 = 0.f, e2 = 0.f, e3 = 0.f;
    u64 ck = 0, cv = 0;
#define EXP4(i) do { e0 = __builtin_amdgcn_exp2f(sc[i]); e1 = __builtin_amdgcn_exp2f(sc[(i) + 1]); e2 = __builtin_amdgcn_exp2f(sc[(i) + 2]); e3 = __builtin_amdgcn_exp2f(sc[(i) + 3]); \
    asm volatile("" : "+v"(e0), "+v"(e1), "+v"(e2), "+v"(e3)); SBAR(); } while (0)
#define CVT2(P, i) do { P[i] = cvtpk(e0, e1); P[(i) + 1] = cvtpk(e2, e3); asm volatile("" : "+v"(P)); SBAR(); } while (0)
#define PART(t, g) do { *(LAS f32x4*)(cmb + (t) * 4096 + (g) * 1024) = f32x4{acc[t][4 * (g)], acc[t][4 * (g) + 1], acc[t][4 * (g) + 2], acc[t][4 * (g) + 3]}; SBAR(); } while (0)
    static_for<0, 64>([&](auto ic) {
      constexpr int i = decltype(ic)::value;
      constexpr int kt = i >> 2, t = i & 3, kn_ = (i + 1) >> 2, tn = (i + 1) & 3, kp = (i - 1) >> 2, tp = (i - 1) & 3;
      constexpr int kl = kt + KVD - 1;
      if (i == 24) STAMP(11);
      if (i == 56) (void)wait_lds_word(lds + L_CTRL, 4);
      f32x16 sn;
      SBAR();
      if (i < 63) sn = MFMA(as_frag(kv[kn_ % KVD][0]), as_frag(q[tn][0]), f32x16{});
      SBAR();
      if constexpr (i >= 62) PART(i - 62, 0);
      if (i > 0) CVT2(p1, 2);
      EXP4(0);
      if (i > 0) acc[tp] = MFMA(as_frag(kv[(kp + KVD) % KVD][3]), as_frag(p1), acc[tp]);
      SBAR();
      if (kl < 16) kv[kl % KVD][t] = ld16c(rsWs, lo + t * 1024, kvbase + kl * 4096);
      if (i == 52) gv = __hip_atomic_load((gu64*)(wsb + g_off), __ATOMIC_RELAXED, __HIP_MEMORY_SCOPE_AGENT);
      if (i == 53) ck = __hip_atomic_load((gu64*)(wsb + chk_off), __ATOMIC_RELAXED, __HIP_MEMORY_SCOPE_AGENT);
      if (i == 54) cv = __hip_atomic_load((gu64*)(wsb + (gvv - gk) * 16 * GQW + chk_off), __ATOMIC_RELAXED, __HIP_MEMORY_SCOPE_AGENT);
      SBAR();
      if constexpr (i >= 62) PART(i - 62, 1);
      CVT2(p0, 0);
      EXP4(4);
      if (i < 63) sn = MFMA(as_frag(kv[kn_ % KVD][1]), as_frag(q[tn][1]), sn);
      SBAR();
      if constexpr (i >= 62) PART(i - 62, 2);
      CVT2(p0, 2);
      EXP4(8);
      acc[t] = MFMA(as_frag(kv[kt % KVD][2]), as_frag(p0), acc[t]);
      SBAR();
      if constexpr (i >= 62) PART(i - 62, 3);
      CVT2(p1, 0);
      EXP4(12);
      if (i < 63) sc = sn;
    });
    CVT2(p1, 2);
    acc[3] = MFMA(as_frag(kv[15 % KVD][3]), as_frag(p1), acc[3]);
    SBAR();
    PART(2, 0); PART(2, 1); PART(2, 2); PART(2, 3);
#undef EXP4
#undef CVT2
#undef PART
    __builtin_amdgcn_s_setprio(2);
    if (__builtin_expect(__all(ck - tag16 <= (u64)thr && cv - tag16 <= 0x7000ull && gv - tag16 <= 0xffffull), 1)) break;
    gv = poll_granule<2>(wsb, g_off, tag16);
    if (pass) break;
    const unsigned pk = poll_granule<2>(wsb, chk_off, tag16), pv = poll_granule<2>(wsb, (gvv - gk) * 16 * GQW + chk_off, tag16);
    if (__all(pk <= thr && pv <= 0x7000u)) break;
    float mx[4];
#pragma unroll
    for (int t = 0; t < 4; ++t) mx[t] = -INFINITY;
    for (int kt = 0; kt < 64; ++kt) {
      const u32x4 k0 = ld16c(rsWs, lo, KV_OFF + (head * 64 + kt) * 4096), k1 = ld16c(rsWs, lo + 1024, KV_OFF + (head * 64 + kt) * 4096);
#pragma unroll
      for (int t = 0; t < 4; ++t) {
        f32x16 s = MFMA(as_frag(k0), as_frag(q[t][0]), f32x16{});
        s = MFMA(as_frag(k1), as_frag(q[t][1]), s);
#pragma unroll
        for (int i = 0; i < 16; ++i) mx[t] = fmaxf(mx[t], s[i]);
      }
    }
#pragma unroll
    for (int t = 0; t < 4; ++t) {
      const float nm = -fmaxf(mx[t], swap_half(mx[t]));
      const unsigned hi = __float_as_uint(nm) & 0xffff0000u;
      const float r1 = nm - __uint_as_float(hi);
      const unsigned mid = __float_as_uint(r1) & 0xffff0000u;
      const float r2 = r1 - __uint_as_float(mid);
      if (h) { q[t][1][2] = (hi >> 16) | mid; q[t][1][3] = __float_as_uint(r2) >> 16; }
    }
  }
  STAMP(6);
  asm volatile("s_waitcnt lgkmcnt(0)" ::: "memory");
  lds_arrive(lds + L_CTRL + 20);
  (void)wait_lds_word(lds + L_CTRL + 20, 4);
  f32x16 o = acc[3];
#pragma unroll
  for (int u = 0; u < 3; ++u) add_tile(o, lds + L_CMB + ((wt + 3 - u) & 3) * 12288 + u * 4096 + lo);
  const auto lsw = __builtin_amdgcn_permlane32_swap(__float_as_uint(o[12]), __float_as_uint(o[12]), false, false);
  const float inv = __uint_as_float((unsigned)gv << 16) * __builtin_amdgcn_rcpf(__uint_as_float(lsw[1]));
  o *= inv;
  st16(rsWs, as_u4(pack8(o, 0)), o_off, 0);
  u32x4 o1 = as_u4(pack8(o, 1));
  o1[2] = h ? 0u : o1[2];
  o1[3] = h ? 0u : o1[3];
  st16(rsWs, o1, o_off, 1024);
  STAMP(7);
  drain();
  if (lane == 0) __hip_atomic_store((gu64*)(wsb + go_off), tag16, __ATOMIC_RELAXED, __HIP_MEMORY_SCOPE_AGENT);
  STAMP(10);
  STAMP_FLUSH(P, 0);
}

constexpr int W2S = 6;
constexpr int HPRE = 5;
#define LEA 5
#define LEB 11
#define LE0 16
#define LU0 3
#define F1LA 2
#define PACE 4
#define PACE2 2
constexpr int ffn2_ops(int f) { return (f + W2S < 14 ? 4 : 0) + 4; }
constexpr int ffn2_younger(int f, int upto) {
  int n = 4;
  for (int i = f - W2S + 1; i < upto; ++i) n += ffn2_ops(i);
  return n;
}

DI void role_f(const Params& P, LAS unsigned char* lds, const int wt) {
  u64 ep;
  const int lane = threadIdx.x & 63, r = lane & 31, h = lane >> 5;
  const int b = blockIdx.x, xcd = b & 7, idx = b >> 3;
  const int rb = (xcd & 1) * 8 + (idx & 7), j = (xcd >> 1) * 4 + (idx >> 3);
  const unsigned lo = lane * 16, lds0 = (unsigned)(uintptr_t)lds;
  const bool ffn = j < 14;
  STAMP_DECL;
  STAMP(0);
  f32x16 acc[4];
  u32x4 wl[28];
  const __amdgpu_buffer_rsrc_t rsL = mk_rsrc(ffn ? P.w1 : P.gw, ffn ? DM * FF * 2 : DM * 2);
  const unsigned lsrc = ffn ? (unsigned)((lane >> 2) * (FF * 2) + (j * 4 + wt) * 64 + (lane & 3) * 16) : (unsigned)(h * 16);
  const unsigned lstep = ffn ? 16 * FF * 2 : 32;
  const int rot = ROT1(rb, j);
  auto kc = [&](int c) { const int v = c + rot; return v >= 7 ? v - 7 : v; };
  auto L = [&](int u0, int u1) {
#pragma unroll
    for (int u = u0; u < u1; ++u) wl[u] = ld16(rsL, lsrc, (kc(u >> 2) * 4 + (u & 3)) * lstep);
    SBAR();
  };
  {
    const __amdgpu_buffer_rsrc_t rsX = mk_rsrc(P.x, SEQ * DM * 2);
    const __amdgpu_buffer_rsrc_t rsW = mk_rsrc(P.wqkv, DM * 3 * DM * 2);
    unsigned xsrc[2], wsrc[3];
#pragma unroll
    for (int i = 0; i < 2; ++i) {
      const int row = 32 * wt + 8 * i + (lane >> 3), ch = (lane & 7) ^ ((row >> 1) & 7);
      xsrc[i] = (unsigned)((rb * 128 + row) * (DM * 2) + ch * 16);
    }
#pragma unroll
    for (int i = 0; i < 3; ++i) {
      const int p = 64 * (3 * wt + i) + lane, kr = p / 12, ch = p % 12;
      wsrc[i] = (unsigned)(kr * (3 * DM * 2) + j * 168 + (ch < 11 ? ch : 10) * 16);
    }
    auto X = [&](int c) {
#pragma unroll
      for (int i = 0; i < 4; ++i) glds16(rsX, lds0 + L_X + c * 16384 + (4 * wt + i) * 1024, xsrc[i & 1], kc(c) * 128 + (i >> 1) * 16 * DM * 2);
    };
    auto W = [&](int c) {
#pragma unroll
      for (int i = 0; i < 3; ++i) glds16(rsW, lds0 + L_WQ + (c & 3) * 12288 + (3 * wt + i) * 1024, wsrc[i], kc(c) * 64 * 3 * DM * 2);
    };
    X(0); W(0); X(1); W(1); X(2);
    QSTAMP(1);
    wait_bar<11>();
    STAMP(1);
    W(2); X(3); W(3);
    QSTAMP(2);
    wait_bar<14>();
    X(4); W(4);
    QSTAMP(3);
    wait_bar<14>();
    X(5); W(5);
    QSTAMP(4);
    wait_bar<14>();
    X(6); W(6);
    QSTAMP(5);
    wait_bar<14>();
    if (wt == 0 && lane < 8) *(LAS unsigned*)(lds + L_CTRL + lane * 4) = 0u;
    L(0, LEA);
    QSTAMP(6);
    ep = wait_bar_epoch<7 + LEA>(P.ws + FL_OFF);
    L(LEA, LEB);
    QSTAMP(7);
    wait_bar<LEB>();
    L(LEB, LE0);
    QSTAMP(8);
    bar_lds();
    __builtin_amdgcn_s_sleep(FDELAY);
  }
  STAMP(2);
  const u64 tag = (ep + 1) << 16;
  unsigned char* const wsb = P.ws + SET_BASE + (((unsigned)ep + 1u) & 3u) * SET_STRIDE;
  const __amdgpu_buffer_rsrc_t rsWs = mk_rsrc(wsb, WS_END);
  unsigned xoff[4];
#pragma unroll
  for (int ks = 0; ks < 4; ++ks) xoff[ks] = (unsigned)(L_X + r * 128 + (((2 * ks + h) ^ ((r >> 1) & 7)) << 4));
  if (ffn) {
    LAS unsigned char* ring = lds + L_W1 + wt * 8192;
    const unsigned troff = (unsigned)((8 * h + ((lane & 15) >> 2)) * 64 + ((lane >> 4) & 1) * 32 + (lane & 3) * 8);
#pragma unroll
    for (int i = 0; i < 4; ++i) acc[i] = f32x16{};
    RSTAMP(1);
    constexpr int LA = F1LA;
    bf16x8 xf[LA + 1][4], wf[LA + 1];
    auto wr = [&](int u) { *(LAS u32x4*)(ring + (u & 7) * 1024 + lo) = wl[u]; };
    auto rdw = [&](int u) { wf[u % (LA + 1)] = tr_frag(ring + (u & 7) * 1024 + troff, 4 * 64); };
    auto rdx = [&](int u, int t) { xf[u % (LA + 1)][t] = *(const LAS bf16x8*)(lds + xoff[u & 3] + (u >> 2) * 16384 + t * 4096); };
#pragma unroll
    for (int u = 0; u < LA + 2; ++u) wr(u);
#pragma unroll
    for (int u = 0; u < LA; ++u) {
      rdw(u);
#pragma unroll
      for (int t = 0; t < 4; ++t) rdx(u, t);
    }
    SBAR();
#pragma unroll
    for (int u = 0; u < 28; ++u) {
      if ((u & 3) == 0) RSTAMP(2 + (u >> 2));
#pragma unroll
      for (int t = 0; t < 4; ++t) {
        if (u + LA < 28) {
          if (t == 1) rdw(u + LA);
          rdx(u + LA, t);
          if (t == 3 && u + LA + 2 < 28) wr(u + LA + 2);
        }
        SBAR();
        acc[t] = MFMA(wf[u % (LA + 1)], xf[u % (LA + 1)][t], acc[t]);
        SBAR();
        if (u >= LU0 && LE0 + (u - LU0) * 4 + t < 28) L(LE0 + (u - LU0) * 4 + t, LE0 + (u - LU0) * 4 + t + 1);
      }
    }
    lds_arrive(lds + L_CTRL);
    RSTAMP(9);
    STAMP(3);
    __builtin_amdgcn_s_setprio(3);
#pragma unroll
    for (int t = 0; t < 4; ++t) {
      f32x16 z = acc[t];
#pragma unroll
      for (int i = 0; i < 16; ++i) z[i] = z[i] * __builtin_amdgcn_rcpf(1.f + __builtin_amdgcn_exp2f(-LOG2E * z[i]));
      const unsigned so = HF_OFF + ((rb * 4 + t) * 56 + j * 4 + wt) * 2048;
      st16(rsWs, as_u4(pack8(z, 0)), lo, so);
      st16(rsWs, as_u4(pack8(z, 1)), lo + 1024, so);
    }
    drain();
    if (lane == 0) put_granule(wsb, GH_OFF + (rb * 16 + j) * 128 + wt * 8, tag, 0);
  } else {
    __builtin_amdgcn_s_setprio(3);
    if (j == 14) {
      L(LE0, 28);
      acc[0] = f32x16{};
#pragma unroll
      for (int u = 0; u < 28; ++u) {
        const bf16x8 xf = *(const LAS bf16x8*)(lds + xoff[u & 3] + (u >> 2) * 16384 + wt * 4096);
        acc[0] = MFMA(as_frag(wl[u]), xf, acc[0]);
      }
      const float g = __builtin_amdgcn_rcpf(1.f + __builtin_amdgcn_exp2f(-LOG2E * acc[0][0]));
      if (h == 0) put_granule(wsb, GG_OFF + ((rb * 4 + wt) * 32 + r) * 8, tag, cvtpk(g, 0.f));
    }
    lds_arrive(lds + L_CTRL);
    const __amdgpu_buffer_rsrc_t rsO = mk_rsrc(P.wo, DM * DM * 2);
    const int id0 = ((j - 14) * 16 + rb) * 4 + wt;
    for (int id = id0; id < 224; id += 128) {
      const int nt = id >> 4, hd = id & 15;
      unsigned rp[16];
#pragma unroll
      for (int e = 0; e < 16; ++e)
        rp[e] = __builtin_amdgcn_raw_buffer_load_b16(rsO, (unsigned)(((4 * h + 8 * (e >> 2) + (e & 3)) * DM + r) * 2), (unsigned)((DH * hd * DM + 32 * nt) * 2), 0);
      u32x4 a, c2;
#pragma unroll
      for (int i = 0; i < 4; ++i) {
        a[i] = (rp[2 * i] & 0xffffu) | (rp[2 * i + 1] << 16);
        c2[i] = (rp[8 + 2 * i] & 0xffffu) | (rp[8 + 2 * i + 1] << 16);
      }
      c2[2] = h ? 0u : c2[2];
      c2[3] = h ? 0u : c2[3];
      st16(rsWs, a, lo, WOF_OFF + id * 2048);
      st16(rsWs, c2, lo + 1024, WOF_OFF + id * 2048);
      drain();
      if (lane == 0) put_granule(wsb, GW_OFF + id * 8, tag, 0);
    }
  }
  STAMP(4);
  if (idx >= 28) {
    if (b == 255 && wt == 0) {
      __builtin_amdgcn_s_sleep(127);
#pragma unroll
      for (int i = 0; i < 4; ++i) (void)poll_granule<64>(wsb, GQ_OFF + (i * 64 + lane) * GQW, tag);
      if (lane == 0) __hip_atomic_store((gu64*)(P.ws + FL_OFF), ep + 1, __ATOMIC_RELAXED, __HIP_MEMORY_SCOPE_AGENT);
    }
    STAMP(10);
    STAMP_FLUSH(P, 0);
    return;
  }
  const int mb = 4 * xcd + (idx & 3), nb = idx >> 2;
  const __amdgpu_buffer_rsrc_t rsW2 = mk_rsrc(P.w2, FF * DM * 2), rsH = rsWs;
  const unsigned w2src = (unsigned)((wt * 448 + (lane >> 3)) * (DM * 2) + nb * 128 + ((((lane >> 2) & 1) ^ ((lane >> 4) & 1)) * 64) + (lane & 3) * 16);
  const unsigned ring0 = lds0 + L_W2 + wt * 24576;
  const int rot2 = ROT2;
  auto fr = [&](int f) { const int v = f + rot2; return v >= 14 ? v - 14 : v; };
  auto D = [&](int f) {
#pragma unroll
    for (int i = 0; i < 4; ++i) glds16(rsW2, ring0 + (f % W2S) * 4096 + i * 1024, w2src, (fr(f) * 32 + i * 8) * DM * 2);
  };
  (void)wait_lds_word(lds + L_CTRL, 4);
#pragma unroll
  for (int f = 0; f < W2S; ++f) { D(f); __builtin_amdgcn_s_sleep(PACE); }
  const int ftl = wt * 14 + ((lane & 15) < 14 ? (lane & 15) : 0);
  (void)poll_granule<8>(wsb, (lane & 16) ? GW_OFF + ((nb * 2 + ((lane >> 2) & 1)) * 16 + wt * 4 + (lane & 3)) * 8
                                        : GH_OFF + ((mb >> 1) * 16 + (ftl >> 2)) * 128 + (ftl & 3) * 8, tag);
  STAMP(5);
  u32x4 hb[HPRE][4], wo[16];
  const unsigned hbase = HF_OFF + (mb * 2 * 56 + wt * 14) * 2048;
  auto H = [&](int f) {
#pragma unroll
    for (int e = 0; e < 4; ++e) hb[f % HPRE][e] = ld16c(rsH, lo + (e & 1) * 1024, hbase + ((e >> 1) * 56 + fr(f)) * 2048);
  };
#pragma unroll
  for (int f = 0; f < HPRE; ++f) { H(f); SBAR(); }
  const float gl = __uint_as_float(poll_granule<8>(wsb, GG_OFF + (mb * 64 + lane) * 8, tag) << 16), gs = swap_half(gl);
  const float og0 = 1.f - (h ? gs : gl), og1 = 1.f - (h ? gl : gs);
  const int q4 = (lane & 15) >> 2;
  unsigned woff[2];
#pragma unroll
  for (int nt = 0; nt < 2; ++nt) woff[nt] = (unsigned)(L_W2 + wt * 24576 + (4 * h + q4) * 128 + ((nt ^ (q4 >> 1)) * 64) + ((lane >> 4) & 1) * 32 + (lane & 3) * 8);
#pragma unroll
  for (int i = 0; i < 4; ++i) acc[i] = f32x16{};
  STAMP(6);
  __builtin_amdgcn_s_setprio(0);
  bf16x8 wf[2][2][2];
  auto RW = [&](int f) {
#pragma unroll
    for (int s = 0; s < 2; ++s)
#pragma unroll
      for (int nt = 0; nt < 2; ++nt) wf[f & 1][s][nt] = tr_frag(lds + woff[nt] + (f % W2S) * 4096 + s * 2048, 1024);
  };
  RW(0);
  static_for<0, 14>([&](auto fc) {
    constexpr int f = decltype(fc)::value;
    asm volatile("s_waitcnt lgkmcnt(0)" ::: "memory");
    SBAR();
    if constexpr (f + 1 < 14) {
      if constexpr (f + 1 >= W2S) wait_vm<ffn2_younger(f + 1, f)>();
      RW(f + 1);
    }
    SBAR();
#pragma unroll
    for (int s = 0; s < 2; ++s)
#pragma unroll
      for (int nt = 0; nt < 2; ++nt)
#pragma unroll
        for (int mt = 0; mt < 2; ++mt) {
          acc[nt * 2 + mt] = MFMA(wf[f & 1][s][nt], as_frag(hb[f % HPRE][mt * 2 + s]), acc[nt * 2 + mt]);
        }
    SBAR();
    if constexpr (f + W2S < 14) D(f + W2S);
    if constexpr (f + HPRE < 14) H(f + HPRE);
    else if constexpr (f + HPRE < 18) {
      constexpr int e0 = (f + HPRE - 14) * 4;
#pragma unroll
      for (int e = e0; e < e0 + 4; ++e) wo[e] = ld16c(rsWs, lo, WOF_OFF + ((nb * 2 + (e >> 3)) * 16 + wt * 4) * 2048 + (e & 7) * 1024);
    }
    SBAR();
    __builtin_amdgcn_s_sleep(PACE2);
    SBAR();
  });
  STAMP(7);
  __builtin_amdgcn_s_setprio(3);
#pragma unroll
  for (int i = 0; i < 16; ++i) { acc[0][i] *= og0; acc[1][i] *= og1; acc[2][i] *= og0; acc[3][i] *= og1; }
  (void)wait_lds_word(lds + L_CTRL + 20, 1);
  __builtin_amdgcn_s_sleep(ODELAY);
  (void)poll_granule<POLL_A>(wsb, GO_OFF + ((mb * 2 + ((lane >> 2) & 1)) * 16 + wt * 4 + (lane & 3)) * 128, tag);
  STAMP(8);
  u32x4 of[16];
#pragma unroll
  for (int e = 0; e < 8; ++e)
#pragma unroll
    for (int mt = 0; mt < 2; ++mt) of[mt * 8 + e] = ld16c(rsWs, lo, OF_OFF + ((mb * 2 + mt) * 16 + wt * 4) * 2048 + e * 1024);
  SBAR();
#pragma unroll
  for (int e = 0; e < 8; ++e)
#pragma unroll
    for (int nt = 0; nt < 2; ++nt)
#pragma unroll
      for (int mt = 0; mt < 2; ++mt) acc[nt * 2 + mt] = MFMA(as_frag(wo[nt * 8 + e]), as_frag(of[mt * 8 + e]), acc[nt * 2 + mt]);
#pragma unroll
  for (int t = 0; t < 4; ++t) st_tile(lds + L_W2 + wt * 24576 + t * 4096 + lo, acc[t]);
  STAMP(9);
  bar_lds();
  f32x16 a = f32x16{};
#pragma unroll
  for (int f = 0; f < 4; ++f) add_tile(a, lds + L_W2 + f * 24576 + wt * 4096 + lo);
  unsigned short* dst = P.out + (size_t)((mb * 2 + (wt & 1)) * 32 + r) * DM + (nb * 2 + (wt >> 1)) * 32 + 8 * h;
#pragma unroll
  for (int k = 0; k < 2; ++k) {
    const auto x = __builtin_amdgcn_permlane32_swap(cvtpk(a[8 * k], a[8 * k + 1]), cvtpk(a[8 * k + 4], a[8 * k + 5]), false, false);
    const auto y = __builtin_amdgcn_permlane32_swap(cvtpk(a[8 * k + 2], a[8 * k + 3]), cvtpk(a[8 * k + 6], a[8 * k + 7]), false, false);
    *(u32x4*)(dst + 16 * k) = u32x4{x[0], y[0], x[1], y[1]};
  }
  STAMP(10);
  STAMP_FLUSH(P, 0);
}

extern __shared__ __attribute__((aligned(16))) unsigned char g_lds[];

__global__ __launch_bounds__(512, 2) void k_fused(Params P) {
  u64 pc;
  asm volatile("s_getpc_b64 %0" : "=s"(pc));
  const unsigned code_word = __builtin_amdgcn_raw_buffer_load_b32(mk_rsrc((const void*)(pc & ~63ull), CODE_PF),
      (threadIdx.x & 256) * 4096 + (((blockIdx.x >> 3) * 32 + ((threadIdx.x >> 3) & 24) + (threadIdx.x & 7)) * 64), 0, 0);
  SBAR();
  LAS unsigned char* lds = (LAS unsigned char*)g_lds;
  const int w = __builtin_amdgcn_readfirstlane(threadIdx.x >> 6);
  if (w < 4) role_a(P, lds, w, code_word);
  else role_f(P, lds, w - 4);
}

extern "C" void kernel_launch(void* const* d_in, const int* in_sizes, int n_in, void* d_out, int out_size, void* d_ws,
                              size_t ws_size, hipStream_t stream) {
  Params P;
  P.x = (const unsigned short*)d_in[0];
  P.wqkv = (const unsigned short*)d_in[1];
  P.wo = (const unsigned short*)d_in[2];
  P.w1 = (const unsigned short*)d_in[4];
  P.w2 = (const unsigned short*)d_in[5];
  P.gw = (const unsigned short*)d_in[6];
  P.out = (unsigned short*)d_out;
  P.ws = (unsigned char*)d_ws;
  (void)hipFuncSetAttribute((const void*)k_fused, hipFuncAttributeMaxDynamicSharedMemorySize, LDS_BYTES);
  k_fused<<<256, 512, LDS_BYTES, stream>>>(P);
}
```

```cpp
#define OAUX 16
#include <hip/hip_runtime.h>
#include <hip/hip_bf16.h>
#include <stdint.h>

typedef short bf16x8 __attribute__((ext_vector_type(8)));
typedef short s16x4 __attribute__((ext_vector_type(4)));
typedef float f32x16 __attribute__((ext_vector_type(16)));
typedef float f32x4 __attribute__((ext_vector_type(4)));
typedef unsigned u32x4 __attribute__((ext_vector_type(4)));
typedef unsigned u32x2 __attribute__((ext_vector_type(2)));
#define LAS __attribute__((address_space(3)))
#define DI __device__ __forceinline__
#define MFMA(a, b, c) __builtin_amdgcn_mfma_f32_32x32x16_bf16((a), (b), (c), 0, 0, 0)
#define SBAR() __builtin_amdgcn_sched_barrier(0)

constexpr int SEQ = 2048, DM = 448, NH = 16, DH = 28, FF = 1792;
constexpr unsigned FL_OFF = 0;
#define SETB 1u
constexpr unsigned SET_BASE = SETB << 20, SET_STRIDE = 24u << 20;
#define GQS 16u
constexpr unsigned GQW = GQS == 16u ? 128u : 4u * GQS;
constexpr unsigned GQ_OFF = 0;
constexpr unsigned GH_OFF = GQ_OFF + 256 * GQW;
constexpr unsigned GO_OFF = GH_OFF + 256 * 128;
constexpr unsigned GW_OFF = GO_OFF + 1024 * 128;
constexpr unsigned GG_OFF = GW_OFF + 256 * 8;
constexpr unsigned QF_OFF = 524288;
constexpr unsigned KV_OFF = QF_OFF + 16 * 64 * 2048;
constexpr unsigned HF_OFF = KV_OFF + 16 * 64 * 4096;
constexpr unsigned WOF_OFF = HF_OFF + 64 * 56 * 2048;
constexpr unsigned OF_OFF = WOF_OFF + 14 * 16 * 2048;
constexpr unsigned WS_END = OF_OFF + 64 * 16 * 2048;
static_assert(GG_OFF + 2048 * 8 <= QF_OFF && WS_END <= SET_STRIDE, "workspace map");

constexpr int L_X = 0;
constexpr int L_WQ = 114688;
constexpr int L_W1 = 114688;
constexpr int L_CTRL = 163776;
constexpr int L_W2 = 0;
constexpr int L_CMB = 98304;
constexpr int LDS_BYTES = 163840;

constexpr float QSCALE = 0.18898223650461362f * 1.4426950408889634f;
constexpr float LOG2E = 1.4426950408889634f;
constexpr float SAFE_BOUND = 60.0f;
constexpr unsigned SPIN_CAP = 1u << 18;
#define ROT1(rb, j) ((((rb) + (j)) & 1) * 3)
#define KVD 4
#define ROTA 0
#define ROT2 0
#define PDELAY 16
#define ODELAY 10
#define FDELAY 18
#define CODE_PF 44032
#define MPF 1
#define HDELAY 1
#define TRIG 3
#define WDELAY 100
#define FTHR 8
#define DEARLY W2S
#define F1PRIO 3
#define POLL_A 4

struct Params {
  const unsigned short* x;
  const unsigned short* wqkv;
  const unsigned short* wo;
  const unsigned short* w1;
  const unsigned short* w2;
  const unsigned short* gw;
  unsigned short* out;
  unsigned char* ws;
};
typedef unsigned long long u64;
typedef __attribute__((address_space(1))) u64 gu64;

DI unsigned cvtpk(float lo, float hi) {
  typedef __bf16 bf2 __attribute__((ext_vector_type(2)));
  typedef float f2 __attribute__((ext_vector_type(2)));
  f2 v = {lo, hi};
  bf2 r = __builtin_convertvector(v, bf2);
  return __builtin_bit_cast(unsigned, r);
}
DI bf16x8 pack8(const f32x16& x, int s) {
  u32x4 p;
  p[0] = cvtpk(x[8 * s + 0], x[8 * s + 1]);
  p[1] = cvtpk(x[8 * s + 2], x[8 * s + 3]);
  p[2] = cvtpk(x[8 * s + 4], x[8 * s + 5]);
  p[3] = cvtpk(x[8 * s + 6], x[8 * s + 7]);
  return __builtin_bit_cast(bf16x8, p);
}
DI float swap_half(float v) {
  auto r = __builtin_amdgcn_permlane32_swap(__float_as_uint(v), __float_as_uint(v), false, false);
  return (threadIdx.x & 32) ? __uint_as_float(r[0]) : __uint_as_float(r[1]);
}
template <int CTRL> DI float dpp_mov(float v) {
  return __uint_as_float(__builtin_amdgcn_update_dpp(__float_as_uint(v), __float_as_uint(v), CTRL, 0xf, 0xf, false));
}
DI float row16_max(float v) {
  v = fmaxf(v, dpp_mov<0x128>(v));
  v = fmaxf(v, dpp_mov<0x124>(v));
  v = fmaxf(v, dpp_mov<0x122>(v));
  v = fmaxf(v, dpp_mov<0x121>(v));
  return v;
}
DI unsigned row16_umax(unsigned v) {
  v = max(v, (unsigned)__builtin_amdgcn_update_dpp(v, v, 0x128, 0xf, 0xf, false));
  v = max(v, (unsigned)__builtin_amdgcn_update_dpp(v, v, 0x124, 0xf, 0xf, false));
  v = max(v, (unsigned)__builtin_amdgcn_update_dpp(v, v, 0x122, 0xf, 0xf, false));
  v = max(v, (unsigned)__builtin_amdgcn_update_dpp(v, v, 0x121, 0xf, 0xf, false));
  return v;
}
DI float rdlane(float v, int l) { return __uint_as_float(__builtin_amdgcn_readlane(__float_as_uint(v), l)); }
DI s16x4 tr_read(const LAS unsigned char* p) {
  typedef short v4i16_t __attribute__((ext_vector_type(4)));
  return __builtin_bit_cast(s16x4, __builtin_amdgcn_ds_read_tr16_b64_v4i16((LAS v4i16_t*)p));
}
DI bf16x8 tr_frag(const LAS unsigned char* p, int row_stride4) {
  s16x4 lo = tr_read(p), hi = tr_read(p + row_stride4);
  return __builtin_shufflevector(lo, hi, 0, 1, 2, 3, 4, 5, 6, 7);
}
DI __amdgpu_buffer_rsrc_t mk_rsrc(const void* p, unsigned bytes) {
  return __builtin_amdgcn_make_buffer_rsrc((void*)p, (short)0, (int)bytes, 0x00020000);
}
DI void glds16(__amdgpu_buffer_rsrc_t rs, unsigned dst, unsigned voff, unsigned soff) {
  asm volatile("s_mov_b32 m0, %0\n\ts_nop 0\n\tbuffer_load_dwordx4 %1, %2, %3 offen lds" ::"s"(dst), "v"(voff), "s"(rs), "s"(soff) : "memory");
}
DI u32x4 ld16(__amdgpu_buffer_rsrc_t rs, unsigned voff, unsigned soff) { return __builtin_amdgcn_raw_buffer_load_b128(rs, voff, soff, 0); }
DI u32x4 ld16c(__amdgpu_buffer_rsrc_t rs, unsigned voff, unsigned soff) { return __builtin_amdgcn_raw_buffer_load_b128(rs, voff, soff, 16); }
DI void st16(__amdgpu_buffer_rsrc_t rs, u32x4 v, unsigned voff, unsigned soff) { __builtin_amdgcn_raw_buffer_store_b128(v, rs, voff + soff, 0, 16); }
DI bf16x8 as_frag(u32x4 v) { return __builtin_bit_cast(bf16x8, v); }
DI u32x4 as_u4(bf16x8 v) { return __builtin_bit_cast(u32x4, v); }
template <int N> DI void wait_bar() { asm volatile("s_waitcnt vmcnt(%0) lgkmcnt(0)\n\ts_barrier" ::"n"(N) : "memory"); }

template <int V> struct IC { static constexpr int value = V; };
template <int I, int N, class Fn> DI void static_for(Fn&& fn) {
  if constexpr (I < N) { fn(IC<I>{}); static_for<I + 1, N>(fn); }
}
DI void drain() { asm volatile("s_waitcnt vmcnt(0)" ::: "memory"); }
template <int N> DI void wait_vm() { asm volatile("s_waitcnt vmcnt(%0)" ::"n"(N) : "memory"); }
DI void put_granule(unsigned char* ws, unsigned off, u64 tag16, unsigned payload) {
  __hip_atomic_store((gu64*)(ws + off), tag16 | (u64)(payload & 0xffffu), __ATOMIC_RELAXED, __HIP_MEMORY_SCOPE_AGENT);
}
template <int SLEEP> DI unsigned poll_granule(unsigned char* ws, unsigned off, u64 tag16) {
  u64 v = ~tag16;
  for (unsigned spins = 0;; ++spins) {
    if (v - tag16 > 0xffffull) v = __hip_atomic_load((gu64*)(ws + off), __ATOMIC_RELAXED, __HIP_MEMORY_SCOPE_AGENT);
    if (__all(v - tag16 <= 0xffffull) || spins > SPIN_CAP) break;
    __builtin_amdgcn_s_sleep(SLEEP);
  }
  __builtin_amdgcn_fence(__ATOMIC_ACQUIRE, "wavefront");
  return (unsigned)v & 0xffffu;
}
template <int SLEEP = 1> DI unsigned wait_lds_word(LAS unsigned char* p, unsigned want) {
  unsigned v = 0;
#pragma nounroll
  for (unsigned spins = 0; spins < (1u << 24); ++spins) {
    v = __builtin_amdgcn_readfirstlane(__hip_atomic_load((LAS unsigned*)p, __ATOMIC_RELAXED, __HIP_MEMORY_SCOPE_WORKGROUP));
    if (v >= want) break;
    __builtin_amdgcn_s_sleep(SLEEP);
  }
  asm volatile("" ::: "memory");
  return v;
}
template <int N> DI u64 wait_bar_epoch(const unsigned char* p) {
  u64 ep;
  asm volatile("s_load_dwordx2 %0, %1, 0x0\n\ts_waitcnt vmcnt(%2) lgkmcnt(0)\n\ts_barrier" : "=&s"(ep) : "s"(p), "n"(N) : "memory");
  return ep;
}
DI void bar_lds() { asm volatile("s_waitcnt lgkmcnt(0)\n\ts_barrier" ::: "memory"); }
DI void st_tile(LAS unsigned char* p, const f32x16& a) {
#pragma unroll
  for (int g = 0; g < 4; ++g) *(LAS f32x4*)(p + g * 1024) = f32x4{a[4 * g], a[4 * g + 1], a[4 * g + 2], a[4 * g + 3]};
}
DI void add_tile(f32x16& a, const LAS unsigned char* p) {
#pragma unroll
  for (int g = 0; g < 4; ++g) {
    const f32x4 v = *(const LAS f32x4*)(p + g * 1024);
    a[4 * g] += v[0]; a[4 * g + 1] += v[1]; a[4 * g + 2] += v[2]; a[4 * g + 3] += v[3];
  }
}

#define STAMP_DECL do {} while (0)
#define STAMP(id) do {} while (0)
#define QSTAMP(id) do {} while (0)
#define RSTAMP(id) do {} while (0)
#define STAMP_FLUSH(P, ph) do {} while (0)

DI void lds_arrive(LAS unsigned char* p) {
  asm volatile("s_mov_b64 exec, 1\n\tds_add_u32 %0, %1\n\ts_mov_b64 exec, -1" ::"v"((unsigned)(uintptr_t)p), "v"(1u) : "memory");
}

#define PINV(x) asm volatile("" : "+v"(x))
#define PINS(x) asm volatile("" : "+s"(x))
DI float sumsq(const u32x4& a, const u32x4& b) {
  typedef __bf16 bf2 __attribute__((ext_vector_type(2)));
  float n0 = 0.f, n1 = 0.f;
#pragma unroll
  for (int i = 0; i < 4; ++i) {
    n0 = __builtin_amdgcn_fdot2_f32_bf16(__builtin_bit_cast(bf2, a[i]), __builtin_bit_cast(bf2, a[i]), n0, false);
    n1 = __builtin_amdgcn_fdot2_f32_bf16(__builtin_bit_cast(bf2, b[i]), __builtin_bit_cast(bf2, b[i]), n1, false);
  }
  return n0 + n1;
}

DI void role_a(const Params& P, LAS unsigned char* lds, const int wt, const unsigned code_word) {
  const int lane = threadIdx.x & 63, r = lane & 31, h = lane >> 5;
  const int b = blockIdx.x, xcd = b & 7, idx = b >> 3;
  const unsigned lo = lane * 16;
  STAMP_DECL;
  STAMP(0);
  __builtin_amdgcn_s_setprio(2);
  unsigned xtouch = 0;
  if (wt < 2) xtouch = __builtin_amdgcn_raw_buffer_load_b32(wt ? mk_rsrc(P.wqkv, DM * 3 * DM * 2) : mk_rsrc(P.x, SEQ * DM * 2),
                                                            (unsigned)(wt ? b * 37 + (lane < 37 ? lane : 0) : b * 56 + (lane < 56 ? lane : 0)) * 128, 0, 0);
  SBAR();
  f32x16 acc[4];
  const int rb = (xcd & 1) * 8 + (idx & 7), j = (xcd >> 1) * 4 + (idx >> 3);
  const int head = xcd * 2 + (idx & 1), qb = idx >> 1;
  unsigned sto[3];
#pragma unroll
  for (int ct = 0; ct < 3; ++ct) {
    const int u = 3 * j + ct, part = u >> 4, t = (u & 15) * 64 + rb * 4 + wt;
    sto[ct] = lo + (part == 0 ? QF_OFF + t * 2048 : KV_OFF + t * 4096 + (part - 1) * 2048);
    PINV(sto[ct]);
  }
  unsigned padv = r == DH ? 0x3f803f80u : 0u;
  PINV(padv);
  const int gq = head / 3, gk = (16 + head) / 3, gvv = (32 + head) / 3;
  unsigned pub_off = GQ_OFF + (j * 16 + rb) * GQW + wt * GQS + (lane & 1) * 8;
  unsigned poll_off = GQ_OFF + (h ? gq * 16 + qb : (lane < 16 ? gk : gvv) * 16 + wt * 4 + ((lane >> 2) & 3)) * GQW + (lane & 3) * GQS + h * 8;
  unsigned chk_off = GQ_OFF + (gk * 16 + (lane >> 2)) * GQW + (lane & 3) * GQS;
  unsigned g_off = GG_OFF + ((qb * 4 + wt) * 32 + r) * 8;
  unsigned o_off = lo + OF_OFF + ((qb * 4 + wt) * 16 + head) * 2048;
  PINV(pub_off); PINV(poll_off); PINV(chk_off); PINV(g_off); PINV(o_off);
  unsigned kvbase = KV_OFF + (head * 64 + wt * 16) * 4096, go_off = GO_OFF + ((qb * 4 + wt) * 16 + head) * 128;
  unsigned qofs[4];
#pragma unroll
  for (int t = 0; t < 4; ++t) { qofs[t] = QF_OFF + (unsigned)(head * 64 + qb * 4 + ((wt + 1 + t) & 3)) * 2048; PINS(qofs[t]); }
  PINS(kvbase); PINS(go_off);
  unsigned xoff[4];
#pragma unroll
  for (int ks = 0; ks < 4; ++ks) xoff[ks] = (unsigned)(L_X + wt * 4096 + r * 128 + (((2 * ks + h) ^ ((r >> 1) & 7)) << 4));
  const unsigned wqoff = (unsigned)(L_WQ + (8 * h + ((lane & 15) >> 2)) * 192 + ((lane >> 4) & 1) * 32 + (lane & 3) * 8 + (j & 1) * 8);
  u64 ep;
  asm volatile("s_load_dwordx2 %0, %1, 0x0\n\ts_waitcnt lgkmcnt(0)" : "=&s"(ep) : "s"(P.ws + FL_OFF) : "memory");
  u64 tag16 = (ep + 1) << 16;
  unsigned char* wsb = P.ws + SET_BASE + (((unsigned)ep + 1u) & 3u) * SET_STRIDE;
  PINS(tag16); PINS(wsb);
  const __amdgpu_buffer_rsrc_t rsWs = mk_rsrc(wsb, WS_END);
#pragma unroll
  for (int i = 0; i < 3; ++i) acc[i] = f32x16{};
  unsigned wtouch = 0;
  auto kloop = [&](auto nvc) {
    constexpr int NV = decltype(nvc)::value;
    bf16x8 xf[4], wf[4][3];
    auto mm = [&](int ks, int ct) { acc[ct] = ct >= 3 - NV ? MFMA(xf[ks], wf[ks][ct], acc[ct]) : MFMA(wf[ks][ct], xf[ks], acc[ct]); };
    auto step = [&](int c, int ks, bool domm) {
      const int km = (ks + 2) & 3;
#pragma unroll
      for (int ct = 0; ct < 3; ++ct) {
        if (domm) mm(km, ct);
        SBAR();
        if (ct == 0) xf[ks] = *(const LAS bf16x8*)(lds + xoff[ks] + c * 16384);
        wf[ks][ct] = tr_frag(lds + wqoff + (c & 3) * 12288 + ks * 16 * 192 + ct * 56, 4 * 192);
        SBAR();
      }
    };
#pragma unroll
    for (int c = 0; c < 7; ++c) {
      bar_lds();
      if (c == 0) STAMP(1);
      if (c == MPF) {
        const unsigned l = (unsigned)(b * 128 + (wt & 1) * 64 + lane) * 128;
        if (wt < 2) wtouch = l < DM * FF * 2 ? __builtin_amdgcn_raw_buffer_load_b32(mk_rsrc(P.w1, DM * FF * 2), l, 0, 0)
                           : l < 2 * DM * FF * 2 ? __builtin_amdgcn_raw_buffer_load_b32(mk_rsrc(P.w2, DM * FF * 2), l - DM * FF * 2, 0, 0)
                                                 : __builtin_amdgcn_raw_buffer_load_b32(mk_rsrc(P.wo, DM * DM * 2), l - 2 * DM * FF * 2, 0, 0);
        SBAR();
      }
      QSTAMP(1 + c);
      step(c, 0, c > 0);
      step(c, 1, c > 0);
      step(c, 2, true);
      step(c, 3, true);
    }
    bar_lds();
    QSTAMP(8);
#pragma unroll
    for (int ct = 0; ct < 3; ++ct) { mm(2, ct); mm(3, ct); SBAR(); }
  };
  auto epi = [&](auto nqc, auto nvc) {
    constexpr int NQ = decltype(nqc)::value, NV = decltype(nvc)::value;
    STAMP(2);
    asm volatile("" ::"v"(code_word), "v"(wtouch), "v"(xtouch));
    u32x4 t0[3], t1[3];
#pragma unroll
    for (int ct = 0; ct < 3; ++ct) {
      if (ct < NQ) acc[ct] *= QSCALE;
      t0[ct] = as_u4(pack8(acc[ct], 0));
      if (ct >= 3 - NV) {
#pragma unroll
        for (int i = 0; i < 4; ++i) t0[ct][i] = r < DH ? t0[ct][i] : padv;
      }
      st16(rsWs, t0[ct], sto[ct], 0);
      t1[ct] = as_u4(pack8(acc[ct], 1));
      if (ct >= 3 - NV) {
#pragma unroll
        for (int i = 0; i < 4; ++i) t1[ct][i] = r < DH ? t1[ct][i] : padv;
      } else {
        t1[ct][2] = h ? (ct < NQ ? 0u : 0x3f803f80u) : t1[ct][2];
        t1[ct][3] = h ? (ct < NQ ? 0u : 0x00003f80u) : t1[ct][3];
      }
      st16(rsWs, t1[ct], sto[ct], 1024);
      SBAR();
    }
    STAMP(8);
    unsigned mq = 0, mk = 0;
#pragma unroll
    for (int ct = 0; ct < 3; ++ct) {
      const unsigned n = __float_as_uint(sumsq(t0[ct], t1[ct])) & 0x7fffffffu;
      if (ct < NQ) mq = max(mq, n); else mk = max(mk, n);
    }
    auto bound = [&](unsigned m) {
      m = row16_umax(m);
      const float lo2 = __uint_as_float(max((unsigned)__builtin_amdgcn_readlane(m, 0), (unsigned)__builtin_amdgcn_readlane(m, 16)));
      const float hi2 = __uint_as_float(max((unsigned)__builtin_amdgcn_readlane(m, 32), (unsigned)__builtin_amdgcn_readlane(m, 48)));
      return ((__float_as_uint(lo2 + hi2) >> 16) + 1u) & 0xffffu;
    };
    const unsigned pq = NQ > 0 ? bound(mq) : 0u, pk = NQ < 3 ? bound(mk) : 0u;
    u64 gran = tag16 | ((lane & 1) ? pq : pk);
    PINV(gran);
    drain();
    STAMP(9);
    if (lane < 2) __hip_atomic_store((gu64*)(wsb + pub_off), gran, __ATOMIC_RELAXED, __HIP_MEMORY_SCOPE_AGENT);
  };
  if (j < 10) {
    kloop(IC<0>{});
    if (j < 5) epi(IC<3>{}, IC<0>{});
    else if (j == 5) epi(IC<1>{}, IC<0>{});
    else epi(IC<0>{}, IC<0>{});
  } else if (j == 10) {
    kloop(IC<1>{});
    epi(IC<0>{}, IC<1>{});
  } else {
    kloop(IC<3>{});
    epi(IC<0>{}, IC<3>{});
  }
  STAMP(3);
  __builtin_amdgcn_s_sleep(PDELAY);
  const unsigned pay = poll_granule<POLL_A>(wsb, poll_off, tag16);
  STAMP(4);
  u32x4 q[4][2];
  LAS unsigned char* const cmb = lds + L_CMB + wt * 12288 + lo;
  u64 gv = 0;
  unsigned thr = 0;
#pragma nounroll
  for (int pass = 0;; ++pass) {
    u32x4 kv[KVD][4];
    kv[0][0] = ld16c(rsWs, lo, kvbase);
    SBAR();
    if (pass == 0) { q[0][0] = ld16c(rsWs, lo, qofs[0]); q[0][1] = ld16c(rsWs, lo + 1024, qofs[0]); }
    SBAR();
    kv[0][1] = ld16c(rsWs, lo + 1024, kvbase);
    SBAR();
    if (pass == 0) {
#pragma unroll
      for (int t = 1; t < 4; ++t) { q[t][0] = ld16c(rsWs, lo, qofs[t]); q[t][1] = ld16c(rsWs, lo + 1024, qofs[t]); SBAR(); }
    }
#pragma unroll
    for (int e = 2; e < 4 * (KVD - 1); ++e) { kv[e >> 2][e & 3] = ld16c(rsWs, lo + (e & 3) * 1024, kvbase + (e >> 2) * 4096); SBAR(); }
#pragma unroll
    for (int i = 0; i < 4; ++i) acc[i] = f32x16{};
    {
      const float q2 = __uint_as_float(__builtin_amdgcn_readlane(row16_umax(pay), 32) << 16);
      const float lim = SAFE_BOUND * SAFE_BOUND * __builtin_amdgcn_rcpf(q2);
      thr = lim < 1e30f ? __float_as_uint(lim) >> 16 : (lim >= 1e30f ? 0x7000u : 0u);
      PINV(thr);
    }
    __builtin_amdgcn_s_setprio(1);
    f32x16 sc = MFMA(as_frag(kv[0][0]), as_frag(q[0][0]), f32x16{});
    sc = MFMA(as_frag(kv[0][1]), as_frag(q[0][1]), sc);
    STAMP(5);
    u32x4 p0, p1 = {0, 0, 0, 0};
    float e0 = 0.f, e1 = 0.f, e2 = 0.f, e3 = 0.f;
    u64 ck = 0, cv = 0;
#define EXP4(i) do { e0 = __builtin_amdgcn_exp2f(sc[i]); e1 = __builtin_amdgcn_exp2f(sc[(i) + 1]); e2 = __builtin_amdgcn_exp2f(sc[(i) + 2]); e3 = __builtin_amdgcn_exp2f(sc[(i) + 3]); \
    asm volatile("" : "+v"(e0), "+v"(e1), "+v"(e2), "+v"(e3)); SBAR(); } while (0)
#define CVT2(P, i) do { P[i] = cvtpk(e0, e1); P[(i) + 1] = cvtpk(e2, e3); asm volatile("" : "+v"(P)); SBAR(); } while (0)
#define PART(t, g) do { *(LAS f32x4*)(cmb + (t) * 4096 + (g) * 1024) = f32x4{acc[t][4 * (g)], acc[t][4 * (g) + 1], acc[t][4 * (g) + 2], acc[t][4 * (g) + 3]}; SBAR(); } while (0)
    static_for<0, 64>([&](auto ic) {
      constexpr int i = decltype(ic)::value;
      constexpr int kt = i >> 2, t = i & 3, kn_ = (i + 1) >> 2, tn = (i + 1) & 3, kp = (i - 1) >> 2, tp = (i - 1) & 3;
      constexpr int kl = kt + KVD - 1;
      if (i == 24) STAMP(11);
      if (i == 56) (void)wait_lds_word(lds + L_CTRL, 4);
      f32x16 sn;
      SBAR();
      if (i < 63) sn = MFMA(as_frag(kv[kn_ % KVD][0]), as_frag(q[tn][0]), f32x16{});
      SBAR();
      if constexpr (i >= 62) PART(i - 62, 0);
      if (i > 0) CVT2(p1, 2);
      EXP4(0);
      if (i > 0) acc[tp] = MFMA(as_frag(kv[(kp + KVD) % KVD][3]), as_frag(p1), acc[tp]);
      SBAR();
      if constexpr (t == 0 && kl < 16) {
#pragma unroll
        for (int e = 0; e < 4; ++e) kv[kl % KVD][e] = ld16c(rsWs, lo + e * 1024, kvbase + kl * 4096);
      }
      if constexpr (t == 2 && kt + 1 < 16) {
        constexpr int n = 4 * ((kl < 16 ? kl : 15) - (kt + 1)) + (i > 53 ? 3 : 0);
        __builtin_amdgcn_s_waitcnt(0x0f70 | (n & 15) | ((n >> 4) << 14));
      }
      if (i == 51) gv = __hip_atomic_load((gu64*)(wsb + g_off), __ATOMIC_RELAXED, __HIP_MEMORY_SCOPE_AGENT);
      if (i == 52) ck = __hip_atomic_load((gu64*)(wsb + chk_off), __ATOMIC_RELAXED, __HIP_MEMORY_SCOPE_AGENT);
      if (i == 53) cv = __hip_atomic_load((gu64*)(wsb + (gvv - gk) * 16 * GQW + chk_off), __ATOMIC_RELAXED, __HIP_MEMORY_SCOPE_AGENT);
      SBAR();
      if constexpr (i >= 62) PART(i - 62, 1);
      CVT2(p0, 0);
      EXP4(4);
      if (i < 63) sn = MFMA(as_frag(kv[kn_ % KVD][1]), as_frag(q[tn][1]), sn);
      SBAR();
      if constexpr (i >= 62) PART(i - 62, 2);
      CVT2(p0, 2);
      EXP4(8);
      acc[t] = MFMA(as_frag(kv[kt % KVD][2]), as_frag(p0), acc[t]);
      SBAR();
      if constexpr (i >= 62) PART(i - 62, 3);
      CVT2(p1, 0);
      EXP4(12);
      if (i < 63) sc = sn;
    });
    CVT2(p1, 2);
    acc[3] = MFMA(as_frag(kv[15 % KVD][3]), as_frag(p1), acc[3]);
    SBAR();
    PART(2, 0); PART(2, 1); PART(2, 2); PART(2, 3);
#undef EXP4
#undef CVT2
#undef PART
    __builtin_amdgcn_s_setprio(2);
    if (__builtin_expect(__all(ck - tag16 <= (u64)thr && cv - tag16 <= 0x7000ull && gv - tag16 <= 0xffffull), 1)) break;
    gv = poll_granule<2>(wsb, g_off, tag16);
    if (pass) break;
    const unsigned pk = poll_granule<2>(wsb, chk_off, tag16), pv = poll_granule<2>(wsb, (gvv - gk) * 16 * GQW + chk_off, tag16);
    if (__all(pk <= thr && pv <= 0x7000u)) break;
    float mx[4];
#pragma unroll
    for (int t = 0; t < 4; ++t) mx[t] = -INFINITY;
    for (int kt = 0; kt < 64; ++kt) {
      const u32x4 k0 = ld16c(rsWs, lo, KV_OFF + (head * 64 + kt) * 4096), k1 = ld16c(rsWs, lo + 1024, KV_OFF + (head * 64 + kt) * 4096);
#pragma unroll
      for (int t = 0; t < 4; ++t) {
        f32x16 s = MFMA(as_frag(k0), as_frag(q[t][0]), f32x16{});
        s = MFMA(as_frag(k1), as_frag(q[t][1]), s);
#pragma unroll
        for (int i = 0; i < 16; ++i) mx[t] = fmaxf(mx[t], s[i]);
      }
    }
#pragma unroll
    for (int t = 0; t < 4; ++t) {
      const float nm = -fmaxf(mx[t], swap_half(mx[t]));
      const unsigned hi = __float_as_uint(nm) & 0xffff0000u;
      const float r1 = nm - __uint_as_float(hi);
      const unsigned mid = __float_as_uint(r1) & 0xffff0000u;
      const float r2 = r1 - __uint_as_float(mid);
      if (h) { q[t][1][2] = (hi >> 16) | mid; q[t][1][3] = __float_as_uint(r2) >> 16; }
    }
  }
  STAMP(6);
  asm volatile("s_waitcnt lgkmcnt(0)" ::: "memory");
  lds_arrive(lds + L_CTRL + 20);
  (void)wait_lds_word(lds + L_CTRL + 20, 4);
  f32x16 o = acc[3];
#pragma unroll
  for (int u = 0; u < 3; ++u) add_tile(o, lds + L_CMB + ((wt + 3 - u) & 3) * 12288 + u * 4096 + lo);
  const auto lsw = __builtin_amdgcn_permlane32_swap(__float_as_uint(o[12]), __float_as_uint(o[12]), false, false);
  const float inv = __uint_as_float((unsigned)gv << 16) * __builtin_amdgcn_rcpf(__uint_as_float(lsw[1]));
  o *= inv;
  st16(rsWs, as_u4(pack8(o, 0)), o_off, 0);
  u32x4 o1 = as_u4(pack8(o, 1));
  o1[2] = h ? 0u : o1[2];
  o1[3] = h ? 0u : o1[3];
  st16(rsWs, o1, o_off, 1024);
  STAMP(7);
  drain();
  if (lane == 0) __hip_atomic_store((gu64*)(wsb + go_off), tag16, __ATOMIC_RELAXED, __HIP_MEMORY_SCOPE_AGENT);
  STAMP(10);
  STAMP_FLUSH(P, 0);
}

#define W2S 6
#define HPRE 6
#define NPRE 14
#define LEA 5
#define LEB 11
#define LE0 16
#define LU0 3
#define F1LA 2
#define PACE 2
#define PACE2 2
constexpr int ffn2_tail(int f) { return f + HPRE < 18 ? 4 : 0; }
constexpr int ffn2_ops(int f) { return (f + W2S < 14 ? 4 : 0) + ffn2_tail(f); }
constexpr int ffn2_younger(int f, int upto) {
  int n = ffn2_tail(f - W2S);
  for (int i = f - W2S + 1; i < upto; ++i) n += ffn2_ops(i);
  return n;
}

DI void role_f(const Params& P, LAS unsigned char* lds, const int wt) {
  u64 ep;
  const int lane = threadIdx.x & 63, r = lane & 31, h = lane >> 5;
  const int b = blockIdx.x, xcd = b & 7, idx = b >> 3;
  const int rb = (xcd & 1) * 8 + (idx & 7), j = (xcd >> 1) * 4 + (idx >> 3);
  const unsigned lo = lane * 16, lds0 = (unsigned)(uintptr_t)lds;
  const bool ffn = j < 14;
  STAMP_DECL;
  STAMP(0);
  f32x16 acc[4];
  u32x4 wl[28];
  const __amdgpu_buffer_rsrc_t rsL = mk_rsrc(ffn ? P.w1 : P.gw, ffn ? DM * FF * 2 : DM * 2);
  const unsigned lsrc = ffn ? (unsigned)((lane >> 2) * (FF * 2) + (j * 4 + wt) * 64 + (lane & 3) * 16) : (unsigned)(h * 16);
  const unsigned lstep = ffn ? 16 * FF * 2 : 32;
  const int rot = ROT1(rb, j);
  auto kc = [&](int c) { const int v = c + rot; return v >= 7 ? v - 7 : v; };
  auto L = [&](int u0, int u1) {
#pragma unroll
    for (int u = u0; u < u1; ++u) wl[u] = ld16(rsL, lsrc, (kc(u >> 2) * 4 + (u & 3)) * lstep);
    SBAR();
  };
  {
    const __amdgpu_buffer_rsrc_t rsX = mk_rsrc(P.x, SEQ * DM * 2);
    const __amdgpu_buffer_rsrc_t rsW = mk_rsrc(P.wqkv, DM * 3 * DM * 2);
    unsigned xsrc[2], wsrc[3];
#pragma unroll
    for (int i = 0; i < 2; ++i) {
      const int row = 32 * wt + 8 * i + (lane >> 3), ch = (lane & 7) ^ ((row >> 1) & 7);
      xsrc[i] = (unsigned)((rb * 128 + row) * (DM * 2) + ch * 16);
    }
#pragma unroll
    for (int i = 0; i < 3; ++i) {
      const int p = 64 * (3 * wt + i) + lane, kr = p / 12, ch = p % 12;
      wsrc[i] = (unsigned)(kr * (3 * DM * 2) + j * 168 - (j & 1) * 8 + (ch < 11 ? ch : 10) * 16);
    }
    auto X = [&](int c) {
#pragma unroll
      for (int i = 0; i < 4; ++i) glds16(rsX, lds0 + L_X + c * 16384 + (4 * wt + i) * 1024, xsrc[i & 1], kc(c) * 128 + (i >> 1) * 16 * DM * 2);
    };
    auto W = [&](int c) {
#pragma unroll
      for (int i = 0; i < 3; ++i) glds16(rsW, lds0 + L_WQ + (c & 3) * 12288 + (3 * wt + i) * 1024, wsrc[i], kc(c) * 64 * 3 * DM * 2);
    };
    X(0); W(0); X(1); W(1); X(2);
    QSTAMP(1);
    wait_bar<11>();
    STAMP(1);
    W(2); X(3); W(3);
    QSTAMP(2);
    wait_bar<14>();
    X(4); W(4);
    QSTAMP(3);
    wait_bar<14>();
    X(5); W(5);
    QSTAMP(4);
    wait_bar<14>();
    X(6); W(6);
    QSTAMP(5);
    wait_bar<14>();
    if (wt == 0 && lane < 8) *(LAS unsigned*)(lds + L_CTRL + lane * 4) = 0u;
    L(0, LEA);
    QSTAMP(6);
    ep = wait_bar_epoch<7 + LEA>(P.ws + FL_OFF);
    L(LEA, LEB);
    QSTAMP(7);
    wait_bar<LEB>();
    L(LEB, LE0);
    QSTAMP(8);
    bar_lds();
    __builtin_amdgcn_s_sleep(FDELAY);
    __builtin_amdgcn_s_setprio(F1PRIO);
  }
  STAMP(2);
  const u64 tag = (ep + 1) << 16;
  unsigned char* const wsb = P.ws + SET_BASE + (((unsigned)ep + 1u) & 3u) * SET_STRIDE;
  const __amdgpu_buffer_rsrc_t rsWs = mk_rsrc(wsb, WS_END);
  unsigned xoff[4];
#pragma unroll
  for (int ks = 0; ks < 4; ++ks) xoff[ks] = (unsigned)(L_X + r * 128 + (((2 * ks + h) ^ ((r >> 1) & 7)) << 4));
  const int mb = 4 * xcd + (idx & 3), nb = idx >> 2;
  const __amdgpu_buffer_rsrc_t rsW2 = mk_rsrc(P.w2, FF * DM * 2), rsH = rsWs;
  const unsigned w2src = (unsigned)((wt * 448 + (lane >> 3)) * (DM * 2) + nb * 128 + ((((lane >> 2) & 1) ^ ((lane >> 4) & 1)) * 64) + (lane & 3) * 16);
  const unsigned ring0 = lds0 + L_W2 + wt * (W2S * 4096);
  const int rot2 = ROT2;
  auto fr = [&](int f) { const int v = f + rot2; return v >= 14 ? v - 14 : v; };
  auto D = [&](int f) {
#pragma unroll
    for (int i = 0; i < 4; ++i) glds16(rsW2, ring0 + (f % W2S) * 4096 + i * 1024, w2src, (fr(f) * 32 + i * 8) * DM * 2);
  };
  const bool has_tile = idx < 28;
  auto fill_ring = [&]() {
    if (has_tile) {
      (void)wait_lds_word(lds + L_CTRL, 4);
#pragma unroll
      for (int f = 0; f < DEARLY; ++f) { D(f); wait_vm<FTHR>(); __builtin_amdgcn_s_sleep(PACE); }
    }
  };
  if (ffn) {
    LAS unsigned char* ring = lds + L_W1 + wt * 8192;
    const unsigned troff = (unsigned)((8 * h + ((lane & 15) >> 2)) * 64 + ((lane >> 4) & 1) * 32 + (lane & 3) * 8);
#pragma unroll
    for (int i = 0; i < 4; ++i) acc[i] = f32x16{};
    RSTAMP(1);
    constexpr int LA = F1LA;
    bf16x8 xf[LA + 1][4], wf[LA + 1];
    auto wr = [&](int u) { *(LAS u32x4*)(ring + (u & 7) * 1024 + lo) = wl[u]; };
    auto rdw = [&](int u) { wf[u % (LA + 1)] = tr_frag(ring + (u & 7) * 1024 + troff, 4 * 64); };
    auto rdx = [&](int u, int t) { xf[u % (LA + 1)][t] = *(const LAS bf16x8*)(lds + xoff[u & 3] + (u >> 2) * 16384 + t * 4096); };
#pragma unroll
    for (int u = 0; u < LA + 2; ++u) wr(u);
#pragma unroll
    for (int u = 0; u < LA; ++u) {
      rdw(u);
#pragma unroll
      for (int t = 0; t < 4; ++t) rdx(u, t);
    }
    SBAR();
#pragma unroll
    for (int u = 0; u < 28; ++u) {
      if ((u & 3) == 0) RSTAMP(2 + (u >> 2));
#pragma unroll
      for (int t = 0; t < 4; ++t) {
        if (u + LA < 28) {
          if (t == 1) rdw(u + LA);
          rdx(u + LA, t);
          if (t == 3 && u + LA + 2 < 28) wr(u + LA + 2);
        }
        SBAR();
        acc[t] = MFMA(wf[u % (LA + 1)], xf[u % (LA + 1)][t], acc[t]);
        SBAR();
        if (u >= LU0 && LE0 + (u - LU0) * 4 + t < 28) L(LE0 + (u - LU0) * 4 + t, LE0 + (u - LU0) * 4 + t + 1);
      }
    }
    lds_arrive(lds + L_CTRL);
    RSTAMP(9);
    STAMP(3);
    __builtin_amdgcn_s_setprio(3);
#pragma unroll
    for (int t = 0; t < 4; ++t) {
      f32x16 z = acc[t];
#pragma unroll
      for (int i = 0; i < 16; ++i) z[i] = z[i] * __builtin_amdgcn_rcpf(1.f + __builtin_amdgcn_exp2f(-LOG2E * z[i]));
      const unsigned so = HF_OFF + ((rb * 4 + t) * 56 + j * 4 + wt) * 2048;
      st16(rsWs, as_u4(pack8(z, 0)), lo, so);
      st16(rsWs, as_u4(pack8(z, 1)), lo + 1024, so);
    }
    STAMP(11);
    drain();
    if (lane == 0) put_granule(wsb, GH_OFF + (rb * 16 + j) * 128 + wt * 8, tag, 0);
  } else {
    __builtin_amdgcn_s_setprio(3);
    if (j == 14) {
      L(LE0, 28);
      acc[0] = f32x16{};
#pragma unroll
      for (int u = 0; u < 28; ++u) {
        const bf16x8 xf = *(const LAS bf16x8*)(lds + xoff[u & 3] + (u >> 2) * 16384 + wt * 4096);
        acc[0] = MFMA(as_frag(wl[u]), xf, acc[0]);
      }
      const float g = __builtin_amdgcn_rcpf(1.f + __builtin_amdgcn_exp2f(-LOG2E * acc[0][0]));
      if (h == 0) put_granule(wsb, GG_OFF + ((rb * 4 + wt) * 32 + r) * 8, tag, cvtpk(g, 0.f));
    }
    lds_arrive(lds + L_CTRL);
    __builtin_amdgcn_s_sleep(WDELAY);
    const __amdgpu_buffer_rsrc_t rsO = mk_rsrc(P.wo, DM * DM * 2);
    const int id0 = ((j - 14) * 16 + rb) * 4 + wt;
    for (int id = id0; id < 224; id += 128) {
      const int nt = id >> 4, hd = id & 15;
      unsigned rp[16];
#pragma unroll
      for (int e = 0; e < 16; ++e)
        rp[e] = __builtin_amdgcn_raw_buffer_load_b16(rsO, (unsigned)(((4 * h + 8 * (e >> 2) + (e & 3)) * DM + r) * 2), (unsigned)((DH * hd * DM + 32 * nt) * 2), 0);
      u32x4 a, c2;
#pragma unroll
      for (int i = 0; i < 4; ++i) {
        a[i] = (rp[2 * i] & 0xffffu) | (rp[2 * i + 1] << 16);
        c2[i] = (rp[8 + 2 * i] & 0xffffu) | (rp[8 + 2 * i + 1] << 16);
      }
      c2[2] = h ? 0u : c2[2];
      c2[3] = h ? 0u : c2[3];
      st16(rsWs, a, lo, WOF_OFF + id * 2048);
      st16(rsWs, c2, lo + 1024, WOF_OFF + id * 2048);
      drain();
      if (lane == 0) put_granule(wsb, GW_OFF + id * 8, tag, 0);
    }
  }
  STAMP(4);
  if (idx >= 28) {
    if (b == 255 && wt == 0) {
      __builtin_amdgcn_s_sleep(127);
#pragma unroll
      for (int i = 0; i < 4; ++i) (void)poll_granule<64>(wsb, GQ_OFF + (i * 64 + lane) * GQW, tag);
      if (lane == 0) __hip_atomic_store((gu64*)(P.ws + FL_OFF), ep + 1, __ATOMIC_RELAXED, __HIP_MEMORY_SCOPE_AGENT);
    }
    STAMP(10);
    STAMP_FLUSH(P, 0);
    return;
  }
  fill_ring();
  const float gl = __uint_as_float(poll_granule<8>(wsb, GG_OFF + (mb * 64 + lane) * 8, tag) << 16), gs = swap_half(gl);
  const float og0 = 1.f - (h ? gs : gl), og1 = 1.f - (h ? gl : gs);
  __builtin_amdgcn_s_sleep(HDELAY);
  const int ftl = wt * 14 + ((lane & 15) < 14 ? (lane & 15) : 0);
  (void)poll_granule<8>(wsb, GH_OFF + ((mb >> 1) * 16 + (ftl >> 2)) * 128 + (ftl & 3) * 8, tag);
  STAMP(5);
  u32x4 hb[HPRE][4], wo[16];
  const unsigned hbase = HF_OFF + (mb * 2 * 56 + wt * 14) * 2048;
  auto H = [&](int f) {
#pragma unroll
    for (int e = 0; e < 4; ++e) hb[f % HPRE][e] = ld16c(rsH, lo + (e & 1) * 1024, hbase + ((e >> 1) * 56 + fr(f)) * 2048);
  };
#pragma unroll
  for (int f = 0; f < (HPRE > W2S ? HPRE : W2S); ++f) {
    if (f >= DEARLY && f < W2S) D(f);
    if (f < HPRE) H(f);
    SBAR(); wait_vm<FTHR>(); __builtin_amdgcn_s_sleep(PACE); SBAR();
  }
  if (DEARLY < W2S) drain();
  const int q4 = (lane & 15) >> 2;
  unsigned woff[2];
#pragma unroll
  for (int nt = 0; nt < 2; ++nt) woff[nt] = (unsigned)(L_W2 + wt * (W2S * 4096) + (4 * h + q4) * 128 + ((nt ^ (q4 >> 1)) * 64) + ((lane >> 4) & 1) * 32 + (lane & 3) * 8);
#pragma unroll
  for (int i = 0; i < 4; ++i) acc[i] = f32x16{};
  STAMP(6);
  __builtin_amdgcn_s_setprio(0);
  bf16x8 wf[2][2][2];
  auto RW = [&](int f) {
#pragma unroll
    for (int s = 0; s < 2; ++s)
#pragma unroll
      for (int nt = 0; nt < 2; ++nt) wf[f & 1][s][nt] = tr_frag(lds + woff[nt] + (f % W2S) * 4096 + s * 2048, 1024);
  };
  RW(0);
  static_for<0, 14>([&](auto fc) {
    constexpr int f = decltype(fc)::value;
    if constexpr (f == 14 - HPRE) {
      PINV(acc[0]); PINV(acc[1]); PINV(acc[2]); PINV(acc[3]);
      (void)poll_granule<8>(wsb, GW_OFF + ((nb * 2 + ((lane >> 2) & 1)) * 16 + wt * 4 + (lane & 3)) * 8, tag);
    }
    if constexpr (f == NPRE) {
      PINV(acc[0]); PINV(acc[1]); PINV(acc[2]); PINV(acc[3]);
      STAMP(11);
      (void)wait_lds_word<2>(lds + L_CTRL + 20, TRIG);
    }
    asm volatile("s_waitcnt lgkmcnt(0)" ::: "memory");
    SBAR();
    if constexpr (f + 1 < 14) {
      if constexpr (f + 1 >= W2S) wait_vm<ffn2_younger(f + 1, f)>();
      RW(f + 1);
    }
    SBAR();
#pragma unroll
    for (int u = 0; u < 8; ++u) {
      const int s = u >> 2, nt = (u >> 1) & 1, mt = u & 1;
      acc[nt * 2 + mt] = MFMA(wf[f & 1][s][nt], as_frag(hb[f % HPRE][mt * 2 + s]), acc[nt * 2 + mt]);
      SBAR();
      if (u < 4) {
        if constexpr (f + W2S < 14) glds16(rsW2, ring0 + (f % W2S) * 4096 + u * 1024, w2src, (fr(f + W2S) * 32 + u * 8) * DM * 2);
      } else {
        const int e = u == 4 ? 0 : u == 5 ? 2 : u == 6 ? 1 : 3;
        if constexpr (f + HPRE < 14) hb[f % HPRE][e] = ld16c(rsH, lo + (e & 1) * 1024, hbase + ((e >> 1) * 56 + fr(f + HPRE)) * 2048);
        else if constexpr (f + HPRE < 18) {
          const int x = (f + HPRE - 14) * 4 + u - 4;
          wo[x] = ld16c(rsWs, lo, WOF_OFF + ((nb * 2 + (x >> 3)) * 16 + wt * 4) * 2048 + (x & 7) * 1024);
        }
      }
      SBAR();
    }
    if constexpr (f < NPRE) { wait_vm<FTHR>(); __builtin_amdgcn_s_sleep(PACE2); }
    SBAR();
  });
  STAMP(7);
  __builtin_amdgcn_s_setprio(3);
#pragma unroll
  for (int i = 0; i < 16; ++i) { acc[0][i] *= og0; acc[1][i] *= og1; acc[2][i] *= og0; acc[3][i] *= og1; }
  __builtin_amdgcn_s_sleep(ODELAY);
  (void)poll_granule<POLL_A>(wsb, GO_OFF + ((mb * 2 + ((lane >> 2) & 1)) * 16 + wt * 4 + (lane & 3)) * 128, tag);
  STAMP(8);
  u32x4 of[16];
#pragma unroll
  for (int e = 0; e < 8; ++e)
#pragma unroll
    for (int mt = 0; mt < 2; ++mt) of[mt * 8 + e] = ld16c(rsWs, lo, OF_OFF + ((mb * 2 + mt) * 16 + wt * 4) * 2048 + e * 1024);
  SBAR();
#pragma unroll
  for (int e = 0; e < 8; ++e)
#pragma unroll
    for (int nt = 0; nt < 2; ++nt)
#pragma unroll
      for (int mt = 0; mt < 2; ++mt) acc[nt * 2 + mt] = MFMA(as_frag(wo[nt * 8 + e]), as_frag(of[mt * 8 + e]), acc[nt * 2 + mt]);
#pragma unroll
  for (int t = 0; t < 4; ++t) st_tile(lds + L_W2 + wt * (W2S * 4096) + t * 4096 + lo, acc[t]);
  STAMP(9);
  bar_lds();
  f32x16 a = f32x16{};
#pragma unroll
  for (int f = 0; f < 4; ++f) add_tile(a, lds + L_W2 + f * (W2S * 4096) + wt * 4096 + lo);
  unsigned short* dst = P.out + (size_t)((mb * 2 + (wt & 1)) * 32 + r) * DM + (nb * 2 + (wt >> 1)) * 32 + 8 * h;
#pragma unroll
  for (int k = 0; k < 2; ++k) {
    const auto x = __builtin_amdgcn_permlane32_swap(cvtpk(a[8 * k], a[8 * k + 1]), cvtpk(a[8 * k + 4], a[8 * k + 5]), false, false);
    const auto y = __builtin_amdgcn_permlane32_swap(cvtpk(a[8 * k + 2], a[8 * k + 3]), cvtpk(a[8 * k + 6], a[8 * k + 7]), false, false);
    __builtin_amdgcn_raw_buffer_store_b128(u32x4{x[0], y[0], x[1], y[1]}, mk_rsrc(P.out, SEQ * DM * 2), (unsigned)((dst - P.out + 16 * k) * 2), 0, OAUX);
  }
  STAMP(10);
  STAMP_FLUSH(P, 0);
}

extern __shared__ __attribute__((aligned(16))) unsigned char g_lds[];

__global__ __launch_bounds__(512, 2) void k_fused(Params P) {
  u64 pc;
  asm volatile("s_getpc_b64 %0" : "=s"(pc));
  const unsigned code_word = __builtin_amdgcn_raw_buffer_load_b32(mk_rsrc((const void*)(pc & ~63ull), CODE_PF),
      (threadIdx.x & 256) * 4096 + (((blockIdx.x >> 3) * 32 + ((threadIdx.x >> 3) & 24) + (threadIdx.x & 7)) * 64), 0, 0);
  SBAR();
  LAS unsigned char* lds = (LAS unsigned char*)g_lds;
  const int w = __builtin_amdgcn_readfirstlane(threadIdx.x >> 6);
  if (w < 4) role_a(P, lds, w, code_word);
  else role_f(P, lds, w - 4);
}

extern "C" void kernel_launch(void* const* d_in, const int* in_sizes, int n_in, void* d_out, int out_size, void* d_ws,
                              size_t ws_size, hipStream_t stream) {
  Params P;
  P.x = (const unsigned short*)d_in[0];
  P.wqkv = (const unsigned short*)d_in[1];
  P.wo = (const unsigned short*)d_in[2];
  P.w1 = (const unsigned short*)d_in[4];
  P.w2 = (const unsigned short*)d_in[5];
  P.gw = (const unsigned short*)d_in[6];
  P.out = (unsigned short*)d_out;
  P.ws = (unsigned char*)d_ws;
  (void)hipFuncSetAttribute((const void*)k_fused, hipFuncAttributeMaxDynamicSharedMemorySize, LDS_BYTES);
  k_fused<<<256, 512, LDS_BYTES, stream>>>(P);
}
```

```cpp
#define HDELAY 16
#define FDELAY 12
#include <hip/hip_runtime.h>
#include <hip/hip_bf16.h>
#include <stdint.h>

typedef short bf16x8 __attribute__((ext_vector_type(8)));
typedef short s16x4 __attribute__((ext_vector_type(4)));
typedef float f32x16 __attribute__((ext_vector_type(16)));
typedef float f32x4 __attribute__((ext_vector_type(4)));
typedef unsigned u32x4 __attribute__((ext_vector_type(4)));
typedef unsigned u32x2 __attribute__((ext_vector_type(2)));
#define LAS __attribute__((address_space(3)))
#define DI __device__ __forceinline__
#define MFMA(a, b, c) __builtin_amdgcn_mfma_f32_32x32x16_bf16((a), (b), (c), 0, 0, 0)
#define SBAR() __builtin_amdgcn_sched_barrier(0)

constexpr int SEQ = 2048, DM = 448, NH = 16, DH = 28, FF = 1792;
constexpr unsigned FL_OFF = 0;
#define SETB 1u
constexpr unsigned SET_BASE = SETB << 20, SET_STRIDE = 24u << 20;
#define GQS 16u
constexpr unsigned GQW = GQS == 16u ? 128u : 4u * GQS;
constexpr unsigned GQ_OFF = 0;
constexpr unsigned GH_OFF = GQ_OFF + 256 * GQW;
constexpr unsigned GO_OFF = GH_OFF + 256 * 128;
constexpr unsigned GW_OFF = GO_OFF + 1024 * 128;
constexpr unsigned GG_OFF = GW_OFF + 256 * 8;
constexpr unsigned QF_OFF = 524288;
constexpr unsigned KV_OFF = QF_OFF + 16 * 64 * 2048;
constexpr unsigned HF_OFF = KV_OFF + 16 * 64 * 4096;
constexpr unsigned WOF_OFF = HF_OFF + 64 * 56 * 2048;
constexpr unsigned OF_OFF = WOF_OFF + 14 * 16 * 2048;
constexpr unsigned WS_END = OF_OFF + 64 * 16 * 2048;
static_assert(GG_OFF + 2048 * 8 <= QF_OFF && WS_END <= SET_STRIDE, "workspace map");

constexpr int L_X = 0;
constexpr int L_WQ = 114688;
constexpr int L_W1 = 114688;
constexpr int L_CTRL = 163776;
constexpr int L_W2 = 0;
constexpr int L_CMB = 98304;
constexpr int LDS_BYTES = 163840;

constexpr float QSCALE = 0.18898223650461362f * 1.4426950408889634f;
constexpr float LOG2E = 1.4426950408889634f;
constexpr float SAFE_BOUND = 60.0f;
constexpr unsigned SPIN_CAP = 1u << 18;
#define ROT1(rb, j) ((((rb) + (j)) & 1) * 3)
#define KVD 4
#define ROTA 0
#define ROT2 0
#define PDELAY 16
#define ODELAY 10
#define CODE_PF 44032
#define MPF 1
#define TRIG 3
#define WDELAY 100
#define FTHR 8
#define DEARLY W2S
#define F1PRIO 3
#define POLL_A 4

struct Params {
  const unsigned short* x;
  const unsigned short* wqkv;
  const unsigned short* wo;
  const unsigned short* w1;
  const unsigned short* w2;
  const unsigned short* gw;
  unsigned short* out;
  unsigned char* ws;
};
typedef unsigned long long u64;
typedef __attribute__((address_space(1))) u64 gu64;

DI unsigned cvtpk(float lo, float hi) {
  typedef __bf16 bf2 __attribute__((ext_vector_type(2)));
  typedef float f2 __attribute__((ext_vector_type(2)));
  f2 v = {lo, hi};
  bf2 r = __builtin_convertvector(v, bf2);
  return __builtin_bit_cast(unsigned, r);
}
DI bf16x8 pack8(const f32x16& x, int s) {
  u32x4 p;
  p[0] = cvtpk(x[8 * s + 0], x[8 * s + 1]);
  p[1] = cvtpk(x[8 * s + 2], x[8 * s + 3]);
  p[2] = cvtpk(x[8 * s + 4], x[8 * s + 5]);
  p[3] = cvtpk(x[8 * s + 6], x[8 * s + 7]);
  return __builtin_bit_cast(bf16x8, p);
}
DI float swap_half(float v) {
  auto r = __builtin_amdgcn_permlane32_swap(__float_as_uint(v), __float_as_uint(v), false, false);
  return (threadIdx.x & 32) ? __uint_as_float(r[0]) : __uint_as_float(r[1]);
}
template <int CTRL> DI float dpp_mov(float v) {
  return __uint_as_float(__builtin_amdgcn_update_dpp(__float_as_uint(v), __float_as_uint(v), CTRL, 0xf, 0xf, false));
}
DI float row16_max(float v) {
  v = fmaxf(v, dpp_mov<0x128>(v));
  v = fmaxf(v, dpp_mov<0x124>(v));
  v = fmaxf(v, dpp_mov<0x122>(v));
  v = fmaxf(v, dpp_mov<0x121>(v));
  return v;
}
DI unsigned row16_umax(unsigned v) {
  v = max(v, (unsigned)__builtin_amdgcn_update_dpp(v, v, 0x128, 0xf, 0xf, false));
  v = max(v, (unsigned)__builtin_amdgcn_update_dpp(v, v, 0x124, 0xf, 0xf, false));
  v = max(v, (unsigned)__builtin_amdgcn_update_dpp(v, v, 0x122, 0xf, 0xf, false));
  v = max(v, (unsigned)__builtin_amdgcn_update_dpp(v, v, 0x121, 0xf, 0xf, false));
  return v;
}
DI float rdlane(float v, int l) { return __uint_as_float(__builtin_amdgcn_readlane(__float_as_uint(v), l)); }
DI s16x4 tr_read(const LAS unsigned char* p) {
  typedef short v4i16_t __attribute__((ext_vector_type(4)));
  return __builtin_bit_cast(s16x4, __builtin_amdgcn_ds_read_tr16_b64_v4i16((LAS v4i16_t*)p));
}
DI bf16x8 tr_frag(const LAS unsigned char* p, int row_stride4) {
  s16x4 lo = tr_read(p), hi = tr_read(p + row_stride4);
  return __builtin_shufflevector(lo, hi, 0, 1, 2, 3, 4, 5, 6, 7);
}
DI __amdgpu_buffer_rsrc_t mk_rsrc(const void* p, unsigned bytes) {
  return __builtin_amdgcn_make_buffer_rsrc((void*)p, (short)0, (int)bytes, 0x00020000);
}
DI void glds16(__amdgpu_buffer_rsrc_t rs, unsigned dst, unsigned voff, unsigned soff) {
  asm volatile("s_mov_b32 m0, %0\n\ts_nop 0\n\tbuffer_load_dwordx4 %1, %2, %3 offen lds" ::"s"(dst), "v"(voff), "s"(rs), "s"(soff) : "memory");
}
DI u32x4 ld16(__amdgpu_buffer_rsrc_t rs, unsigned voff, unsigned soff) { return __builtin_amdgcn_raw_buffer_load_b128(rs, voff, soff, 0); }
DI u32x4 ld16c(__amdgpu_buffer_rsrc_t rs, unsigned voff, unsigned soff) { return __builtin_amdgcn_raw_buffer_load_b128(rs, voff, soff, 16); }
DI void st16(__amdgpu_buffer_rsrc_t rs, u32x4 v, unsigned voff, unsigned soff) { __builtin_amdgcn_raw_buffer_store_b128(v, rs, voff + soff, 0, 16); }
DI bf16x8 as_frag(u32x4 v) { return __builtin_bit_cast(bf16x8, v); }
DI u32x4 as_u4(bf16x8 v) { return __builtin_bit_cast(u32x4, v); }
template <int N> DI void wait_bar() { asm volatile("s_waitcnt vmcnt(%0) lgkmcnt(0)\n\ts_barrier" ::"n"(N) : "memory"); }

template <int V> struct IC { static constexpr int value = V; };
template <int I, int N, class Fn> DI void static_for(Fn&& fn) {
  if constexpr (I < N) { fn(IC<I>{}); static_for<I + 1, N>(fn); }
}
DI void drain() { asm volatile("s_waitcnt vmcnt(0)" ::: "memory"); }
template <int N> DI void wait_vm() { asm volatile("s_waitcnt vmcnt(%0)" ::"n"(N) : "memory"); }
DI void put_granule(unsigned char* ws, unsigned off, u64 tag16, unsigned payload) {
  __hip_atomic_store((gu64*)(ws + off), tag16 | (u64)(payload & 0xffffu), __ATOMIC_RELAXED, __HIP_MEMORY_SCOPE_AGENT);
}
template <int SLEEP> DI unsigned poll_granule(unsigned char* ws, unsigned off, u64 tag16) {
  u64 v = ~tag16;
  for (unsigned spins = 0;; ++spins) {
    if (v - tag16 > 0xffffull) v = __hip_atomic_load((gu64*)(ws + off), __ATOMIC_RELAXED, __HIP_MEMORY_SCOPE_AGENT);
    if (__all(v - tag16 <= 0xffffull) || spins > SPIN_CAP) break;
    __builtin_amdgcn_s_sleep(SLEEP);
  }
  __builtin_amdgcn_fence(__ATOMIC_ACQUIRE, "wavefront");
  return (unsigned)v & 0xffffu;
}
template <int SLEEP = 1> DI unsigned wait_lds_word(LAS unsigned char* p, unsigned want) {
  unsigned v = 0;
#pragma nounroll
  for (unsigned spins = 0; spins < (1u << 24); ++spins) {
    v = __builtin_amdgcn_readfirstlane(__hip_atomic_load((LAS unsigned*)p, __ATOMIC_RELAXED, __HIP_MEMORY_SCOPE_WORKGROUP));
    if (v >= want) break;
    __builtin_amdgcn_s_sleep(SLEEP);
  }
  asm volatile("" ::: "memory");
  return v;
}
template <int N> DI u64 wait_bar_epoch(const unsigned char* p) {
  u64 ep;
  asm volatile("s_load_dwordx2 %0, %1, 0x0\n\ts_waitcnt vmcnt(%2) lgkmcnt(0)\n\ts_barrier" : "=&s"(ep) : "s"(p), "n"(N) : "memory");
  return ep;
}
DI void bar_lds() { asm volatile("s_waitcnt lgkmcnt(0)\n\ts_barrier" ::: "memory"); }
DI void st_tile(LAS unsigned char* p, const f32x16& a) {
#pragma unroll
  for (int g = 0; g < 4; ++g) *(LAS f32x4*)(p + g * 1024) = f32x4{a[4 * g], a[4 * g + 1], a[4 * g + 2], a[4 * g + 3]};
}
DI void add_tile(f32x16& a, const LAS unsigned char* p) {
#pragma unroll
  for (int g = 0; g < 4; ++g) {
    const f32x4 v = *(const LAS f32x4*)(p + g * 1024);
    a[4 * g] += v[0]; a[4 * g + 1] += v[1]; a[4 * g + 2] += v[2]; a[4 * g + 3] += v[3];
  }
}

#define STAMP_DECL do {} while (0)
#define STAMP(id) do {} while (0)
#define QSTAMP(id) do {} while (0)
#define RSTAMP(id) do {} while (0)
#define STAMP_FLUSH(P, ph) do {} while (0)

DI void lds_arrive(LAS unsigned char* p) {
  asm volatile("s_mov_b64 exec, 1\n\tds_add_u32 %0, %1\n\ts_mov_b64 exec, -1" ::"v"((unsigned)(uintptr_t)p), "v"(1u) : "memory");
}

#define PINV(x) asm volatile("" : "+v"(x))
#define PINS(x) asm volatile("" : "+s"(x))
DI float sumsq(const u32x4& a, const u32x4& b) {
  typedef __bf16 bf2 __attribute__((ext_vector_type(2)));
  float n0 = 0.f, n1 = 0.f;
#pragma unroll
  for (int i = 0; i < 4; ++i) {
    n0 = __builtin_amdgcn_fdot2_f32_bf16(__builtin_bit_cast(bf2, a[i]), __builtin_bit_cast(bf2, a[i]), n0, false);
    n1 = __builtin_amdgcn_fdot2_f32_bf16(__builtin_bit_cast(bf2, b[i]), __builtin_bit_cast(bf2, b[i]), n1, false);
  }
  return n0 + n1;
}

DI void role_a(const Params& P, LAS unsigned char* lds, const int wt, const unsigned code_word) {
  const int lane = threadIdx.x & 63, r = lane & 31, h = lane >> 5;
  const int b = blockIdx.x, xcd = b & 7, idx = b >> 3;
  const unsigned lo = lane * 16;
  STAMP_DECL;
  STAMP(0);
  __builtin_amdgcn_s_setprio(2);
  unsigned xtouch = 0;
  if (wt < 2) xtouch = __builtin_amdgcn_raw_buffer_load_b32(wt ? mk_rsrc(P.wqkv, DM * 3 * DM * 2) : mk_rsrc(P.x, SEQ * DM * 2),
                                                            (unsigned)(wt ? b * 37 + (lane < 37 ? lane : 0) : b * 56 + (lane < 56 ? lane : 0)) * 128, 0, 0);
  SBAR();
  f32x16 acc[4];
  const int rb = (xcd & 1) * 8 + (idx & 7), j = (xcd >> 1) * 4 + (idx >> 3);
  const int head = xcd * 2 + (idx & 1), qb = idx >> 1;
  unsigned sto[3];
#pragma unroll
  for (int ct = 0; ct < 3; ++ct) {
    const int u = 3 * j + ct, part = u >> 4, t = (u & 15) * 64 + rb * 4 + wt;
    sto[ct] = lo + (part == 0 ? QF_OFF + t * 2048 : KV_OFF + t * 4096 + (part - 1) * 2048);
    PINV(sto[ct]);
  }
  unsigned padv = r == DH ? 0x3f803f80u : 0u;
  PINV(padv);
  const int gq = head / 3, gk = (16 + head) / 3, gvv = (32 + head) / 3;
  unsigned pub_off = GQ_OFF + (j * 16 + rb) * GQW + wt * GQS + (lane & 1) * 8;
  unsigned poll_off = GQ_OFF + (h ? gq * 16 + qb : (lane < 16 ? gk : gvv) * 16 + wt * 4 + ((lane >> 2) & 3)) * GQW + (lane & 3) * GQS + h * 8;
  unsigned chk_off = GQ_OFF + (gk * 16 + (lane >> 2)) * GQW + (lane & 3) * GQS;
  unsigned g_off = GG_OFF + ((qb * 4 + wt) * 32 + r) * 8;
  unsigned o_off = lo + OF_OFF + ((qb * 4 + wt) * 16 + head) * 2048;
  PINV(pub_off); PINV(poll_off); PINV(chk_off); PINV(g_off); PINV(o_off);
  unsigned kvbase = KV_OFF + (head * 64 + wt * 16) * 4096, go_off = GO_OFF + ((qb * 4 + wt) * 16 + head) * 128;
  unsigned qofs[4];
#pragma unroll
  for (int t = 0; t < 4; ++t) { qofs[t] = QF_OFF + (unsigned)(head * 64 + qb * 4 + ((wt + 1 + t) & 3)) * 2048; PINS(qofs[t]); }
  PINS(kvbase); PINS(go_off);
  unsigned xoff[4];
#pragma unroll
  for (int ks = 0; ks < 4; ++ks) xoff[ks] = (unsigned)(L_X + wt * 4096 + r * 128 + (((2 * ks + h) ^ ((r >> 1) & 7)) << 4));
  const unsigned wqoff = (unsigned)(L_WQ + (8 * h + ((lane & 15) >> 2)) * 192 + ((lane >> 4) & 1) * 32 + (lane & 3) * 8 + (j & 1) * 8);
  u64 ep;
  asm volatile("s_load_dwordx2 %0, %1, 0x0\n\ts_waitcnt lgkmcnt(0)" : "=&s"(ep) : "s"(P.ws + FL_OFF) : "memory");
  u64 tag16 = (ep + 1) << 16;
  unsigned char* wsb = P.ws + SET_BASE + (((unsigned)ep + 1u) & 3u) * SET_STRIDE;
  PINS(tag16); PINS(wsb);
  const __amdgpu_buffer_rsrc_t rsWs = mk_rsrc(wsb, WS_END);
#pragma unroll
  for (int i = 0; i < 3; ++i) acc[i] = f32x16{};
  unsigned wtouch = 0;
  auto kloop = [&](auto nvc) {
    constexpr int NV = decltype(nvc)::value;
    bf16x8 xf[4], wf[4][3];
    auto mm = [&](int ks, int ct) { acc[ct] = ct >= 3 - NV ? MFMA(xf[ks], wf[ks][ct], acc[ct]) : MFMA(wf[ks][ct], xf[ks], acc[ct]); };
    auto step = [&](int c, int ks, bool domm) {
      const int km = (ks + 2) & 3;
#pragma unroll
      for (int ct = 0; ct < 3; ++ct) {
        if (domm) mm(km, ct);
        SBAR();
        if (ct == 0) xf[ks] = *(const LAS bf16x8*)(lds + xoff[ks] + c * 16384);
        wf[ks][ct] = tr_frag(lds + wqoff + (c & 3) * 12288 + ks * 16 * 192 + ct * 56, 4 * 192);
        SBAR();
      }
    };
#pragma unroll
    for (int c = 0; c < 7; ++c) {
      bar_lds();
      if (c == 0) STAMP(1);
      if (c == MPF) {
        const unsigned l = (unsigned)(b * 128 + (wt & 1) * 64 + lane) * 128;
        if (wt < 2) wtouch = l < DM * FF * 2 ? __builtin_amdgcn_raw_buffer_load_b32(mk_rsrc(P.w1, DM * FF * 2), l, 0, 0)
                           : l < 2 * DM * FF * 2 ? __builtin_amdgcn_raw_buffer_load_b32(mk_rsrc(P.w2, DM * FF * 2), l - DM * FF * 2, 0, 0)
                                                 : __builtin_amdgcn_raw_buffer_load_b32(mk_rsrc(P.wo, DM * DM * 2), l - 2 * DM * FF * 2, 0, 0);
        SBAR();
      }
      QSTAMP(1 + c);
      step(c, 0, c > 0);
      step(c, 1, c > 0);
      step(c, 2, true);
      step(c, 3, true);
    }
    bar_lds();
    QSTAMP(8);
#pragma unroll
    for (int ct = 0; ct < 3; ++ct) { mm(2, ct); mm(3, ct); SBAR(); }
  };
  auto epi = [&](auto nqc, auto nvc) {
    constexpr int NQ = decltype(nqc)::value, NV = decltype(nvc)::value;
    STAMP(2);
    asm volatile("" ::"v"(code_word), "v"(wtouch), "v"(xtouch));
    u32x4 t0[3], t1[3];
#pragma unroll
    for (int ct = 0; ct < 3; ++ct) {
      if (ct < NQ) acc[ct] *= QSCALE;
      t0[ct] = as_u4(pack8(acc[ct], 0));
      if (ct >= 3 - NV) {
#pragma unroll
        for (int i = 0; i < 4; ++i) t0[ct][i] = r < DH ? t0[ct][i] : padv;
      }
      st16(rsWs, t0[ct], sto[ct], 0);
      t1[ct] = as_u4(pack8(acc[ct], 1));
      if (ct >= 3 - NV) {
#pragma unroll
        for (int i = 0; i < 4; ++i) t1[ct][i] = r < DH ? t1[ct][i] : padv;
      } else {
        t1[ct][2] = h ? (ct < NQ ? 0u : 0x3f803f80u) : t1[ct][2];
        t1[ct][3] = h ? (ct < NQ ? 0u : 0x00003f80u) : t1[ct][3];
      }
      st16(rsWs, t1[ct], sto[ct], 1024);
      SBAR();
    }
    STAMP(8);
    unsigned mq = 0, mk = 0;
#pragma unroll
    for (int ct = 0; ct < 3; ++ct) {
      const unsigned n = __float_as_uint(sumsq(t0[ct], t1[ct])) & 0x7fffffffu;
      if (ct < NQ) mq = max(mq, n); else mk = max(mk, n);
    }
    auto bound = [&](unsigned m) {
      m = row16_umax(m);
      const float lo2 = __uint_as_float(max((unsigned)__builtin_amdgcn_readlane(m, 0), (unsigned)__builtin_amdgcn_readlane(m, 16)));
      const float hi2 = __uint_as_float(max((unsigned)__builtin_amdgcn_readlane(m, 32), (unsigned)__builtin_amdgcn_readlane(m, 48)));
      return ((__float_as_uint(lo2 + hi2) >> 16) + 1u) & 0xffffu;
    };
    const unsigned pq = NQ > 0 ? bound(mq) : 0u, pk = NQ < 3 ? bound(mk) : 0u;
    u64 gran = tag16 | ((lane & 1) ? pq : pk);
    PINV(gran);
    drain();
    STAMP(9);
    if (lane < 2) __hip_atomic_store((gu64*)(wsb + pub_off), gran, __ATOMIC_RELAXED, __HIP_MEMORY_SCOPE_AGENT);
  };
  if (j < 10) {
    kloop(IC<0>{});
    if (j < 5) epi(IC<3>{}, IC<0>{});
    else if (j == 5) epi(IC<1>{}, IC<0>{});
    else epi(IC<0>{}, IC<0>{});
  } else if (j == 10) {
    kloop(IC<1>{});
    epi(IC<0>{}, IC<1>{});
  } else {
    kloop(IC<3>{});
    epi(IC<0>{}, IC<3>{});
  }
  STAMP(3);
  __builtin_amdgcn_s_sleep(PDELAY);
  const unsigned pay = poll_granule<POLL_A>(wsb, poll_off, tag16);
  STAMP(4);
  u32x4 q[4][2];
  LAS unsigned char* const cmb = lds + L_CMB + wt * 12288 + lo;
  u64 gv = 0;
  unsigned thr = 0;
#pragma nounroll
  for (int pass = 0;; ++pass) {
    u32x4 kv[KVD][4];
    kv[0][0] = ld16c(rsWs, lo, kvbase);
    SBAR();
    if (pass == 0) { q[0][0] = ld16c(rsWs, lo, qofs[0]); q[0][1] = ld16c(rsWs, lo + 1024, qofs[0]); }
    SBAR();
    kv[0][1] = ld16c(rsWs, lo + 1024, kvbase);
    SBAR();
    if (pass == 0) {
#pragma unroll
      for (int t = 1; t < 4; ++t) { q[t][0] = ld16c(rsWs, lo, qofs[t]); q[t][1] = ld16c(rsWs, lo + 1024, qofs[t]); SBAR(); }
    }
#pragma unroll
    for (int e = 2; e < 4 * (KVD - 1); ++e) { kv[e >> 2][e & 3] = ld16c(rsWs, lo + (e & 3) * 1024, kvbase + (e >> 2) * 4096); SBAR(); }
#pragma unroll
    for (int i = 0; i < 4; ++i) acc[i] = f32x16{};
    {
      const float q2 = __uint_as_float(__builtin_amdgcn_readlane(row16_umax(pay), 32) << 16);
      const float lim = SAFE_BOUND * SAFE_BOUND * __builtin_amdgcn_rcpf(q2);
      thr = lim < 1e30f ? __float_as_uint(lim) >> 16 : (lim >= 1e30f ? 0x7000u : 0u);
      PINV(thr);
    }
    __builtin_amdgcn_s_setprio(1);
    f32x16 sc = MFMA(as_frag(kv[0][0]), as_frag(q[0][0]), f32x16{});
    sc = MFMA(as_frag(kv[0][1]), as_frag(q[0][1]), sc);
    STAMP(5);
    u32x4 p0, p1 = {0, 0, 0, 0};
    float e0 = 0.f, e1 = 0.f, e2 = 0.f, e3 = 0.f;
    u64 ck = 0, cv = 0;
#define EXP4(i) do { e0 = __builtin_amdgcn_exp2f(sc[i]); e1 = __builtin_amdgcn_exp2f(sc[(i) + 1]); e2 = __builtin_amdgcn_exp2f(sc[(i) + 2]); e3 = __builtin_amdgcn_exp2f(sc[(i) + 3]); \
    asm volatile("" : "+v"(e0), "+v"(e1), "+v"(e2), "+v"(e3)); SBAR(); } while (0)
#define CVT2(P, i) do { P[i] = cvtpk(e0, e1); P[(i) + 1] = cvtpk(e2, e3); asm volatile("" : "+v"(P)); SBAR(); } while (0)
#define PART(t, g) do { *(LAS f32x4*)(cmb + (t) * 4096 + (g) * 1024) = f32x4{acc[t][4 * (g)], acc[t][4 * (g) + 1], acc[t][4 * (g) + 2], acc[t][4 * (g) + 3]}; SBAR(); } while (0)
    static_for<0, 64>([&](auto ic) {
      constexpr int i = decltype(ic)::value;
      constexpr int kt = i >> 2, t = i & 3, kn_ = (i + 1) >> 2, tn = (i + 1) & 3, kp = (i - 1) >> 2, tp = (i - 1) & 3;
      constexpr int kl = kt + KVD - 1;
      if (i == 24) STAMP(11);
      if (i == 56) (void)wait_lds_word(lds + L_CTRL, 4);
      f32x16 sn;
      SBAR();
      if (i < 63) sn = MFMA(as_frag(kv[kn_ % KVD][0]), as_frag(q[tn][0]), f32x16{});
      SBAR();
      if constexpr (i >= 62) PART(i - 62, 0);
      if (i > 0) CVT2(p1, 2);
      EXP4(0);
      if (i > 0) acc[tp] = MFMA(as_frag(kv[(kp + KVD) % KVD][3]), as_frag(p1), acc[tp]);
      SBAR();
      if constexpr (t == 0 && kl < 16) {
#pragma unroll
        for (int e = 0; e < 4; ++e) kv[kl % KVD][e] = ld16c(rsWs, lo + e * 1024, kvbase + kl * 4096);
      }
      if constexpr (t == 2 && kt + 1 < 16) {
        constexpr int n = 4 * ((kl < 16 ? kl : 15) - (kt + 1)) + (i > 53 ? 3 : 0);
        __builtin_amdgcn_s_waitcnt(0x0f70 | (n & 15) | ((n >> 4) << 14));
      }
      if (i == 51) gv = __hip_atomic_load((gu64*)(wsb + g_off), __ATOMIC_RELAXED, __HIP_MEMORY_SCOPE_AGENT);
      if (i == 52) ck = __hip_atomic_load((gu64*)(wsb + chk_off), __ATOMIC_RELAXED, __HIP_MEMORY_SCOPE_AGENT);
      if (i == 53) cv = __hip_atomic_load((gu64*)(wsb + (gvv - gk) * 16 * GQW + chk_off), __ATOMIC_RELAXED, __HIP_MEMORY_SCOPE_AGENT);
      SBAR();
      if constexpr (i >= 62) PART(i - 62, 1);
      CVT2(p0, 0);
      EXP4(4);
      if (i < 63) sn = MFMA(as_frag(kv[kn_ % KVD][1]), as_frag(q[tn][1]), sn);
      SBAR();
      if constexpr (i >= 62) PART(i - 62, 2);
      CVT2(p0, 2);
      EXP4(8);
      acc[t] = MFMA(as_frag(kv[kt % KVD][2]), as_frag(p0), acc[t]);
      SBAR();
      if constexpr (i >= 62) PART(i - 62, 3);
      CVT2(p1, 0);
      EXP4(12);
      if (i < 63) sc = sn;
    });
    CVT2(p1, 2);
    acc[3] = MFMA(as_frag(kv[15 % KVD][3]), as_frag(p1), acc[3]);
    SBAR();
    PART(2, 0); PART(2, 1); PART(2, 2); PART(2, 3);
#undef EXP4
#undef CVT2
#undef PART
    __builtin_amdgcn_s_setprio(2);
    if (__builtin_expect(__all(ck - tag16 <= (u64)thr && cv - tag16 <= 0x7000ull && gv - tag16 <= 0xffffull), 1)) break;
    gv = poll_granule<2>(wsb, g_off, tag16);
    if (pass) break;
    const unsigned pk = poll_granule<2>(wsb, chk_off, tag16), pv = poll_granule<2>(wsb, (gvv - gk) * 16 * GQW + chk_off, tag16);
    if (__all(pk <= thr && pv <= 0x7000u)) break;
    float mx[4];
#pragma unroll
    for (int t = 0; t < 4; ++t) mx[t] = -INFINITY;
    for (int kt = 0; kt < 64; ++kt) {
      const u32x4 k0 = ld16c(rsWs, lo, KV_OFF + (head * 64 + kt) * 4096), k1 = ld16c(rsWs, lo + 1024, KV_OFF + (head * 64 + kt) * 4096);
#pragma unroll
      for (int t = 0; t < 4; ++t) {
        f32x16 s = MFMA(as_frag(k0), as_frag(q[t][0]), f32x16{});
        s = MFMA(as_frag(k1), as_frag(q[t][1]), s);
#pragma unroll
        for (int i = 0; i < 16; ++i) mx[t] = fmaxf(mx[t], s[i]);
      }
    }
#pragma unroll
    for (int t = 0; t < 4; ++t) {
      const float nm = -fmaxf(mx[t], swap_half(mx[t]));
      const unsigned hi = __float_as_uint(nm) & 0xffff0000u;
      const float r1 = nm - __uint_as_float(hi);
      const unsigned mid = __float_as_uint(r1) & 0xffff0000u;
      const float r2 = r1 - __uint_as_float(mid);
      if (h) { q[t][1][2] = (hi >> 16) | mid; q[t][1][3] = __float_as_uint(r2) >> 16; }
    }
  }
  STAMP(6);
  asm volatile("s_waitcnt lgkmcnt(0)" ::: "memory");
  lds_arrive(lds + L_CTRL + 20);
  (void)wait_lds_word(lds + L_CTRL + 20, 4);
  f32x16 o = acc[3];
#pragma unroll
  for (int u = 0; u < 3; ++u) add_tile(o, lds + L_CMB + ((wt + 3 - u) & 3) * 12288 + u * 4096 + lo);
  const auto lsw = __builtin_amdgcn_permlane32_swap(__float_as_uint(o[12]), __float_as_uint(o[12]), false, false);
  const float inv = __uint_as_float((unsigned)gv << 16) * __builtin_amdgcn_rcpf(__uint_as_float(lsw[1]));
  o *= inv;
  st16(rsWs, as_u4(pack8(o, 0)), o_off, 0);
  u32x4 o1 = as_u4(pack8(o, 1));
  o1[2] = h ? 0u : o1[2];
  o1[3] = h ? 0u : o1[3];
  st16(rsWs, o1, o_off, 1024);
  STAMP(7);
  drain();
  if (lane == 0) __hip_atomic_store((gu64*)(wsb + go_off), tag16, __ATOMIC_RELAXED, __HIP_MEMORY_SCOPE_AGENT);
  STAMP(10);
  STAMP_FLUSH(P, 0);
}

#define W2S 6
#define HPRE 6
#define NPRE 14
#define LEA 5
#define LEB 11
#define LE0 16
#define LU0 3
#define F1LA 2
#define PACE 2
#define PACE2 2
constexpr int ffn2_tail(int f) { return f + HPRE < 18 ? 4 : 0; }
constexpr int ffn2_ops(int f) { return (f + W2S < 14 ? 4 : 0) + ffn2_tail(f); }
constexpr int ffn2_younger(int f, int upto) {
  int n = ffn2_tail(f - W2S);
  for (int i = f - W2S + 1; i < upto; ++i) n += ffn2_ops(i);
  return n;
}

DI void role_f(const Params& P, LAS unsigned char* lds, const int wt) {
  u64 ep;
  const int lane = threadIdx.x & 63, r = lane & 31, h = lane >> 5;
  const int b = blockIdx.x, xcd = b & 7, idx = b >> 3;
  const int rb = (xcd & 1) * 8 + (idx & 7), j = (xcd >> 1) * 4 + (idx >> 3);
  const unsigned lo = lane * 16, lds0 = (unsigned)(uintptr_t)lds;
  const bool ffn = j < 14;
  STAMP_DECL;
  STAMP(0);
  f32x16 acc[4];
  u32x4 wl[28];
  const __amdgpu_buffer_rsrc_t rsL = mk_rsrc(ffn ? P.w1 : P.gw, ffn ? DM * FF * 2 : DM * 2);
  const unsigned lsrc = ffn ? (unsigned)((lane >> 2) * (FF * 2) + (j * 4 + wt) * 64 + (lane & 3) * 16) : (unsigned)(h * 16);
  const unsigned lstep = ffn ? 16 * FF * 2 : 32;
  const int rot = ROT1(rb, j);
  auto kc = [&](int c) { const int v = c + rot; return v >= 7 ? v - 7 : v; };
  auto L = [&](int u0, int u1) {
#pragma unroll
    for (int u = u0; u < u1; ++u) wl[u] = ld16(rsL, lsrc, (kc(u >> 2) * 4 + (u & 3)) * lstep);
    SBAR();
  };
  {
    const __amdgpu_buffer_rsrc_t rsX = mk_rsrc(P.x, SEQ * DM * 2);
    const __amdgpu_buffer_rsrc_t rsW = mk_rsrc(P.wqkv, DM * 3 * DM * 2);
    unsigned xsrc[2], wsrc[3];
#pragma unroll
    for (int i = 0; i < 2; ++i) {
      const int row = 32 * wt + 8 * i + (lane >> 3), ch = (lane & 7) ^ ((row >> 1) & 7);
      xsrc[i] = (unsigned)((rb * 128 + row) * (DM * 2) + ch * 16);
    }
#pragma unroll
    for (int i = 0; i < 3; ++i) {
      const int p = 64 * (3 * wt + i) + lane, kr = p / 12, ch = p % 12;
      wsrc[i] = (unsigned)(kr * (3 * DM * 2) + j * 168 - (j & 1) * 8 + (ch < 11 ? ch : 10) * 16);
    }
    auto X = [&](int c) {
#pragma unroll
      for (int i = 0; i < 4; ++i) glds16(rsX, lds0 + L_X + c * 16384 + (4 * wt + i) * 1024, xsrc[i & 1], kc(c) * 128 + (i >> 1) * 16 * DM * 2);
    };
    auto W = [&](int c) {
#pragma unroll
      for (int i = 0; i < 3; ++i) glds16(rsW, lds0 + L_WQ + (c & 3) * 12288 + (3 * wt + i) * 1024, wsrc[i], kc(c) * 64 * 3 * DM * 2);
    };
    X(0); W(0); X(1); W(1); X(2);
    QSTAMP(1);
    wait_bar<11>();
    STAMP(1);
    W(2); X(3); W(3);
    QSTAMP(2);
    wait_bar<14>();
    X(4); W(4);
    QSTAMP(3);
    wait_bar<14>();
    X(5); W(5);
    QSTAMP(4);
    wait_bar<14>();
    X(6); W(6);
    QSTAMP(5);
    wait_bar<14>();
    if (wt == 0 && lane < 8) *(LAS unsigned*)(lds + L_CTRL + lane * 4) = 0u;
    L(0, LEA);
    QSTAMP(6);
    ep = wait_bar_epoch<7 + LEA>(P.ws + FL_OFF);
    L(LEA, LEB);
    QSTAMP(7);
    wait_bar<LEB>();
    L(LEB, LE0);
    QSTAMP(8);
    bar_lds();
    __builtin_amdgcn_s_sleep(FDELAY);
    __builtin_amdgcn_s_setprio(F1PRIO);
  }
  STAMP(2);
  const u64 tag = (ep + 1) << 16;
  unsigned char* const wsb = P.ws + SET_BASE + (((unsigned)ep + 1u) & 3u) * SET_STRIDE;
  const __amdgpu_buffer_rsrc_t rsWs = mk_rsrc(wsb, WS_END);
  unsigned xoff[4];
#pragma unroll
  for (int ks = 0; ks < 4; ++ks) xoff[ks] = (unsigned)(L_X + r * 128 + (((2 * ks + h) ^ ((r >> 1) & 7)) << 4));
  const int mb = 4 * xcd + (idx & 3), nb = idx >> 2;
  const __amdgpu_buffer_rsrc_t rsW2 = mk_rsrc(P.w2, FF * DM * 2), rsH = rsWs;
  const unsigned w2src = (unsigned)((wt * 448 + (lane >> 3)) * (DM * 2) + nb * 128 + ((((lane >> 2) & 1) ^ ((lane >> 4) & 1)) * 64) + (lane & 3) * 16);
  const unsigned ring0 = lds0 + L_W2 + wt * (W2S * 4096);
  const int rot2 = ROT2;
  auto fr = [&](int f) { const int v = f + rot2; return v >= 14 ? v - 14 : v; };
  auto D = [&](int f) {
#pragma unroll
    for (int i = 0; i < 4; ++i) glds16(rsW2, ring0 + (f % W2S) * 4096 + i * 1024, w2src, (fr(f) * 32 + i * 8) * DM * 2);
  };
  const bool has_tile = idx < 28;
  auto fill_ring = [&]() {
    if (has_tile) {
      (void)wait_lds_word(lds + L_CTRL, 4);
#pragma unroll
      for (int f = 0; f < DEARLY; ++f) { D(f); wait_vm<FTHR>(); __builtin_amdgcn_s_sleep(PACE); }
    }
  };
  if (ffn) {
    LAS unsigned char* ring = lds + L_W1 + wt * 8192;
    const unsigned troff = (unsigned)((8 * h + ((lane & 15) >> 2)) * 64 + ((lane >> 4) & 1) * 32 + (lane & 3) * 8);
#pragma unroll
    for (int i = 0; i < 4; ++i) acc[i] = f32x16{};
    RSTAMP(1);
    constexpr int LA = F1LA;
    bf16x8 xf[LA + 1][4], wf[LA + 1];
    auto wr = [&](int u) { *(LAS u32x4*)(ring + (u & 7) * 1024 + lo) = wl[u]; };
    auto rdw = [&](int u) { wf[u % (LA + 1)] = tr_frag(ring + (u & 7) * 1024 + troff, 4 * 64); };
    auto rdx = [&](int u, int t) { xf[u % (LA + 1)][t] = *(const LAS bf16x8*)(lds + xoff[u & 3] + (u >> 2) * 16384 + t * 4096); };
#pragma unroll
    for (int u = 0; u < LA + 2; ++u) wr(u);
#pragma unroll
    for (int u = 0; u < LA; ++u) {
      rdw(u);
#pragma unroll
      for (int t = 0; t < 4; ++t) rdx(u, t);
    }
    SBAR();
#pragma unroll
    for (int u = 0; u < 28; ++u) {
      if ((u & 3) == 0) RSTAMP(2 + (u >> 2));
#pragma unroll
      for (int t = 0; t < 4; ++t) {
        if (u + LA < 28) {
          if (t == 1) rdw(u + LA);
          rdx(u + LA, t);
          if (t == 3 && u + LA + 2 < 28) wr(u + LA + 2);
        }
        SBAR();
        acc[t] = MFMA(wf[u % (LA + 1)], xf[u % (LA + 1)][t], acc[t]);
        SBAR();
        if (u >= LU0 && LE0 + (u - LU0) * 4 + t < 28) L(LE0 + (u - LU0) * 4 + t, LE0 + (u - LU0) * 4 + t + 1);
      }
    }
    lds_arrive(lds + L_CTRL);
    RSTAMP(9);
    STAMP(3);
    __builtin_amdgcn_s_setprio(3);
#pragma unroll
    for (int t = 0; t < 4; ++t) {
      f32x16 z = acc[t];
#pragma unroll
      for (int i = 0; i < 16; ++i) z[i] = z[i] * __builtin_amdgcn_rcpf(1.f + __builtin_amdgcn_exp2f(-LOG2E * z[i]));
      const unsigned so = HF_OFF + ((rb * 4 + t) * 56 + j * 4 + wt) * 2048;
      st16(rsWs, as_u4(pack8(z, 0)), lo, so);
      st16(rsWs, as_u4(pack8(z, 1)), lo + 1024, so);
    }
    STAMP(11);
    drain();
    if (lane == 0) put_granule(wsb, GH_OFF + (rb * 16 + j) * 128 + wt * 8, tag, 0);
  } else {
    __builtin_amdgcn_s_setprio(3);
    if (j == 14) {
      L(LE0, 28);
      acc[0] = f32x16{};
#pragma unroll
      for (int u = 0; u < 28; ++u) {
        const bf16x8 xf = *(const LAS bf16x8*)(lds + xoff[u & 3] + (u >> 2) * 16384 + wt * 4096);
        acc[0] = MFMA(as_frag(wl[u]), xf, acc[0]);
      }
      const float g = __builtin_amdgcn_rcpf(1.f + __builtin_amdgcn_exp2f(-LOG2E * acc[0][0]));
      if (h == 0) put_granule(wsb, GG_OFF + ((rb * 4 + wt) * 32 + r) * 8, tag, cvtpk(g, 0.f));
    }
    lds_arrive(lds + L_CTRL);
    __builtin_amdgcn_s_sleep(WDELAY);
    const __amdgpu_buffer_rsrc_t rsO = mk_rsrc(P.wo, DM * DM * 2);
    const int id0 = ((j - 14) * 16 + rb) * 4 + wt;
    for (int id = id0; id < 224; id += 128) {
      const int nt = id >> 4, hd = id & 15;
      unsigned rp[16];
#pragma unroll
      for (int e = 0; e < 16; ++e)
        rp[e] = __builtin_amdgcn_raw_buffer_load_b16(rsO, (unsigned)(((4 * h + 8 * (e >> 2) + (e & 3)) * DM + r) * 2), (unsigned)((DH * hd * DM + 32 * nt) * 2), 0);
      u32x4 a, c2;
#pragma unroll
      for (int i = 0; i < 4; ++i) {
        a[i] = (rp[2 * i] & 0xffffu) | (rp[2 * i + 1] << 16);
        c2[i] = (rp[8 + 2 * i] & 0xffffu) | (rp[8 + 2 * i + 1] << 16);
      }
      c2[2] = h ? 0u : c2[2];
      c2[3] = h ? 0u : c2[3];
      st16(rsWs, a, lo, WOF_OFF + id * 2048);
      st16(rsWs, c2, lo + 1024, WOF_OFF + id * 2048);
      drain();
      if (lane == 0) put_granule(wsb, GW_OFF + id * 8, tag, 0);
    }
  }
  STAMP(4);
  if (idx >= 28) {
    if (b == 255 && wt == 0) {
      __builtin_amdgcn_s_sleep(127);
#pragma unroll
      for (int i = 0; i < 4; ++i) (void)poll_granule<64>(wsb, GQ_OFF + (i * 64 + lane) * GQW, tag);
      if (lane == 0) __hip_atomic_store((gu64*)(P.ws + FL_OFF), ep + 1, __ATOMIC_RELAXED, __HIP_MEMORY_SCOPE_AGENT);
    }
    STAMP(10);
    STAMP_FLUSH(P, 0);
    return;
  }
  fill_ring();
  const float gl = __uint_as_float(poll_granule<8>(wsb, GG_OFF + (mb * 64 + lane) * 8, tag) << 16), gs = swap_half(gl);
  const float og0 = 1.f - (h ? gs : gl), og1 = 1.f - (h ? gl : gs);
  __builtin_amdgcn_s_sleep(HDELAY);
  const int ftl = wt * 14 + ((lane & 15) < 14 ? (lane & 15) : 0);
  (void)poll_granule<8>(wsb, GH_OFF + ((mb >> 1) * 16 + (ftl >> 2)) * 128 + (ftl & 3) * 8, tag);
  STAMP(5);
  u32x4 hb[HPRE][4], wo[16];
  const unsigned hbase = HF_OFF + (mb * 2 * 56 + wt * 14) * 2048;
  auto H = [&](int f) {
#pragma unroll
    for (int e = 0; e < 4; ++e) hb[f % HPRE][e] = ld16c(rsH, lo + (e & 1) * 1024, hbase + ((e >> 1) * 56 + fr(f)) * 2048);
  };
#pragma unroll
  for (int f = 0; f < (HPRE > W2S ? HPRE : W2S); ++f) {
    if (f >= DEARLY && f < W2S) D(f);
    if (f < HPRE) H(f);
    SBAR(); wait_vm<FTHR>(); __builtin_amdgcn_s_sleep(PACE); SBAR();
  }
  if (DEARLY < W2S) drain();
  const int q4 = (lane & 15) >> 2;
  unsigned woff[2];
#pragma unroll
  for (int nt = 0; nt < 2; ++nt) woff[nt] = (unsigned)(L_W2 + wt * (W2S * 4096) + (4 * h + q4) * 128 + ((nt ^ (q4 >> 1)) * 64) + ((lane >> 4) & 1) * 32 + (lane & 3) * 8);
#pragma unroll
  for (int i = 0; i < 4; ++i) acc[i] = f32x16{};
  STAMP(6);
  __builtin_amdgcn_s_setprio(0);
  bf16x8 wf[2][2][2];
  auto RW = [&](int f) {
#pragma unroll
    for (int s = 0; s < 2; ++s)
#pragma unroll
      for (int nt = 0; nt < 2; ++nt) wf[f & 1][s][nt] = tr_frag(lds + woff[nt] + (f % W2S) * 4096 + s * 2048, 1024);
  };
  RW(0);
  static_for<0, 14>([&](auto fc) {
    constexpr int f = decltype(fc)::value;
    if constexpr (f == 14 - HPRE) {
      PINV(acc[0]); PINV(acc[1]); PINV(acc[2]); PINV(acc[3]);
      (void)poll_granule<8>(wsb, GW_OFF + ((nb * 2 + ((lane >> 2) & 1)) * 16 + wt * 4 + (lane & 3)) * 8, tag);
    }
    if constexpr (f == NPRE) {
      PINV(acc[0]); PINV(acc[1]); PINV(acc[2]); PINV(acc[3]);
      STAMP(11);
      (void)wait_lds_word<2>(lds + L_CTRL + 20, TRIG);
    }
    asm volatile("s_waitcnt lgkmcnt(0)" ::: "memory");
    SBAR();
    if constexpr (f + 1 < 14) {
      if constexpr (f + 1 >= W2S) wait_vm<ffn2_younger(f + 1, f)>();
      RW(f + 1);
    }
    SBAR();
#pragma unroll
    for (int u = 0; u < 8; ++u) {
      const int s = u >> 2, nt = (u >> 1) & 1, mt = u & 1;
      acc[nt * 2 + mt] = MFMA(wf[f & 1][s][nt], as_frag(hb[f % HPRE][mt * 2 + s]), acc[nt * 2 + mt]);
      SBAR();
      if (u < 4) {
        if constexpr (f + W2S < 14) glds16(rsW2, ring0 + (f % W2S) * 4096 + u * 1024, w2src, (fr(f + W2S) * 32 + u * 8) * DM * 2);
      } else {
        const int e = u == 4 ? 0 : u == 5 ? 2 : u == 6 ? 1 : 3;
        if constexpr (f + HPRE < 14) hb[f % HPRE][e] = ld16c(rsH, lo + (e & 1) * 1024, hbase + ((e >> 1) * 56 + fr(f + HPRE)) * 2048);
        else if constexpr (f + HPRE < 18) {
          const int x = (f + HPRE - 14) * 4 + u - 4;
          wo[x] = ld16c(rsWs, lo, WOF_OFF + ((nb * 2 + (x >> 3)) * 16 + wt * 4) * 2048 + (x & 7) * 1024);
        }
      }
      SBAR();
    }
    if constexpr (f < NPRE) { wait_vm<FTHR>(); __builtin_amdgcn_s_sleep(PACE2); }
    SBAR();
  });
  STAMP(7);
  __builtin_amdgcn_s_setprio(3);
#pragma unroll
  for (int i = 0; i < 16; ++i) { acc[0][i] *= og0; acc[1][i] *= og1; acc[2][i] *= og0; acc[3][i] *= og1; }
  __builtin_amdgcn_s_sleep(ODELAY);
  (void)poll_granule<POLL_A>(wsb, GO_OFF + ((mb * 2 + ((lane >> 2) & 1)) * 16 + wt * 4 + (lane & 3)) * 128, tag);
  STAMP(8);
  u32x4 of[16];
#pragma unroll
  for (int e = 0; e < 8; ++e)
#pragma unroll
    for (int mt = 0; mt < 2; ++mt) of[mt * 8 + e] = ld16c(rsWs, lo, OF_OFF + ((mb * 2 + mt) * 16 + wt * 4) * 2048 + e * 1024);
  SBAR();
#pragma unroll
  for (int e = 0; e < 8; ++e)
#pragma unroll
    for (int nt = 0; nt < 2; ++nt)
#pragma unroll
      for (int mt = 0; mt < 2; ++mt) acc[nt * 2 + mt] = MFMA(as_frag(wo[nt * 8 + e]), as_frag(of[mt * 8 + e]), acc[nt * 2 + mt]);
#pragma unroll
  for (int t = 0; t < 4; ++t) st_tile(lds + L_W2 + wt * (W2S * 4096) + t * 4096 + lo, acc[t]);
  STAMP(9);
  bar_lds();
  f32x16 a = f32x16{};
#pragma unroll
  for (int f = 0; f < 4; ++f) add_tile(a, lds + L_W2 + f * (W2S * 4096) + wt * 4096 + lo);
  unsigned short* dst = P.out + (size_t)((mb * 2 + (wt & 1)) * 32 + r) * DM + (nb * 2 + (wt >> 1)) * 32 + 8 * h;
#pragma unroll
  for (int k = 0; k < 2; ++k) {
    const auto x = __builtin_amdgcn_permlane32_swap(cvtpk(a[8 * k], a[8 * k + 1]), cvtpk(a[8 * k + 4], a[8 * k + 5]), false, false);
    const auto y = __builtin_amdgcn_permlane32_swap(cvtpk(a[8 * k + 2], a[8 * k + 3]), cvtpk(a[8 * k + 6], a[8 * k + 7]), false, false);
    *(u32x4*)(dst + 16 * k) = u32x4{x[0], y[0], x[1], y[1]};
  }
  STAMP(10);
  STAMP_FLUSH(P, 0);
}

extern __shared__ __attribute__((aligned(16))) unsigned char g_lds[];

__global__ __launch_bounds__(512, 2) void k_fused(Params P) {
  u64 pc;
  asm volatile("s_getpc_b64 %0" : "=s"(pc));
  const unsigned code_word = __builtin_amdgcn_raw_buffer_load_b32(mk_rsrc((const void*)(pc & ~63ull), CODE_PF),
      (threadIdx.x & 256) * 4096 + (((blockIdx.x >> 3) * 32 + ((threadIdx.x >> 3) & 24) + (threadIdx.x & 7)) * 64), 0, 0);
  SBAR();
  LAS unsigned char* lds = (LAS unsigned char*)g_lds;
  const int w = __builtin_amdgcn_readfirstlane(threadIdx.x >> 6);
  if (w < 4) role_a(P, lds, w, code_word);
  else role_f(P, lds, w - 4);
}

extern "C" void kernel_launch(void* const* d_in, const int* in_sizes, int n_in, void* d_out, int out_size, void* d_ws,
                              size_t ws_size, hipStream_t stream) {
  Params P;
  P.x = (const unsigned short*)d_in[0];
  P.wqkv = (const unsigned short*)d_in[1];
  P.wo = (const unsigned short*)d_in[2];
  P.w1 = (const unsigned short*)d_in[4];
  P.w2 = (const unsigned short*)d_in[5];
  P.gw = (const unsigned short*)d_in[6];
  P.out = (unsigned short*)d_out;
  P.ws = (unsigned char*)d_ws;
  (void)hipFuncSetAttribute((const void*)k_fused, hipFuncAttributeMaxDynamicSharedMemorySize, LDS_BYTES);
  k_fused<<<256, 512, LDS_BYTES, stream>>>(P);
}
```

```cpp
#define OAUX 2
#include <hip/hip_runtime.h>
#include <hip/hip_bf16.h>
#include <stdint.h>

typedef short bf16x8 __attribute__((ext_vector_type(8)));
typedef short s16x4 __attribute__((ext_vector_type(4)));
typedef float f32x16 __attribute__((ext_vector_type(16)));
typedef float f32x4 __attribute__((ext_vector_type(4)));
typedef unsigned u32x4 __attribute__((ext_vector_type(4)));
typedef unsigned u32x2 __attribute__((ext_vector_type(2)));
#define LAS __attribute__((address_space(3)))
#define DI __device__ __forceinline__
#define MFMA(a, b, c) __builtin_amdgcn_mfma_f32_32x32x16_bf16((a), (b), (c), 0, 0, 0)
#define SBAR() __builtin_amdgcn_sched_barrier(0)

constexpr int SEQ = 2048, DM = 448, NH = 16, DH = 28, FF = 1792;
constexpr unsigned FL_OFF = 0;
#define SETB 1u
constexpr unsigned SET_BASE = SETB << 20, SET_STRIDE = 24u << 20;
#define GQS 16u
constexpr unsigned GQW = GQS == 16u ? 128u : 4u * GQS;
constexpr unsigned GQ_OFF = 0;
constexpr unsigned GH_OFF = GQ_OFF + 256 * GQW;
constexpr unsigned GO_OFF = GH_OFF + 256 * 128;
constexpr unsigned GW_OFF = GO_OFF + 1024 * 128;
constexpr unsigned GG_OFF = GW_OFF + 256 * 8;
constexpr unsigned QF_OFF = 524288;
constexpr unsigned KV_OFF = QF_OFF + 16 * 64 * 2048;
constexpr unsigned HF_OFF = KV_OFF + 16 * 64 * 4096;
constexpr unsigned WOF_OFF = HF_OFF + 64 * 56 * 2048;
constexpr unsigned OF_OFF = WOF_OFF + 14 * 16 * 2048;
constexpr unsigned WS_END = OF_OFF + 64 * 16 * 2048;
static_assert(GG_OFF + 2048 * 8 <= QF_OFF && WS_END <= SET_STRIDE, "workspace map");

constexpr int L_X = 0;
constexpr int L_WQ = 114688;
constexpr int L_W1 = 114688;
constexpr int L_CTRL = 163776;
constexpr int L_W2 = 0;
constexpr int L_CMB = 98304;
constexpr int LDS_BYTES = 163840;

constexpr float QSCALE = 0.18898223650461362f * 1.4426950408889634f;
constexpr float LOG2E = 1.4426950408889634f;
constexpr float SAFE_BOUND = 60.0f;
constexpr unsigned SPIN_CAP = 1u << 18;
#define ROT1(rb, j) ((((rb) + (j)) & 1) * 3)
#define KVD 4
#define ROTA 0
#define ROT2 0
#define PDELAY 16
#define ODELAY 10
#define FDELAY 18
#define CODE_PF 44032
#define MPF 1
#define HDELAY 1
#define TRIG 3
#define WDELAY 100
#define FTHR 8
#define DEARLY W2S
#define F1PRIO 3
#define POLL_A 4

struct Params {
  const unsigned short* x;
  const unsigned short* wqkv;
  const unsigned short* wo;
  const unsigned short* w1;
  const unsigned short* w2;
  const unsigned short* gw;
  unsigned short* out;
  unsigned char* ws;
};
typedef unsigned long long u64;
typedef __attribute__((address_space(1))) u64 gu64;

DI unsigned cvtpk(float lo, float hi) {
  typedef __bf16 bf2 __attribute__((ext_vector_type(2)));
  typedef float f2 __attribute__((ext_vector_type(2)));
  f2 v = {lo, hi};
  bf2 r = __builtin_convertvector(v, bf2);
  return __builtin_bit_cast(unsigned, r);
}
DI bf16x8 pack8(const f32x16& x, int s) {
  u32x4 p;
  p[0] = cvtpk(x[8 * s + 0], x[8 * s + 1]);
  p[1] = cvtpk(x[8 * s + 2], x[8 * s + 3]);
  p[2] = cvtpk(x[8 * s + 4], x[8 * s + 5]);
  p[3] = cvtpk(x[8 * s + 6], x[8 * s + 7]);
  return __builtin_bit_cast(bf16x8, p);
}
DI float swap_half(float v) {
  auto r = __builtin_amdgcn_permlane32_swap(__float_as_uint(v), __float_as_uint(v), false, false);
  return (threadIdx.x & 32) ? __uint_as_float(r[0]) : __uint_as_float(r[1]);
}
template <int CTRL> DI float dpp_mov(float v) {
  return __uint_as_float(__builtin_amdgcn_update_dpp(__float_as_uint(v), __float_as_uint(v), CTRL, 0xf, 0xf, false));
}
DI float row16_max(float v) {
  v = fmaxf(v, dpp_mov<0x128>(v));
  v = fmaxf(v, dpp_mov<0x124>(v));
  v = fmaxf(v, dpp_mov<0x122>(v));
  v = fmaxf(v, dpp_mov<0x121>(v));
  return v;
}
DI unsigned row16_umax(unsigned v) {
  v = max(v, (unsigned)__builtin_amdgcn_update_dpp(v, v, 0x128, 0xf, 0xf, false));
  v = max(v, (unsigned)__builtin_amdgcn_update_dpp(v, v, 0x124, 0xf, 0xf, false));
  v = max(v, (unsigned)__builtin_amdgcn_update_dpp(v, v, 0x122, 0xf, 0xf, false));
  v = max(v, (unsigned)__builtin_amdgcn_update_dpp(v, v, 0x121, 0xf, 0xf, false));
  return v;
}
DI float rdlane(float v, int l) { return __uint_as_float(__builtin_amdgcn_readlane(__float_as_uint(v), l)); }
DI s16x4 tr_read(const LAS unsigned char* p) {
  typedef short v4i16_t __attribute__((ext_vector_type(4)));
  return __builtin_bit_cast(s16x4, __builtin_amdgcn_ds_read_tr16_b64_v4i16((LAS v4i16_t*)p));
}
DI bf16x8 tr_frag(const LAS unsigned char* p, int row_stride4) {
  s16x4 lo = tr_read(p), hi = tr_read(p + row_stride4);
  return __builtin_shufflevector(lo, hi, 0, 1, 2, 3, 4, 5, 6, 7);
}
DI __amdgpu_buffer_rsrc_t mk_rsrc(const void* p, unsigned bytes) {
  return __builtin_amdgcn_make_buffer_rsrc((void*)p, (short)0, (int)bytes, 0x00020000);
}
DI void glds16(__amdgpu_buffer_rsrc_t rs, unsigned dst, unsigned voff, unsigned soff) {
  asm volatile("s_mov_b32 m0, %0\n\ts_nop 0\n\tbuffer_load_dwordx4 %1, %2, %3 offen lds" ::"s"(dst), "v"(voff), "s"(rs), "s"(soff) : "memory");
}
DI u32x4 ld16(__amdgpu_buffer_rsrc_t rs, unsigned voff, unsigned soff) { return __builtin_amdgcn_raw_buffer_load_b128(rs, voff, soff, 0); }
DI u32x4 ld16c(__amdgpu_buffer_rsrc_t rs, unsigned voff, unsigned soff) { return __builtin_amdgcn_raw_buffer_load_b128(rs, voff, soff, 16); }
DI void st16(__amdgpu_buffer_rsrc_t rs, u32x4 v, unsigned voff, unsigned soff) { __builtin_amdgcn_raw_buffer_store_b128(v, rs, voff + soff, 0, 16); }
DI bf16x8 as_frag(u32x4 v) { return __builtin_bit_cast(bf16x8, v); }
DI u32x4 as_u4(bf16x8 v) { return __builtin_bit_cast(u32x4, v); }
template <int N> DI void wait_bar() { asm volatile("s_waitcnt vmcnt(%0) lgkmcnt(0)\n\ts_barrier" ::"n"(N) : "memory"); }

template <int V> struct IC { static constexpr int value = V; };
template <int I, int N, class Fn> DI void static_for(Fn&& fn) {
  if constexpr (I < N) { fn(IC<I>{}); static_for<I + 1, N>(fn); }
}
DI void drain() { asm volatile("s_waitcnt vmcnt(0)" ::: "memory"); }
template <int N> DI void wait_vm() { asm volatile("s_waitcnt vmcnt(%0)" ::"n"(N) : "memory"); }
DI void put_granule(unsigned char* ws, unsigned off, u64 tag16, unsigned payload) {
  __hip_atomic_store((gu64*)(ws + off), tag16 | (u64)(payload & 0xffffu), __ATOMIC_RELAXED, __HIP_MEMORY_SCOPE_AGENT);
}
template <int SLEEP> DI unsigned poll_granule(unsigned char* ws, unsigned off, u64 tag16) {
  u64 v = ~tag16;
  for (unsigned spins = 0;; ++spins) {
    if (v - tag16 > 0xffffull) v = __hip_atomic_load((gu64*)(ws + off), __ATOMIC_RELAXED, __HIP_MEMORY_SCOPE_AGENT);
    if (__all(v - tag16 <= 0xffffull) || spins > SPIN_CAP) break;
    __builtin_amdgcn_s_sleep(SLEEP);
  }
  __builtin_amdgcn_fence(__ATOMIC_ACQUIRE, "wavefront");
  return (unsigned)v & 0xffffu;
}
template <int SLEEP = 1> DI unsigned wait_lds_word(LAS unsigned char* p, unsigned want) {
  unsigned v = 0;
#pragma nounroll
  for (unsigned spins = 0; spins < (1u << 24); ++spins) {
    v = __builtin_amdgcn_readfirstlane(__hip_atomic_load((LAS unsigned*)p, __ATOMIC_RELAXED, __HIP_MEMORY_SCOPE_WORKGROUP));
    if (v >= want) break;
    __builtin_amdgcn_s_sleep(SLEEP);
  }
  asm volatile("" ::: "memory");
  return v;
}
template <int N> DI u64 wait_bar_epoch(const unsigned char* p) {
  u64 ep;
  asm volatile("s_load_dwordx2 %0, %1, 0x0\n\ts_waitcnt vmcnt(%2) lgkmcnt(0)\n\ts_barrier" : "=&s"(ep) : "s"(p), "n"(N) : "memory");
  return ep;
}
DI void bar_lds() { asm volatile("s_waitcnt lgkmcnt(0)\n\ts_barrier" ::: "memory"); }
DI void st_tile(LAS unsigned char* p, const f32x16& a) {
#pragma unroll
  for (int g = 0; g < 4; ++g) *(LAS f32x4*)(p + g * 1024) = f32x4{a[4 * g], a[4 * g + 1], a[4 * g + 2], a[4 * g + 3]};
}
DI void add_tile(f32x16& a, const LAS unsigned char* p) {
#pragma unroll
  for (int g = 0; g < 4; ++g) {
    const f32x4 v = *(const LAS f32x4*)(p + g * 1024);
    a[4 * g] += v[0]; a[4 * g + 1] += v[1]; a[4 * g + 2] += v[2]; a[4 * g + 3] += v[3];
  }
}

#define STAMP_DECL do {} while (0)
#define STAMP(id) do {} while (0)
#define QSTAMP(id) do {} while (0)
#define RSTAMP(id) do {} while (0)
#define STAMP_FLUSH(P, ph) do {} while (0)

DI void lds_arrive(LAS unsigned char* p) {
  asm volatile("s_mov_b64 exec, 1\n\tds_add_u32 %0, %1\n\ts_mov_b64 exec, -1" ::"v"((unsigned)(uintptr_t)p), "v"(1u) : "memory");
}

#define PINV(x) asm volatile("" : "+v"(x))
#define PINS(x) asm volatile("" : "+s"(x))
DI float sumsq(const u32x4& a, const u32x4& b) {
  typedef __bf16 bf2 __attribute__((ext_vector_type(2)));
  float n0 = 0.f, n1 = 0.f;
#pragma unroll
  for (int i = 0; i < 4; ++i) {
    n0 = __builtin_amdgcn_fdot2_f32_bf16(__builtin_bit_cast(bf2, a[i]), __builtin_bit_cast(bf2, a[i]), n0, false);
    n1 = __builtin_amdgcn_fdot2_f32_bf16(__builtin_bit_cast(bf2, b[i]), __builtin_bit_cast(bf2, b[i]), n1, false);
  }
  return n0 + n1;
}

DI void role_a(const Params& P, LAS unsigned char* lds, const int wt, const unsigned code_word) {
  const int lane = threadIdx.x & 63, r = lane & 31, h = lane >> 5;
  const int b = blockIdx.x, xcd = b & 7, idx = b >> 3;
  const unsigned lo = lane * 16;
  STAMP_DECL;
  STAMP(0);
  __builtin_amdgcn_s_setprio(2);
  unsigned xtouch = 0;
  if (wt < 2) xtouch = __builtin_amdgcn_raw_buffer_load_b32(wt ? mk_rsrc(P.wqkv, DM * 3 * DM * 2) : mk_rsrc(P.x, SEQ * DM * 2),
                                                            (unsigned)(wt ? b * 37 + (lane < 37 ? lane : 0) : b * 56 + (lane < 56 ? lane : 0)) * 128, 0, 0);
  SBAR();
  f32x16 acc[4];
  const int rb = (xcd & 1) * 8 + (idx & 7), j = (xcd >> 1) * 4 + (idx >> 3);
  const int head = xcd * 2 + (idx & 1), qb = idx >> 1;
  unsigned sto[3];
#pragma unroll
  for (int ct = 0; ct < 3; ++ct) {
    const int u = 3 * j + ct, part = u >> 4, t = (u & 15) * 64 + rb * 4 + wt;
    sto[ct] = lo + (part == 0 ? QF_OFF + t * 2048 : KV_OFF + t * 4096 + (part - 1) * 2048);
    PINV(sto[ct]);
  }
  unsigned padv = r == DH ? 0x3f803f80u : 0u;
  PINV(padv);
  const int gq = head / 3, gk = (16 + head) / 3, gvv = (32 + head) / 3;
  unsigned pub_off = GQ_OFF + (j * 16 + rb) * GQW + wt * GQS + (lane & 1) * 8;
  unsigned poll_off = GQ_OFF + (h ? gq * 16 + qb : (lane < 16 ? gk : gvv) * 16 + wt * 4 + ((lane >> 2) & 3)) * GQW + (lane & 3) * GQS + h * 8;
  unsigned chk_off = GQ_OFF + (gk * 16 + (lane >> 2)) * GQW + (lane & 3) * GQS;
  unsigned g_off = GG_OFF + ((qb * 4 + wt) * 32 + r) * 8;
  unsigned o_off = lo + OF_OFF + ((qb * 4 + wt) * 16 + head) * 2048;
  PINV(pub_off); PINV(poll_off); PINV(chk_off); PINV(g_off); PINV(o_off);
  unsigned kvbase = KV_OFF + (head * 64 + wt * 16) * 4096, go_off = GO_OFF + ((qb * 4 + wt) * 16 + head) * 128;
  unsigned qofs[4];
#pragma unroll
  for (int t = 0; t < 4; ++t) { qofs[t] = QF_OFF + (unsigned)(head * 64 + qb * 4 + ((wt + 1 + t) & 3)) * 2048; PINS(qofs[t]); }
  PINS(kvbase); PINS(go_off);
  unsigned xoff[4];
#pragma unroll
  for (int ks = 0; ks < 4; ++ks) xoff[ks] = (unsigned)(L_X + wt * 4096 + r * 128 + (((2 * ks + h) ^ ((r >> 1) & 7)) << 4));
  const unsigned wqoff = (unsigned)(L_WQ + (8 * h + ((lane & 15) >> 2)) * 192 + ((lane >> 4) & 1) * 32 + (lane & 3) * 8 + (j & 1) * 8);
  u64 ep;
  asm volatile("s_load_dwordx2 %0, %1, 0x0\n\ts_waitcnt lgkmcnt(0)" : "=&s"(ep) : "s"(P.ws + FL_OFF) : "memory");
  u64 tag16 = (ep + 1) << 16;
  unsigned char* wsb = P.ws + SET_BASE + (((unsigned)ep + 1u) & 3u) * SET_STRIDE;
  PINS(tag16); PINS(wsb);
  const __amdgpu_buffer_rsrc_t rsWs = mk_rsrc(wsb, WS_END);
#pragma unroll
  for (int i = 0; i < 3; ++i) acc[i] = f32x16{};
  unsigned wtouch = 0;
  auto kloop = [&](auto nvc) {
    constexpr int NV = decltype(nvc)::value;
    bf16x8 xf[4], wf[4][3];
    auto mm = [&](int ks, int ct) { acc[ct] = ct >= 3 - NV ? MFMA(xf[ks], wf[ks][ct], acc[ct]) : MFMA(wf[ks][ct], xf[ks], acc[ct]); };
    auto step = [&](int c, int ks, bool domm) {
      const int km = (ks + 2) & 3;
#pragma unroll
      for (int ct = 0; ct < 3; ++ct) {
        if (domm) mm(km, ct);
        SBAR();
        if (ct == 0) xf[ks] = *(const LAS bf16x8*)(lds + xoff[ks] + c * 16384);
        wf[ks][ct] = tr_frag(lds + wqoff + (c & 3) * 12288 + ks * 16 * 192 + ct * 56, 4 * 192);
        SBAR();
      }
    };
#pragma unroll
    for (int c = 0; c < 7; ++c) {
      bar_lds();
      if (c == 0) STAMP(1);
      if (c == MPF) {
        const unsigned l = (unsigned)(b * 128 + (wt & 1) * 64 + lane) * 128;
        if (wt < 2) wtouch = l < DM * FF * 2 ? __builtin_amdgcn_raw_buffer_load_b32(mk_rsrc(P.w1, DM * FF * 2), l, 0, 0)
                           : l < 2 * DM * FF * 2 ? __builtin_amdgcn_raw_buffer_load_b32(mk_rsrc(P.w2, DM * FF * 2), l - DM * FF * 2, 0, 0)
                                                 : __builtin_amdgcn_raw_buffer_load_b32(mk_rsrc(P.wo, DM * DM * 2), l - 2 * DM * FF * 2, 0, 0);
        SBAR();
      }
      QSTAMP(1 + c);
      step(c, 0, c > 0);
      step(c, 1, c > 0);
      step(c, 2, true);
      step(c, 3, true);
    }
    bar_lds();
    QSTAMP(8);
#pragma unroll
    for (int ct = 0; ct < 3; ++ct) { mm(2, ct); mm(3, ct); SBAR(); }
  };
  auto epi = [&](auto nqc, auto nvc) {
    constexpr int NQ = decltype(nqc)::value, NV = decltype(nvc)::value;
    STAMP(2);
    asm volatile("" ::"v"(code_word), "v"(wtouch), "v"(xtouch));
    u32x4 t0[3], t1[3];
#pragma unroll
    for (int ct = 0; ct < 3; ++ct) {
      if (ct < NQ) acc[ct] *= QSCALE;
      t0[ct] = as_u4(pack8(acc[ct], 0));
      if (ct >= 3 - NV) {
#pragma unroll
        for (int i = 0; i < 4; ++i) t0[ct][i] = r < DH ? t0[ct][i] : padv;
      }
      st16(rsWs, t0[ct], sto[ct], 0);
      t1[ct] = as_u4(pack8(acc[ct], 1));
      if (ct >= 3 - NV) {
#pragma unroll
        for (int i = 0; i < 4; ++i) t1[ct][i] = r < DH ? t1[ct][i] : padv;
      } else {
        t1[ct][2] = h ? (ct < NQ ? 0u : 0x3f803f80u) : t1[ct][2];
        t1[ct][3] = h ? (ct < NQ ? 0u : 0x00003f80u) : t1[ct][3];
      }
      st16(rsWs, t1[ct], sto[ct], 1024);
      SBAR();
    }
    STAMP(8);
    unsigned mq = 0, mk = 0;
#pragma unroll
    for (int ct = 0; ct < 3; ++ct) {
      const unsigned n = __float_as_uint(sumsq(t0[ct], t1[ct])) & 0x7fffffffu;
      if (ct < NQ) mq = max(mq, n); else mk = max(mk, n);
    }
    auto bound = [&](unsigned m) {
      m = row16_umax(m);
      const float lo2 = __uint_as_float(max((unsigned)__builtin_amdgcn_readlane(m, 0), (unsigned)__builtin_amdgcn_readlane(m, 16)));
      const float hi2 = __uint_as_float(max((unsigned)__builtin_amdgcn_readlane(m, 32), (unsigned)__builtin_amdgcn_readlane(m, 48)));
      return ((__float_as_uint(lo2 + hi2) >> 16) + 1u) & 0xffffu;
    };
    const unsigned pq = NQ > 0 ? bound(mq) : 0u, pk = NQ < 3 ? bound(mk) : 0u;
    u64 gran = tag16 | ((lane & 1) ? pq : pk);
    PINV(gran);
    drain();
    STAMP(9);
    if (lane < 2) __hip_atomic_store((gu64*)(wsb + pub_off), gran, __ATOMIC_RELAXED, __HIP_MEMORY_SCOPE_AGENT);
  };
  if (j < 10) {
    kloop(IC<0>{});
    if (j < 5) epi(IC<3>{}, IC<0>{});
    else if (j == 5) epi(IC<1>{}, IC<0>{});
    else epi(IC<0>{}, IC<0>{});
  } else if (j == 10) {
    kloop(IC<1>{});
    epi(IC<0>{}, IC<1>{});
  } else {
    kloop(IC<3>{});
    epi(IC<0>{}, IC<3>{});
  }
  STAMP(3);
  __builtin_amdgcn_s_sleep(PDELAY);
  const unsigned pay = poll_granule<POLL_A>(wsb, poll_off, tag16);
  STAMP(4);
  u32x4 q[4][2];
  LAS unsigned char* const cmb = lds + L_CMB + wt * 12288 + lo;
  u64 gv = 0;
  unsigned thr = 0;
#pragma nounroll
  for (int pass = 0;; ++pass) {
    u32x4 kv[KVD][4];
    kv[0][0] = ld16c(rsWs, lo, kvbase);
    SBAR();
    if (pass == 0) { q[0][0] = ld16c(rsWs, lo, qofs[0]); q[0][1] = ld16c(rsWs, lo + 1024, qofs[0]); }
    SBAR();
    kv[0][1] = ld16c(rsWs, lo + 1024, kvbase);
    SBAR();
    if (pass == 0) {
#pragma unroll
      for (int t = 1; t < 4; ++t) { q[t][0] = ld16c(rsWs, lo, qofs[t]); q[t][1] = ld16c(rsWs, lo + 1024, qofs[t]); SBAR(); }
    }
#pragma unroll
    for (int e = 2; e < 4 * (KVD - 1); ++e) { kv[e >> 2][e & 3] = ld16c(rsWs, lo + (e & 3) * 1024, kvbase + (e >> 2) * 4096); SBAR(); }
#pragma unroll
    for (int i = 0; i < 4; ++i) acc[i] = f32x16{};
    {
      const float q2 = __uint_as_float(__builtin_amdgcn_readlane(row16_umax(pay), 32) << 16);
      const float lim = SAFE_BOUND * SAFE_BOUND * __builtin_amdgcn_rcpf(q2);
      thr = lim < 1e30f ? __float_as_uint(lim) >> 16 : (lim >= 1e30f ? 0x7000u : 0u);
      PINV(thr);
    }
    __builtin_amdgcn_s_setprio(1);
    f32x16 sc = MFMA(as_frag(kv[0][0]), as_frag(q[0][0]), f32x16{});
    sc = MFMA(as_frag(kv[0][1]), as_frag(q[0][1]), sc);
    STAMP(5);
    u32x4 p0, p1 = {0, 0, 0, 0};
    float e0 = 0.f, e1 = 0.f, e2 = 0.f, e3 = 0.f;
    u64 ck = 0, cv = 0;
#define EXP4(i) do { e0 = __builtin_amdgcn_exp2f(sc[i]); e1 = __builtin_amdgcn_exp2f(sc[(i) + 1]); e2 = __builtin_amdgcn_exp2f(sc[(i) + 2]); e3 = __builtin_amdgcn_exp2f(sc[(i) + 3]); \
    asm volatile("" : "+v"(e0), "+v"(e1), "+v"(e2), "+v"(e3)); SBAR(); } while (0)
#define CVT2(P, i) do { P[i] = cvtpk(e0, e1); P[(i) + 1] = cvtpk(e2, e3); asm volatile("" : "+v"(P)); SBAR(); } while (0)
#define PART(t, g) do { *(LAS f32x4*)(cmb + (t) * 4096 + (g) * 1024) = f32x4{acc[t][4 * (g)], acc[t][4 * (g) + 1], acc[t][4 * (g) + 2], acc[t][4 * (g) + 3]}; SBAR(); } while (0)
    static_for<0, 64>([&](auto ic) {
      constexpr int i = decltype(ic)::value;
      constexpr int kt = i >> 2, t = i & 3, kn_ = (i + 1) >> 2, tn = (i + 1) & 3, kp = (i - 1) >> 2, tp = (i - 1) & 3;
      constexpr int kl = kt + KVD - 1;
      if (i == 24) STAMP(11);
      if (i == 56) (void)wait_lds_word(lds + L_CTRL, 4);
      f32x16 sn;
      SBAR();
      if (i < 63) sn = MFMA(as_frag(kv[kn_ % KVD][0]), as_frag(q[tn][0]), f32x16{});
      SBAR();
      if constexpr (i >= 62) PART(i - 62, 0);
      if (i > 0) CVT2(p1, 2);
      EXP4(0);
      if (i > 0) acc[tp] = MFMA(as_frag(kv[(kp + KVD) % KVD][3]), as_frag(p1), acc[tp]);
      SBAR();
      if constexpr (t == 0 && kl < 16) {
#pragma unroll
        for (int e = 0; e < 4; ++e) kv[kl % KVD][e] = ld16c(rsWs, lo + e * 1024, kvbase + kl * 4096);
      }
      if constexpr (t == 2 && kt + 1 < 16) {
        constexpr int n = 4 * ((kl < 16 ? kl : 15) - (kt + 1)) + (i > 53 ? 3 : 0);
        __builtin_amdgcn_s_waitcnt(0x0f70 | (n & 15) | ((n >> 4) << 14));
      }
      if (i == 51) gv = __hip_atomic_load((gu64*)(wsb + g_off), __ATOMIC_RELAXED, __HIP_MEMORY_SCOPE_AGENT);
      if (i == 52) ck = __hip_atomic_load((gu64*)(wsb + chk_off), __ATOMIC_RELAXED, __HIP_MEMORY_SCOPE_AGENT);
      if (i == 53) cv = __hip_atomic_load((gu64*)(wsb + (gvv - gk) * 16 * GQW + chk_off), __ATOMIC_RELAXED, __HIP_MEMORY_SCOPE_AGENT);
      SBAR();
      if constexpr (i >= 62) PART(i - 62, 1);
      CVT2(p0, 0);
      EXP4(4);
      if (i < 63) sn = MFMA(as_frag(kv[kn_ % KVD][1]), as_frag(q[tn][1]), sn);
      SBAR();
      if constexpr (i >= 62) PART(i - 62, 2);
      CVT2(p0, 2);
      EXP4(8);
      acc[t] = MFMA(as_frag(kv[kt % KVD][2]), as_frag(p0), acc[t]);
      SBAR();
      if constexpr (i >= 62) PART(i - 62, 3);
      CVT2(p1, 0);
      EXP4(12);
      if (i < 63) sc = sn;
    });
    CVT2(p1, 2);
    acc[3] = MFMA(as_frag(kv[15 % KVD][3]), as_frag(p1), acc[3]);
    SBAR();
    PART(2, 0); PART(2, 1); PART(2, 2); PART(2, 3);
#undef EXP4
#undef CVT2
#undef PART
    __builtin_amdgcn_s_setprio(2);
    if (__builtin_expect(__all(ck - tag16 <= (u64)thr && cv - tag16 <= 0x7000ull && gv - tag16 <= 0xffffull), 1)) break;
    gv = poll_granule<2>(wsb, g_off, tag16);
    if (pass) break;
    const unsigned pk = poll_granule<2>(wsb, chk_off, tag16), pv = poll_granule<2>(wsb, (gvv - gk) * 16 * GQW + chk_off, tag16);
    if (__all(pk <= thr && pv <= 0x7000u)) break;
    float mx[4];
#pragma unroll
    for (int t = 0; t < 4; ++t) mx[t] = -INFINITY;
    for (int kt = 0; kt < 64; ++kt) {
      const u32x4 k0 = ld16c(rsWs, lo, KV_OFF + (head * 64 + kt) * 4096), k1 = ld16c(rsWs, lo + 1024, KV_OFF + (head * 64 + kt) * 4096);
#pragma unroll
      for (int t = 0; t < 4; ++t) {
        f32x16 s = MFMA(as_frag(k0), as_frag(q[t][0]), f32x16{});
        s = MFMA(as_frag(k1), as_frag(q[t][1]), s);
#pragma unroll
        for (int i = 0; i < 16; ++i) mx[t] = fmaxf(mx[t], s[i]);
      }
    }
#pragma unroll
    for (int t = 0; t < 4; ++t) {
      const float nm = -fmaxf(mx[t], swap_half(mx[t]));
      const unsigned hi = __float_as_uint(nm) & 0xffff0000u;
      const float r1 = nm - __uint_as_float(hi);
      const unsigned mid = __float_as_uint(r1) & 0xffff0000u;
      const float r2 = r1 - __uint_as_float(mid);
      if (h) { q[t][1][2] = (hi >> 16) | mid; q[t][1][3] = __float_as_uint(r2) >> 16; }
    }
  }
  STAMP(6);
  asm volatile("s_waitcnt lgkmcnt(0)" ::: "memory");
  lds_arrive(lds + L_CTRL + 20);
  (void)wait_lds_word(lds + L_CTRL + 20, 4);
  f32x16 o = acc[3];
#pragma unroll
  for (int u = 0; u < 3; ++u) add_tile(o, lds + L_CMB + ((wt + 3 - u) & 3) * 12288 + u * 4096 + lo);
  const auto lsw = __builtin_amdgcn_permlane32_swap(__float_as_uint(o[12]), __float_as_uint(o[12]), false, false);
  const float inv = __uint_as_float((unsigned)gv << 16) * __builtin_amdgcn_rcpf(__uint_as_float(lsw[1]));
  o *= inv;
  st16(rsWs, as_u4(pack8(o, 0)), o_off, 0);
  u32x4 o1 = as_u4(pack8(o, 1));
  o1[2] = h ? 0u : o1[2];
  o1[3] = h ? 0u : o1[3];
  st16(rsWs, o1, o_off, 1024);
  STAMP(7);
  drain();
  if (lane == 0) __hip_atomic_store((gu64*)(wsb + go_off), tag16, __ATOMIC_RELAXED, __HIP_MEMORY_SCOPE_AGENT);
  STAMP(10);
  STAMP_FLUSH(P, 0);
}

#define W2S 6
#define HPRE 6
#define NPRE 14
#define LEA 5
#define LEB 11
#define LE0 16
#define LU0 3
#define F1LA 2
#define PACE 2
#define PACE2 2
constexpr int ffn2_tail(int f) { return f + HPRE < 18 ? 4 : 0; }
constexpr int ffn2_ops(int f) { return (f + W2S < 14 ? 4 : 0) + ffn2_tail(f); }
constexpr int ffn2_younger(int f, int upto) {
  int n = ffn2_tail(f - W2S);
  for (int i = f - W2S + 1; i < upto; ++i) n += ffn2_ops(i);
  return n;
}

DI void role_f(const Params& P, LAS unsigned char* lds, const int wt) {
  u64 ep;
  const int lane = threadIdx.x & 63, r = lane & 31, h = lane >> 5;
  const int b = blockIdx.x, xcd = b & 7, idx = b >> 3;
  const int rb = (xcd & 1) * 8 + (idx & 7), j = (xcd >> 1) * 4 + (idx >> 3);
  const unsigned lo = lane * 16, lds0 = (unsigned)(uintptr_t)lds;
  const bool ffn = j < 14;
  STAMP_DECL;
  STAMP(0);
  f32x16 acc[4];
  u32x4 wl[28];
  const __amdgpu_buffer_rsrc_t rsL = mk_rsrc(ffn ? P.w1 : P.gw, ffn ? DM * FF * 2 : DM * 2);
  const unsigned lsrc = ffn ? (unsigned)((lane >> 2) * (FF * 2) + (j * 4 + wt) * 64 + (lane & 3) * 16) : (unsigned)(h * 16);
  const unsigned lstep = ffn ? 16 * FF * 2 : 32;
  const int rot = ROT1(rb, j);
  auto kc = [&](int c) { const int v = c + rot; return v >= 7 ? v - 7 : v; };
  auto L = [&](int u0, int u1) {
#pragma unroll
    for (int u = u0; u < u1; ++u) wl[u] = ld16(rsL, lsrc, (kc(u >> 2) * 4 + (u & 3)) * lstep);
    SBAR();
  };
  {
    const __amdgpu_buffer_rsrc_t rsX = mk_rsrc(P.x, SEQ * DM * 2);
    const __amdgpu_buffer_rsrc_t rsW = mk_rsrc(P.wqkv, DM * 3 * DM * 2);
    unsigned xsrc[2], wsrc[3];
#pragma unroll
    for (int i = 0; i < 2; ++i) {
      const int row = 32 * wt + 8 * i + (lane >> 3), ch = (lane & 7) ^ ((row >> 1) & 7);
      xsrc[i] = (unsigned)((rb * 128 + row) * (DM * 2) + ch * 16);
    }
#pragma unroll
    for (int i = 0; i < 3; ++i) {
      const int p = 64 * (3 * wt + i) + lane, kr = p / 12, ch = p % 12;
      wsrc[i] = (unsigned)(kr * (3 * DM * 2) + j * 168 - (j & 1) * 8 + (ch < 11 ? ch : 10) * 16);
    }
    auto X = [&](int c) {
#pragma unroll
      for (int i = 0; i < 4; ++i) glds16(rsX, lds0 + L_X + c * 16384 + (4 * wt + i) * 1024, xsrc[i & 1], kc(c) * 128 + (i >> 1) * 16 * DM * 2);
    };
    auto W = [&](int c) {
#pragma unroll
      for (int i = 0; i < 3; ++i) glds16(rsW, lds0 + L_WQ + (c & 3) * 12288 + (3 * wt + i) * 1024, wsrc[i], kc(c) * 64 * 3 * DM * 2);
    };
    X(0); W(0); X(1); W(1); X(2);
    QSTAMP(1);
    wait_bar<11>();
    STAMP(1);
    W(2); X(3); W(3);
    QSTAMP(2);
    wait_bar<14>();
    X(4); W(4);
    QSTAMP(3);
    wait_bar<14>();
    X(5); W(5);
    QSTAMP(4);
    wait_bar<14>();
    X(6); W(6);
    QSTAMP(5);
    wait_bar<14>();
    if (wt == 0 && lane < 8) *(LAS unsigned*)(lds + L_CTRL + lane * 4) = 0u;
    L(0, LEA);
    QSTAMP(6);
    ep = wait_bar_epoch<7 + LEA>(P.ws + FL_OFF);
    L(LEA, LEB);
    QSTAMP(7);
    wait_bar<LEB>();
    L(LEB, LE0);
    QSTAMP(8);
    bar_lds();
    __builtin_amdgcn_s_sleep(FDELAY);
    __builtin_amdgcn_s_setprio(F1PRIO);
  }
  STAMP(2);
  const u64 tag = (ep + 1) << 16;
  unsigned char* const wsb = P.ws + SET_BASE + (((unsigned)ep + 1u) & 3u) * SET_STRIDE;
  const __amdgpu_buffer_rsrc_t rsWs = mk_rsrc(wsb, WS_END);
  unsigned xoff[4];
#pragma unroll
  for (int ks = 0; ks < 4; ++ks) xoff[ks] = (unsigned)(L_X + r * 128 + (((2 * ks + h) ^ ((r >> 1) & 7)) << 4));
  const int mb = 4 * xcd + (idx & 3), nb = idx >> 2;
  const __amdgpu_buffer_rsrc_t rsW2 = mk_rsrc(P.w2, FF * DM * 2), rsH = rsWs;
  const unsigned w2src = (unsigned)((wt * 448 + (lane >> 3)) * (DM * 2) + nb * 128 + ((((lane >> 2) & 1) ^ ((lane >> 4) & 1)) * 64) + (lane & 3) * 16);
  const unsigned ring0 = lds0 + L_W2 + wt * (W2S * 4096);
  const int rot2 = ROT2;
  auto fr = [&](int f) { const int v = f + rot2; return v >= 14 ? v - 14 : v; };
  auto D = [&](int f) {
#pragma unroll
    for (int i = 0; i < 4; ++i) glds16(rsW2, ring0 + (f % W2S) * 4096 + i * 1024, w2src, (fr(f) * 32 + i * 8) * DM * 2);
  };
  const bool has_tile = idx < 28;
  auto fill_ring = [&]() {
    if (has_tile) {
      (void)wait_lds_word(lds + L_CTRL, 4);
#pragma unroll
      for (int f = 0; f < DEARLY; ++f) { D(f); wait_vm<FTHR>(); __builtin_amdgcn_s_sleep(PACE); }
    }
  };
  if (ffn) {
    LAS unsigned char* ring = lds + L_W1 + wt * 8192;
    const unsigned troff = (unsigned)((8 * h + ((lane & 15) >> 2)) * 64 + ((lane >> 4) & 1) * 32 + (lane & 3) * 8);
#pragma unroll
    for (int i = 0; i < 4; ++i) acc[i] = f32x16{};
    RSTAMP(1);
    constexpr int LA = F1LA;
    bf16x8 xf[LA + 1][4], wf[LA + 1];
    auto wr = [&](int u) { *(LAS u32x4*)(ring + (u & 7) * 1024 + lo) = wl[u]; };
    auto rdw = [&](int u) { wf[u % (LA + 1)] = tr_frag(ring + (u & 7) * 1024 + troff, 4 * 64); };
    auto rdx = [&](int u, int t) { xf[u % (LA + 1)][t] = *(const LAS bf16x8*)(lds + xoff[u & 3] + (u >> 2) * 16384 + t * 4096); };
#pragma unroll
    for (int u = 0; u < LA + 2; ++u) wr(u);
#pragma unroll
    for (int u = 0; u < LA; ++u) {
      rdw(u);
#pragma unroll
      for (int t = 0; t < 4; ++t) rdx(u, t);
    }
    SBAR();
#pragma unroll
    for (int u = 0; u < 28; ++u) {
      if ((u & 3) == 0) RSTAMP(2 + (u >> 2));
#pragma unroll
      for (int t = 0; t < 4; ++t) {
        if (u + LA < 28) {
          if (t == 1) rdw(u + LA);
          rdx(u + LA, t);
          if (t == 3 && u + LA + 2 < 28) wr(u + LA + 2);
        }
        SBAR();
        acc[t] = MFMA(wf[u % (LA + 1)], xf[u % (LA + 1)][t], acc[t]);
        SBAR();
        if (u >= LU0 && LE0 + (u - LU0) * 4 + t < 28) L(LE0 + (u - LU0) * 4 + t, LE0 + (u - LU0) * 4 + t + 1);
      }
    }
    lds_arrive(lds + L_CTRL);
    RSTAMP(9);
    STAMP(3);
    __builtin_amdgcn_s_setprio(3);
#pragma unroll
    for (int t = 0; t < 4; ++t) {
      f32x16 z = acc[t];
#pragma unroll
      for (int i = 0; i < 16; ++i) z[i] = z[i] * __builtin_amdgcn_rcpf(1.f + __builtin_amdgcn_exp2f(-LOG2E * z[i]));
      const unsigned so = HF_OFF + ((rb * 4 + t) * 56 + j * 4 + wt) * 2048;
      st16(rsWs, as_u4(pack8(z, 0)), lo, so);
      st16(rsWs, as_u4(pack8(z, 1)), lo + 1024, so);
    }
    STAMP(11);
    drain();
    if (lane == 0) put_granule(wsb, GH_OFF + (rb * 16 + j) * 128 + wt * 8, tag, 0);
  } else {
    __builtin_amdgcn_s_setprio(3);
    if (j == 14) {
      L(LE0, 28);
      acc[0] = f32x16{};
#pragma unroll
      for (int u = 0; u < 28; ++u) {
        const bf16x8 xf = *(const LAS bf16x8*)(lds + xoff[u & 3] + (u >> 2) * 16384 + wt * 4096);
        acc[0] = MFMA(as_frag(wl[u]), xf, acc[0]);
      }
      const float g = __builtin_amdgcn_rcpf(1.f + __builtin_amdgcn_exp2f(-LOG2E * acc[0][0]));
      if (h == 0) put_granule(wsb, GG_OFF + ((rb * 4 + wt) * 32 + r) * 8, tag, cvtpk(g, 0.f));
    }
    lds_arrive(lds + L_CTRL);
    __builtin_amdgcn_s_sleep(WDELAY);
    const __amdgpu_buffer_rsrc_t rsO = mk_rsrc(P.wo, DM * DM * 2);
    const int id0 = ((j - 14) * 16 + rb) * 4 + wt;
    for (int id = id0; id < 224; id += 128) {
      const int nt = id >> 4, hd = id & 15;
      unsigned rp[16];
#pragma unroll
      for (int e = 0; e < 16; ++e)
        rp[e] = __builtin_amdgcn_raw_buffer_load_b16(rsO, (unsigned)(((4 * h + 8 * (e >> 2) + (e & 3)) * DM + r) * 2), (unsigned)((DH * hd * DM + 32 * nt) * 2), 0);
      u32x4 a, c2;
#pragma unroll
      for (int i = 0; i < 4; ++i) {
        a[i] = (rp[2 * i] & 0xffffu) | (rp[2 * i + 1] << 16);
        c2[i] = (rp[8 + 2 * i] & 0xffffu) | (rp[8 + 2 * i + 1] << 16);
      }
      c2[2] = h ? 0u : c2[2];
      c2[3] = h ? 0u : c2[3];
      st16(rsWs, a, lo, WOF_OFF + id * 2048);
      st16(rsWs, c2, lo + 1024, WOF_OFF + id * 2048);
      drain();
      if (lane == 0) put_granule(wsb, GW_OFF + id * 8, tag, 0);
    }
  }
  STAMP(4);
  if (idx >= 28) {
    if (b == 255 && wt == 0) {
      __builtin_amdgcn_s_sleep(127);
#pragma unroll
      for (int i = 0; i < 4; ++i) (void)poll_granule<64>(wsb, GQ_OFF + (i * 64 + lane) * GQW, tag);
      if (lane == 0) __hip_atomic_store((gu64*)(P.ws + FL_OFF), ep + 1, __ATOMIC_RELAXED, __HIP_MEMORY_SCOPE_AGENT);
    }
    STAMP(10);
    STAMP_FLUSH(P, 0);
    return;
  }
  fill_ring();
  const float gl = __uint_as_float(poll_granule<8>(wsb, GG_OFF + (mb * 64 + lane) * 8, tag) << 16), gs = swap_half(gl);
  const float og0 = 1.f - (h ? gs : gl), og1 = 1.f - (h ? gl : gs);
  __builtin_amdgcn_s_sleep(HDELAY);
  const int ftl = wt * 14 + ((lane & 15) < 14 ? (lane & 15) : 0);
  (void)poll_granule<8>(wsb, GH_OFF + ((mb >> 1) * 16 + (ftl >> 2)) * 128 + (ftl & 3) * 8, tag);
  STAMP(5);
  u32x4 hb[HPRE][4], wo[16];
  const unsigned hbase = HF_OFF + (mb * 2 * 56 + wt * 14) * 2048;
  auto H = [&](int f) {
#pragma unroll
    for (int e = 0; e < 4; ++e) hb[f % HPRE][e] = ld16c(rsH, lo + (e & 1) * 1024, hbase + ((e >> 1) * 56 + fr(f)) * 2048);
  };
#pragma unroll
  for (int f = 0; f < (HPRE > W2S ? HPRE : W2S); ++f) {
    if (f >= DEARLY && f < W2S) D(f);
    if (f < HPRE) H(f);
    SBAR(); wait_vm<FTHR>(); __builtin_amdgcn_s_sleep(PACE); SBAR();
  }
  if (DEARLY < W2S) drain();
  const int q4 = (lane & 15) >> 2;
  unsigned woff[2];
#pragma unroll
  for (int nt = 0; nt < 2; ++nt) woff[nt] = (unsigned)(L_W2 + wt * (W2S * 4096) + (4 * h + q4) * 128 + ((nt ^ (q4 >> 1)) * 64) + ((lane >> 4) & 1) * 32 + (lane & 3) * 8);
#pragma unroll
  for (int i = 0; i < 4; ++i) acc[i] = f32x16{};
  STAMP(6);
  __builtin_amdgcn_s_setprio(0);
  bf16x8 wf[2][2][2];
  auto RW = [&](int f) {
#pragma unroll
    for (int s = 0; s < 2; ++s)
#pragma unroll
      for (int nt = 0; nt < 2; ++nt) wf[f & 1][s][nt] = tr_frag(lds + woff[nt] + (f % W2S) * 4096 + s * 2048, 1024);
  };
  RW(0);
  static_for<0, 14>([&](auto fc) {
    constexpr int f = decltype(fc)::value;
    if constexpr (f == 14 - HPRE) {
      PINV(acc[0]); PINV(acc[1]); PINV(acc[2]); PINV(acc[3]);
      (void)poll_granule<8>(wsb, GW_OFF + ((nb * 2 + ((lane >> 2) & 1)) * 16 + wt * 4 + (lane & 3)) * 8, tag);
    }
    if constexpr (f == NPRE) {
      PINV(acc[0]); PINV(acc[1]); PINV(acc[2]); PINV(acc[3]);
      STAMP(11);
      (void)wait_lds_word<2>(lds + L_CTRL + 20, TRIG);
    }
    asm volatile("s_waitcnt lgkmcnt(0)" ::: "memory");
    SBAR();
    if constexpr (f + 1 < 14) {
      if constexpr (f + 1 >= W2S) wait_vm<ffn2_younger(f + 1, f)>();
      RW(f + 1);
    }
    SBAR();
#pragma unroll
    for (int u = 0; u < 8; ++u) {
      const int s = u >> 2, nt = (u >> 1) & 1, mt = u & 1;
      acc[nt * 2 + mt] = MFMA(wf[f & 1][s][nt], as_frag(hb[f % HPRE][mt * 2 + s]), acc[nt * 2 + mt]);
      SBAR();
      if (u < 4) {
        if constexpr (f + W2S < 14) glds16(rsW2, ring0 + (f % W2S) * 4096 + u * 1024, w2src, (fr(f + W2S) * 32 + u * 8) * DM * 2);
      } else {
        const int e = u == 4 ? 0 : u == 5 ? 2 : u == 6 ? 1 : 3;
        if constexpr (f + HPRE < 14) hb[f % HPRE][e] = ld16c(rsH, lo + (e & 1) * 1024, hbase + ((e >> 1) * 56 + fr(f + HPRE)) * 2048);
        else if constexpr (f + HPRE < 18) {
          const int x = (f + HPRE - 14) * 4 + u - 4;
          wo[x] = ld16c(rsWs, lo, WOF_OFF + ((nb * 2 + (x >> 3)) * 16 + wt * 4) * 2048 + (x & 7) * 1024);
        }
      }
      SBAR();
    }
    if constexpr (f < NPRE) { wait_vm<FTHR>(); __builtin_amdgcn_s_sleep(PACE2); }
    SBAR();
  });
  STAMP(7);
  __builtin_amdgcn_s_setprio(3);
#pragma unroll
  for (int i = 0; i < 16; ++i) { acc[0][i] *= og0; acc[1][i] *= og1; acc[2][i] *= og0; acc[3][i] *= og1; }
  __builtin_amdgcn_s_sleep(ODELAY);
  (void)poll_granule<POLL_A>(wsb, GO_OFF + ((mb * 2 + ((lane >> 2) & 1)) * 16 + wt * 4 + (lane & 3)) * 128, tag);
  STAMP(8);
  u32x4 of[16];
#pragma unroll
  for (int e = 0; e < 8; ++e)
#pragma unroll
    for (int mt = 0; mt < 2; ++mt) of[mt * 8 + e] = ld16c(rsWs, lo, OF_OFF + ((mb * 2 + mt) * 16 + wt * 4) * 2048 + e * 1024);
  SBAR();
#pragma unroll
  for (int e = 0; e < 8; ++e)
#pragma unroll
    for (int nt = 0; nt < 2; ++nt)
#pragma unroll
      for (int mt = 0; mt < 2; ++mt) acc[nt * 2 + mt] = MFMA(as_frag(wo[nt * 8 + e]), as_frag(of[mt * 8 + e]), acc[nt * 2 + mt]);
#pragma unroll
  for (int t = 0; t < 4; ++t) st_tile(lds + L_W2 + wt * (W2S * 4096) + t * 4096 + lo, acc[t]);
  STAMP(9);
  bar_lds();
  f32x16 a = f32x16{};
#pragma unroll
  for (int f = 0; f < 4; ++f) add_tile(a, lds + L_W2 + f * (W2S * 4096) + wt * 4096 + lo);
  unsigned short* dst = P.out + (size_t)((mb * 2 + (wt & 1)) * 32 + r) * DM + (nb * 2 + (wt >> 1)) * 32 + 8 * h;
#pragma unroll
  for (int k = 0; k < 2; ++k) {
    const auto x = __builtin_amdgcn_permlane32_swap(cvtpk(a[8 * k], a[8 * k + 1]), cvtpk(a[8 * k + 4], a[8 * k + 5]), false, false);
    const auto y = __builtin_amdgcn_permlane32_swap(cvtpk(a[8 * k + 2], a[8 * k + 3]), cvtpk(a[8 * k + 6], a[8 * k + 7]), false, false);
    __builtin_amdgcn_raw_buffer_store_b128(u32x4{x[0], y[0], x[1], y[1]}, mk_rsrc(P.out, SEQ * DM * 2), (unsigned)((dst - P.out + 16 * k) * 2), 0, OAUX);
  }
  STAMP(10);
  STAMP_FLUSH(P, 0);
}

extern __shared__ __attribute__((aligned(16))) unsigned char g_lds[];

__global__ __launch_bounds__(512, 2) void k_fused(Params P) {
  u64 pc;
  asm volatile("s_getpc_b64 %0" : "=s"(pc));
  const unsigned code_word = __builtin_amdgcn_raw_buffer_load_b32(mk_rsrc((const void*)(pc & ~63ull), CODE_PF),
      (threadIdx.x & 256) * 4096 + (((blockIdx.x >> 3) * 32 + ((threadIdx.x >> 3) & 24) + (threadIdx.x & 7)) * 64), 0, 0);
  SBAR();
  LAS unsigned char* lds = (LAS unsigned char*)g_lds;
  const int w = __builtin_amdgcn_readfirstlane(threadIdx.x >> 6);
  if (w < 4) role_a(P, lds, w, code_word);
  else role_f(P, lds, w - 4);
}

extern "C" void kernel_launch(void* const* d_in, const int* in_sizes, int n_in, void* d_out, int out_size, void* d_ws,
                              size_t ws_size, hipStream_t stream) {
  Params P;
  P.x = (const unsigned short*)d_in[0];
  P.wqkv = (const unsigned short*)d_in[1];
  P.wo = (const unsigned short*)d_in[2];
  P.w1 = (const unsigned short*)d_in[4];
  P.w2 = (const unsigned short*)d_in[5];
  P.gw = (const unsigned short*)d_in[6];
  P.out = (unsigned short*)d_out;
  P.ws = (unsigned char*)d_ws;
  (void)hipFuncSetAttribute((const void*)k_fused, hipFuncAttributeMaxDynamicSharedMemorySize, LDS_BYTES);
  k_fused<<<256, 512, LDS_BYTES, stream>>>(P);
}
```

```cpp
#define ROT1(rb,j) ((((rb) + (j)) % 3) * 2)
#define MPF 2
#include <hip/hip_runtime.h>
#include <hip/hip_bf16.h>
#include <stdint.h>

typedef short bf16x8 __attribute__((ext_vector_type(8)));
typedef short s16x4 __attribute__((ext_vector_type(4)));
typedef float f32x16 __attribute__((ext_vector_type(16)));
typedef float f32x4 __attribute__((ext_vector_type(4)));
typedef unsigned u32x4 __attribute__((ext_vector_type(4)));
typedef unsigned u32x2 __attribute__((ext_vector_type(2)));
#define LAS __attribute__((address_space(3)))
#define DI __device__ __forceinline__
#define MFMA(a, b, c) __builtin_amdgcn_mfma_f32_32x32x16_bf16((a), (b), (c), 0, 0, 0)
#define SBAR() __builtin_amdgcn_sched_barrier(0)

constexpr int SEQ = 2048, DM = 448, NH = 16, DH = 28, FF = 1792;
constexpr unsigned FL_OFF = 0;
#define SETB 1u
constexpr unsigned SET_BASE = SETB << 20, SET_STRIDE = 24u << 20;
#define GQS 16u
constexpr unsigned GQW = GQS == 16u ? 128u : 4u * GQS;
constexpr unsigned GQ_OFF = 0;
constexpr unsigned GH_OFF = GQ_OFF + 256 * GQW;
constexpr unsigned GO_OFF = GH_OFF + 256 * 128;
constexpr unsigned GW_OFF = GO_OFF + 1024 * 128;
constexpr unsigned GG_OFF = GW_OFF + 256 * 8;
constexpr unsigned QF_OFF = 524288;
constexpr unsigned KV_OFF = QF_OFF + 16 * 64 * 2048;
constexpr unsigned HF_OFF = KV_OFF + 16 * 64 * 4096;
constexpr unsigned WOF_OFF = HF_OFF + 64 * 56 * 2048;
constexpr unsigned OF_OFF = WOF_OFF + 14 * 16 * 2048;
constexpr unsigned WS_END = OF_OFF + 64 * 16 * 2048;
static_assert(GG_OFF + 2048 * 8 <= QF_OFF && WS_END <= SET_STRIDE, "workspace map");

constexpr int L_X = 0;
constexpr int L_WQ = 114688;
constexpr int L_W1 = 114688;
constexpr int L_CTRL = 163776;
constexpr int L_W2 = 0;
constexpr int L_CMB = 98304;
constexpr int LDS_BYTES = 163840;

constexpr float QSCALE = 0.18898223650461362f * 1.4426950408889634f;
constexpr float LOG2E = 1.4426950408889634f;
constexpr float SAFE_BOUND = 60.0f;
constexpr unsigned SPIN_CAP = 1u << 18;
#define KVD 4
#define ROTA 0
#define ROT2 0
#define PDELAY 16
#define ODELAY 28
#define FDELAY 18
#define CODE_PF 44032
#define POLL_A 4

struct Params {
  const unsigned short* x;
  const unsigned short* wqkv;
  const unsigned short* wo;
  const unsigned short* w1;
  const unsigned short* w2;
  const unsigned short* gw;
  unsigned short* out;
  unsigned char* ws;
};
typedef unsigned long long u64;
typedef __attribute__((address_space(1))) u64 gu64;

DI unsigned cvtpk(float lo, float hi) {
  typedef __bf16 bf2 __attribute__((ext_vector_type(2)));
  typedef float f2 __attribute__((ext_vector_type(2)));
  f2 v = {lo, hi};
  bf2 r = __builtin_convertvector(v, bf2);
  return __builtin_bit_cast(unsigned, r);
}
DI bf16x8 pack8(const f32x16& x, int s) {
  u32x4 p;
  p[0] = cvtpk(x[8 * s + 0], x[8 * s + 1]);
  p[1] = cvtpk(x[8 * s + 2], x[8 * s + 3]);
  p[2] = cvtpk(x[8 * s + 4], x[8 * s + 5]);
  p[3] = cvtpk(x[8 * s + 6], x[8 * s + 7]);
  return __builtin_bit_cast(bf16x8, p);
}
DI float swap_half(float v) {
  auto r = __builtin_amdgcn_permlane32_swap(__float_as_uint(v), __float_as_uint(v), false, false);
  return (threadIdx.x & 32) ? __uint_as_float(r[0]) : __uint_as_float(r[1]);
}
template <int CTRL> DI float dpp_mov(float v) {
  return __uint_as_float(__builtin_amdgcn_update_dpp(__float_as_uint(v), __float_as_uint(v), CTRL, 0xf, 0xf, false));
}
DI float row16_max(float v) {
  v = fmaxf(v, dpp_mov<0x128>(v));
  v = fmaxf(v, dpp_mov<0x124>(v));
  v = fmaxf(v, dpp_mov<0x122>(v));
  v = fmaxf(v, dpp_mov<0x121>(v));
  return v;
}
DI unsigned row16_umax(unsigned v) {
  v = max(v, (unsigned)__builtin_amdgcn_update_dpp(v, v, 0x128, 0xf, 0xf, false));
  v = max(v, (unsigned)__builtin_amdgcn_update_dpp(v, v, 0x124, 0xf, 0xf, false));
  v = max(v, (unsigned)__builtin_amdgcn_update_dpp(v, v, 0x122, 0xf, 0xf, false));
  v = max(v, (unsigned)__builtin_amdgcn_update_dpp(v, v, 0x121, 0xf, 0xf, false));
  return v;
}
DI float rdlane(float v, int l) { return __uint_as_float(__builtin_amdgcn_readlane(__float_as_uint(v), l)); }
DI s16x4 tr_read(const LAS unsigned char* p) {
  typedef short v4i16_t __attribute__((ext_vector_type(4)));
  return __builtin_bit_cast(s16x4, __builtin_amdgcn_ds_read_tr16_b64_v4i16((LAS v4i16_t*)p));
}
DI bf16x8 tr_frag(const LAS unsigned char* p, int row_stride4) {
  s16x4 lo = tr_read(p), hi = tr_read(p + row_stride4);
  return __builtin_shufflevector(lo, hi, 0, 1, 2, 3, 4, 5, 6, 7);
}
DI __amdgpu_buffer_rsrc_t mk_rsrc(const void* p, unsigned bytes) {
  return __builtin_amdgcn_make_buffer_rsrc((void*)p, (short)0, (int)bytes, 0x00020000);
}
DI void glds16(__amdgpu_buffer_rsrc_t rs, unsigned dst, unsigned voff, unsigned soff) {
  asm volatile("s_mov_b32 m0, %0\n\ts_nop 0\n\tbuffer_load_dwordx4 %1, %2, %3 offen lds" ::"s"(dst), "v"(voff), "s"(rs), "s"(soff) : "memory");
}
DI u32x4 ld16(__amdgpu_buffer_rsrc_t rs, unsigned voff, unsigned soff) { return __builtin_amdgcn_raw_buffer_load_b128(rs, voff, soff, 0); }
DI u32x4 ld16c(__amdgpu_buffer_rsrc_t rs, unsigned voff, unsigned soff) { return __builtin_amdgcn_raw_buffer_load_b128(rs, voff, soff, 16); }
DI void st16(__amdgpu_buffer_rsrc_t rs, u32x4 v, unsigned voff, unsigned soff) { __builtin_amdgcn_raw_buffer_store_b128(v, rs, voff + soff, 0, 16); }
DI bf16x8 as_frag(u32x4 v) { return __builtin_bit_cast(bf16x8, v); }
DI u32x4 as_u4(bf16x8 v) { return __builtin_bit_cast(u32x4, v); }
template <int N> DI void wait_bar() { asm volatile("s_waitcnt vmcnt(%0) lgkmcnt(0)\n\ts_barrier" ::"n"(N) : "memory"); }

template <int V> struct IC { static constexpr int value = V; };
template <int I, int N, class Fn> DI void static_for(Fn&& fn) {
  if constexpr (I < N) { fn(IC<I>{}); static_for<I + 1, N>(fn); }
}
DI void drain() { asm volatile("s_waitcnt vmcnt(0)" ::: "memory"); }
template <int N> DI void wait_vm() { asm volatile("s_waitcnt vmcnt(%0)" ::"n"(N) : "memory"); }
DI void put_granule(unsigned char* ws, unsigned off, u64 tag16, unsigned payload) {
  __hip_atomic_store((gu64*)(ws + off), tag16 | (u64)(payload & 0xffffu), __ATOMIC_RELAXED, __HIP_MEMORY_SCOPE_AGENT);
}
template <int SLEEP> DI unsigned poll_granule(unsigned char* ws, unsigned off, u64 tag16) {
  u64 v = ~tag16;
  for (unsigned spins = 0;; ++spins) {
    if (v - tag16 > 0xffffull) v = __hip_atomic_load((gu64*)(ws + off), __ATOMIC_RELAXED, __HIP_MEMORY_SCOPE_AGENT);
    if (__all(v - tag16 <= 0xffffull) || spins > SPIN_CAP) break;
    __builtin_amdgcn_s_sleep(SLEEP);
  }
  __builtin_amdgcn_fence(__ATOMIC_ACQUIRE, "wavefront");
  return (unsigned)v & 0xffffu;
}
DI unsigned wait_lds_word(LAS unsigned char* p, unsigned want) {
  unsigned v = 0;
#pragma nounroll
  for (unsigned spins = 0; spins < (1u << 24); ++spins) {
    v = __builtin_amdgcn_readfirstlane(__hip_atomic_load((LAS unsigned*)p, __ATOMIC_RELAXED, __HIP_MEMORY_SCOPE_WORKGROUP));
    if (v >= want) break;
    __builtin_amdgcn_s_sleep(1);
  }
  asm volatile("" ::: "memory");
  return v;
}
template <int N> DI u64 wait_bar_epoch(const unsigned char* p) {
  u64 ep;
  asm volatile("s_load_dwordx2 %0, %1, 0x0\n\ts_waitcnt vmcnt(%2) lgkmcnt(0)\n\ts_barrier" : "=&s"(ep) : "s"(p), "n"(N) : "memory");
  return ep;
}
DI void bar_lds() { asm volatile("s_waitcnt lgkmcnt(0)\n\ts_barrier" ::: "memory"); }
DI void st_tile(LAS unsigned char* p, const f32x16& a) {
#pragma unroll
  for (int g = 0; g < 4; ++g) *(LAS f32x4*)(p + g * 1024) = f32x4{a[4 * g], a[4 * g + 1], a[4 * g + 2], a[4 * g + 3]};
}
DI void add_tile(f32x16& a, const LAS unsigned char* p) {
#pragma unroll
  for (int g = 0; g < 4; ++g) {
    const f32x4 v = *(const LAS f32x4*)(p + g * 1024);
    a[4 * g] += v[0]; a[4 * g + 1] += v[1]; a[4 * g + 2] += v[2]; a[4 * g + 3] += v[3];
  }
}

#define STAMP_DECL do {} while (0)
#define STAMP(id) do {} while (0)
#define QSTAMP(id) do {} while (0)
#define RSTAMP(id) do {} while (0)
#define STAMP_FLUSH(P, ph) do {} while (0)

DI void lds_arrive(LAS unsigned char* p) {
  asm volatile("s_mov_b64 exec, 1\n\tds_add_u32 %0, %1\n\ts_mov_b64 exec, -1" ::"v"((unsigned)(uintptr_t)p), "v"(1u) : "memory");
}

#define PINV(x) asm volatile("" : "+v"(x))
#define PINS(x) asm volatile("" : "+s"(x))
DI float sumsq(const u32x4& a, const u32x4& b) {
  typedef __bf16 bf2 __attribute__((ext_vector_type(2)));
  float n0 = 0.f, n1 = 0.f;
#pragma unroll
  for (int i = 0; i < 4; ++i) {
    n0 = __builtin_amdgcn_fdot2_f32_bf16(__builtin_bit_cast(bf2, a[i]), __builtin_bit_cast(bf2, a[i]), n0, false);
    n1 = __builtin_amdgcn_fdot2_f32_bf16(__builtin_bit_cast(bf2, b[i]), __builtin_bit_cast(bf2, b[i]), n1, false);
  }
  return n0 + n1;
}

DI void role_a(const Params& P, LAS unsigned char* lds, const int wt, const unsigned code_word) {
  const int lane = threadIdx.x & 63, r = lane & 31, h = lane >> 5;
  const int b = blockIdx.x, xcd = b & 7, idx = b >> 3;
  const unsigned lo = lane * 16;
  STAMP_DECL;
  STAMP(0);
  __builtin_amdgcn_s_setprio(2);
  f32x16 acc[4];
  const int rb = (xcd & 1) * 8 + (idx & 7), j = (xcd >> 1) * 4 + (idx >> 3);
  const int head = xcd * 2 + (idx & 1), qb = idx >> 1;
  unsigned sto[3];
#pragma unroll
  for (int ct = 0; ct < 3; ++ct) {
    const int u = 3 * j + ct, part = u >> 4, t = (u & 15) * 64 + rb * 4 + wt;
    sto[ct] = lo + (part == 0 ? QF_OFF + t * 2048 : KV_OFF + t * 4096 + (part - 1) * 2048);
    PINV(sto[ct]);
  }
  unsigned padv = r == DH ? 0x3f803f80u : 0u;
  PINV(padv);
  const int gq = head / 3, gk = (16 + head) / 3, gvv = (32 + head) / 3;
  unsigned pub_off = GQ_OFF + (j * 16 + rb) * GQW + wt * GQS + (lane & 1) * 8;
  unsigned poll_off = GQ_OFF + (h ? gq * 16 + qb : (lane < 16 ? gk : gvv) * 16 + wt * 4 + ((lane >> 2) & 3)) * GQW + (lane & 3) * GQS + h * 8;
  unsigned chk_off = GQ_OFF + (gk * 16 + (lane >> 2)) * GQW + (lane & 3) * GQS;
  unsigned g_off = GG_OFF + ((qb * 4 + wt) * 32 + r) * 8;
  unsigned o_off = lo + OF_OFF + ((qb * 4 + wt) * 16 + head) * 2048;
  PINV(pub_off); PINV(poll_off); PINV(chk_off); PINV(g_off); PINV(o_off);
  unsigned kvbase = KV_OFF + (head * 64 + wt * 16) * 4096, go_off = GO_OFF + ((qb * 4 + wt) * 16 + head) * 128;
  unsigned qofs[4];
#pragma unroll
  for (int t = 0; t < 4; ++t) { qofs[t] = QF_OFF + (unsigned)(head * 64 + qb * 4 + ((wt + 1 + t) & 3)) * 2048; PINS(qofs[t]); }
  PINS(kvbase); PINS(go_off);
  unsigned xoff[4];
#pragma unroll
  for (int ks = 0; ks < 4; ++ks) xoff[ks] = (unsigned)(L_X + wt * 4096 + r * 128 + (((2 * ks + h) ^ ((r >> 1) & 7)) << 4));
  const unsigned wqoff = (unsigned)(L_WQ + (8 * h + ((lane & 15) >> 2)) * 192 + ((lane >> 4) & 1) * 32 + (lane & 3) * 8);
  u64 ep;
  asm volatile("s_load_dwordx2 %0, %1, 0x0\n\ts_waitcnt lgkmcnt(0)" : "=&s"(ep) : "s"(P.ws + FL_OFF) : "memory");
  u64 tag16 = (ep + 1) << 16;
  unsigned char* wsb = P.ws + SET_BASE + (((unsigned)ep + 1u) & 3u) * SET_STRIDE;
  PINS(tag16); PINS(wsb);
  const __amdgpu_buffer_rsrc_t rsWs = mk_rsrc(wsb, WS_END);
#pragma unroll
  for (int i = 0; i < 3; ++i) acc[i] = f32x16{};
  unsigned wtouch = 0;
  auto kloop = [&](auto nvc) {
    constexpr int NV = decltype(nvc)::value;
    bf16x8 xf[4], wf[4][3];
    auto mm = [&](int ks, int ct) { acc[ct] = ct >= 3 - NV ? MFMA(xf[ks], wf[ks][ct], acc[ct]) : MFMA(wf[ks][ct], xf[ks], acc[ct]); };
    auto step = [&](int c, int ks, bool domm) {
      const int km = (ks + 2) & 3;
#pragma unroll
      for (int ct = 0; ct < 3; ++ct) {
        if (domm) mm(km, ct);
        SBAR();
        if (ct == 0) xf[ks] = *(const LAS bf16x8*)(lds + xoff[ks] + c * 16384);
        wf[ks][ct] = tr_frag(lds + wqoff + (c & 3) * 12288 + ks * 16 * 192 + ct * 56, 4 * 192);
        SBAR();
      }
    };
#pragma unroll
    for (int c = 0; c < 7; ++c) {
      bar_lds();
      if (c == 0) STAMP(1);
      if (c == MPF) {
        const unsigned l = (unsigned)(b * 128 + (wt & 1) * 64 + lane) * 128;
        if (wt < 2) wtouch = l < DM * FF * 2 ? __builtin_amdgcn_raw_buffer_load_b32(mk_rsrc(P.w1, DM * FF * 2), l, 0, 0)
                           : l < 2 * DM * FF * 2 ? __builtin_amdgcn_raw_buffer_load_b32(mk_rsrc(P.w2, DM * FF * 2), l - DM * FF * 2, 0, 0)
                                                 : __builtin_amdgcn_raw_buffer_load_b32(mk_rsrc(P.wo, DM * DM * 2), l - 2 * DM * FF * 2, 0, 0);
        SBAR();
      }
      QSTAMP(1 + c);
      step(c, 0, c > 0);
      step(c, 1, c > 0);
      step(c, 2, true);
      step(c, 3, true);
    }
    bar_lds();
    QSTAMP(8);
#pragma unroll
    for (int ct = 0; ct < 3; ++ct) { mm(2, ct); mm(3, ct); SBAR(); }
  };
  auto epi = [&](auto nqc, auto nvc) {
    constexpr int NQ = decltype(nqc)::value, NV = decltype(nvc)::value;
    STAMP(2);
    asm volatile("" ::"v"(code_word), "v"(wtouch));
    u32x4 t0[3], t1[3];
#pragma unroll
    for (int ct = 0; ct < 3; ++ct) {
      if (ct < NQ) acc[ct] *= QSCALE;
      t0[ct] = as_u4(pack8(acc[ct], 0));
      if (ct >= 3 - NV) {
#pragma unroll
        for (int i = 0; i < 4; ++i) t0[ct][i] = r < DH ? t0[ct][i] : padv;
      }
      st16(rsWs, t0[ct], sto[ct], 0);
      t1[ct] = as_u4(pack8(acc[ct], 1));
      if (ct >= 3 - NV) {
#pragma unroll
        for (int i = 0; i < 4; ++i) t1[ct][i] = r < DH ? t1[ct][i] : padv;
      } else {
        t1[ct][2] = h ? (ct < NQ ? 0u : 0x3f803f80u) : t1[ct][2];
        t1[ct][3] = h ? (ct < NQ ? 0u : 0x00003f80u) : t1[ct][3];
      }
      st16(rsWs, t1[ct], sto[ct], 1024);
      SBAR();
    }
    STAMP(8);
    unsigned mq = 0, mk = 0;
#pragma unroll
    for (int ct = 0; ct < 3; ++ct) {
      const unsigned n = __float_as_uint(sumsq(t0[ct], t1[ct])) & 0x7fffffffu;
      if (ct < NQ) mq = max(mq, n); else mk = max(mk, n);
    }
    auto bound = [&](unsigned m) {
      m = row16_umax(m);
      const float lo2 = __uint_as_float(max((unsigned)__builtin_amdgcn_readlane(m, 0), (unsigned)__builtin_amdgcn_readlane(m, 16)));
      const float hi2 = __uint_as_float(max((unsigned)__builtin_amdgcn_readlane(m, 32), (unsigned)__builtin_amdgcn_readlane(m, 48)));
      return ((__float_as_uint(lo2 + hi2) >> 16) + 1u) & 0xffffu;
    };
    const unsigned pq = NQ > 0 ? bound(mq) : 0u, pk = NQ < 3 ? bound(mk) : 0u;
    u64 gran = tag16 | ((lane & 1) ? pq : pk);
    PINV(gran);
    drain();
    STAMP(9);
    if (lane < 2) __hip_atomic_store((gu64*)(wsb + pub_off), gran, __ATOMIC_RELAXED, __HIP_MEMORY_SCOPE_AGENT);
  };
  if (j < 10) {
    kloop(IC<0>{});
    if (j < 5) epi(IC<3>{}, IC<0>{});
    else if (j == 5) epi(IC<1>{}, IC<0>{});
    else epi(IC<0>{}, IC<0>{});
  } else if (j == 10) {
    kloop(IC<1>{});
    epi(IC<0>{}, IC<1>{});
  } else {
    kloop(IC<3>{});
    epi(IC<0>{}, IC<3>{});
  }
  STAMP(3);
  __builtin_amdgcn_s_sleep(PDELAY);
  const unsigned pay = poll_granule<POLL_A>(wsb, poll_off, tag16);
  STAMP(4);
  u32x4 q[4][2];
  LAS unsigned char* const cmb = lds + L_CMB + wt * 12288 + lo;
  u64 gv = 0;
  unsigned thr = 0;
#pragma nounroll
  for (int pass = 0;; ++pass) {
    u32x4 kv[KVD][4];
    kv[0][0] = ld16c(rsWs, lo, kvbase);
    SBAR();
    if (pass == 0) { q[0][0] = ld16c(rsWs, lo, qofs[0]); q[0][1] = ld16c(rsWs, lo + 1024, qofs[0]); }
    SBAR();
    kv[0][1] = ld16c(rsWs, lo + 1024, kvbase);
    SBAR();
    if (pass == 0) {
#pragma unroll
      for (int t = 1; t < 4; ++t) { q[t][0] = ld16c(rsWs, lo, qofs[t]); q[t][1] = ld16c(rsWs, lo + 1024, qofs[t]); SBAR(); }
    }
#pragma unroll
    for (int e = 2; e < 4 * (KVD - 1); ++e) { kv[e >> 2][e & 3] = ld16c(rsWs, lo + (e & 3) * 1024, kvbase + (e >> 2) * 4096); SBAR(); }
#pragma unroll
    for (int i = 0; i < 4; ++i) acc[i] = f32x16{};
    {
      const float q2 = __uint_as_float(__builtin_amdgcn_readlane(row16_umax(pay), 32) << 16);
      const float lim = SAFE_BOUND * SAFE_BOUND * __builtin_amdgcn_rcpf(q2);
      thr = lim < 1e30f ? __float_as_uint(lim) >> 16 : (lim >= 1e30f ? 0x7000u : 0u);
      PINV(thr);
    }
    __builtin_amdgcn_s_setprio(1);
    f32x16 sc = MFMA(as_frag(kv[0][0]), as_frag(q[0][0]), f32x16{});
    sc = MFMA(as_frag(kv[0][1]), as_frag(q[0][1]), sc);
    STAMP(5);
    u32x4 p0, p1 = {0, 0, 0, 0};
    float e0 = 0.f, e1 = 0.f, e2 = 0.f, e3 = 0.f;
    u64 ck = 0, cv = 0;
#define EXP4(i) do { e0 = __builtin_amdgcn_exp2f(sc[i]); e1 = __builtin_amdgcn_exp2f(sc[(i) + 1]); e2 = __builtin_amdgcn_exp2f(sc[(i) + 2]); e3 = __builtin_amdgcn_exp2f(sc[(i) + 3]); \
    asm volatile("" : "+v"(e0), "+v"(e1), "+v"(e2), "+v"(e3)); SBAR(); } while (0)
#define CVT2(P, i) do { P[i] = cvtpk(e0, e1); P[(i) + 1] = cvtpk(e2, e3); asm volatile("" : "+v"(P)); SBAR(); } while (0)
#define PART(t, g) do { *(LAS f32x4*)(cmb + (t) * 4096 + (g) * 1024) = f32x4{acc[t][4 * (g)], acc[t][4 * (g) + 1], acc[t][4 * (g) + 2], acc[t][4 * (g) + 3]}; SBAR(); } while (0)
    static_for<0, 64>([&](auto ic) {
      constexpr int i = decltype(ic)::value;
      constexpr int kt = i >> 2, t = i & 3, kn_ = (i + 1) >> 2, tn = (i + 1) & 3, kp = (i - 1) >> 2, tp = (i - 1) & 3;
      constexpr int kl = kt + KVD - 1;
      if (i == 24) STAMP(11);
      if (i == 56) (void)wait_lds_word(lds + L_CTRL, 4);
      f32x16 sn;
      SBAR();
      if (i < 63) sn = MFMA(as_frag(kv[kn_ % KVD][0]), as_frag(q[tn][0]), f32x16{});
      SBAR();
      if constexpr (i >= 62) PART(i - 62, 0);
      if (i > 0) CVT2(p1, 2);
      EXP4(0);
      if (i > 0) acc[tp] = MFMA(as_frag(kv[(kp + KVD) % KVD][3]), as_frag(p1), acc[tp]);
      SBAR();
      if (kl < 16) kv[kl % KVD][t] = ld16c(rsWs, lo + t * 1024, kvbase + kl * 4096);
      if (i == 52) gv = __hip_atomic_load((gu64*)(wsb + g_off), __ATOMIC_RELAXED, __HIP_MEMORY_SCOPE_AGENT);
      if (i == 53) ck = __hip_atomic_load((gu64*)(wsb + chk_off), __ATOMIC_RELAXED, __HIP_MEMORY_SCOPE_AGENT);
      if (i == 54) cv = __hip_atomic_load((gu64*)(wsb + (gvv - gk) * 16 * GQW + chk_off), __ATOMIC_RELAXED, __HIP_MEMORY_SCOPE_AGENT);
      SBAR();
      if constexpr (i >= 62) PART(i - 62, 1);
      CVT2(p0, 0);
      EXP4(4);
      if (i < 63) sn = MFMA(as_frag(kv[kn_ % KVD][1]), as_frag(q[tn][1]), sn);
      SBAR();
      if constexpr (i >= 62) PART(i - 62, 2);
      CVT2(p0, 2);
      EXP4(8);
      acc[t] = MFMA(as_frag(kv[kt % KVD][2]), as_frag(p0), acc[t]);
      SBAR();
      if constexpr (i >= 62) PART(i - 62, 3);
      CVT2(p1, 0);
      EXP4(12);
      if (i < 63) sc = sn;
    });
    CVT2(p1, 2);
    acc[3] = MFMA(as_frag(kv[15 % KVD][3]), as_frag(p1), acc[3]);
    SBAR();
    PART(2, 0); PART(2, 1); PART(2, 2); PART(2, 3);
#undef EXP4
#undef CVT2
#undef PART
    __builtin_amdgcn_s_setprio(2);
    if (__builtin_expect(__all(ck - tag16 <= (u64)thr && cv - tag16 <= 0x7000ull && gv - tag16 <= 0xffffull), 1)) break;
    gv = poll_granule<2>(wsb, g_off, tag16);
    if (pass) break;
    const unsigned pk = poll_granule<2>(wsb, chk_off, tag16), pv = poll_granule<2>(wsb, (gvv - gk) * 16 * GQW + chk_off, tag16);
    if (__all(pk <= thr && pv <= 0x7000u)) break;
    float mx[4];
#pragma unroll
    for (int t = 0; t < 4; ++t) mx[t] = -INFINITY;
    for (int kt = 0; kt < 64; ++kt) {
      const u32x4 k0 = ld16c(rsWs, lo, KV_OFF + (head * 64 + kt) * 4096), k1 = ld16c(rsWs, lo + 1024, KV_OFF + (head * 64 + kt) * 4096);
#pragma unroll
      for (int t = 0; t < 4; ++t) {
        f32x16 s = MFMA(as_frag(k0), as_frag(q[t][0]), f32x16{});
        s = MFMA(as_frag(k1), as_frag(q[t][1]), s);
#pragma unroll
        for (int i = 0; i < 16; ++i) mx[t] = fmaxf(mx[t], s[i]);
      }
    }
#pragma unroll
    for (int t = 0; t < 4; ++t) {
      const float nm = -fmaxf(mx[t], swap_half(mx[t]));
      const unsigned hi = __float_as_uint(nm) & 0xffff0000u;
      const float r1 = nm - __uint_as_float(hi);
      const unsigned mid = __float_as_uint(r1) & 0xffff0000u;
      const float r2 = r1 - __uint_as_float(mid);
      if (h) { q[t][1][2] = (hi >> 16) | mid; q[t][1][3] = __float_as_uint(r2) >> 16; }
    }
  }
  STAMP(6);
  asm volatile("s_waitcnt lgkmcnt(0)" ::: "memory");
  lds_arrive(lds + L_CTRL + 20);
  (void)wait_lds_word(lds + L_CTRL + 20, 4);
  f32x16 o = acc[3];
#pragma unroll
  for (int u = 0; u < 3; ++u) add_tile(o, lds + L_CMB + ((wt + 3 - u) & 3) * 12288 + u * 4096 + lo);
  const auto lsw = __builtin_amdgcn_permlane32_swap(__float_as_uint(o[12]), __float_as_uint(o[12]), false, false);
  const float inv = __uint_as_float((unsigned)gv << 16) * __builtin_amdgcn_rcpf(__uint_as_float(lsw[1]));
  o *= inv;
  st16(rsWs, as_u4(pack8(o, 0)), o_off, 0);
  u32x4 o1 = as_u4(pack8(o, 1));
  o1[2] = h ? 0u : o1[2];
  o1[3] = h ? 0u : o1[3];
  st16(rsWs, o1, o_off, 1024);
  STAMP(7);
  drain();
  if (lane == 0) __hip_atomic_store((gu64*)(wsb + go_off), tag16, __ATOMIC_RELAXED, __HIP_MEMORY_SCOPE_AGENT);
  STAMP(10);
  STAMP_FLUSH(P, 0);
}

constexpr int W2S = 6;
constexpr int HPRE = 5;
#define LEA 5
#define LEB 11
#define LE0 16
#define LU0 3
#define F1LA 2
#define PACE 4
#define PACE2 2
constexpr int ffn2_ops(int f) { return (f + W2S < 14 ? 4 : 0) + 4; }
constexpr int ffn2_younger(int f, int upto) {
  int n = 4;
  for (int i = f - W2S + 1; i < upto; ++i) n += ffn2_ops(i);
  return n;
}

DI void role_f(const Params& P, LAS unsigned char* lds, const int wt) {
  u64 ep;
  const int lane = threadIdx.x & 63, r = lane & 31, h = lane >> 5;
  const int b = blockIdx.x, xcd = b & 7, idx = b >> 3;
  const int rb = (xcd & 1) * 8 + (idx & 7), j = (xcd >> 1) * 4 + (idx >> 3);
  const unsigned lo = lane * 16, lds0 = (unsigned)(uintptr_t)lds;
  const bool ffn = j < 14;
  STAMP_DECL;
  STAMP(0);
  f32x16 acc[4];
  u32x4 wl[28];
  const __amdgpu_buffer_rsrc_t rsL = mk_rsrc(ffn ? P.w1 : P.gw, ffn ? DM * FF * 2 : DM * 2);
  const unsigned lsrc = ffn ? (unsigned)((lane >> 2) * (FF * 2) + (j * 4 + wt) * 64 + (lane & 3) * 16) : (unsigned)(h * 16);
  const unsigned lstep = ffn ? 16 * FF * 2 : 32;
  const int rot = ROT1(rb, j);
  auto kc = [&](int c) { const int v = c + rot; return v >= 7 ? v - 7 : v; };
  auto L = [&](int u0, int u1) {
#pragma unroll
    for (int u = u0; u < u1; ++u) wl[u] = ld16(rsL, lsrc, (kc(u >> 2) * 4 + (u & 3)) * lstep);
    SBAR();
  };
  {
    const __amdgpu_buffer_rsrc_t rsX = mk_rsrc(P.x, SEQ * DM * 2);
    const __amdgpu_buffer_rsrc_t rsW = mk_rsrc(P.wqkv, DM * 3 * DM * 2);
    unsigned xsrc[2], wsrc[3];
#pragma unroll
    for (int i = 0; i < 2; ++i) {
      const int row = 32 * wt + 8 * i + (lane >> 3), ch = (lane & 7) ^ ((row >> 1) & 7);
      xsrc[i] = (unsigned)((rb * 128 + row) * (DM * 2) + ch * 16);
    }
#pragma unroll
    for (int i = 0; i < 3; ++i) {
      const int p = 64 * (3 * wt + i) + lane, kr = p / 12, ch = p % 12;
      wsrc[i] = (unsigned)(kr * (3 * DM * 2) + j * 168 + (ch < 11 ? ch : 10) * 16);
    }
    auto X = [&](int c) {
#pragma unroll
      for (int i = 0; i < 4; ++i) glds16(rsX, lds0 + L_X + c * 16384 + (4 * wt + i) * 1024, xsrc[i & 1], kc(c) * 128 + (i >> 1) * 16 * DM * 2);
    };
    auto W = [&](int c) {
#pragma unroll
      for (int i = 0; i < 3; ++i) glds16(rsW, lds0 + L_WQ + (c & 3) * 12288 + (3 * wt + i) * 1024, wsrc[i], kc(c) * 64 * 3 * DM * 2);
    };
    X(0); W(0); X(1); W(1); X(2);
    QSTAMP(1);
    wait_bar<11>();
    STAMP(1);
    W(2); X(3); W(3);
    QSTAMP(2);
    wait_bar<14>();
    X(4); W(4);
    QSTAMP(3);
    wait_bar<14>();
    X(5); W(5);
    QSTAMP(4);
    wait_bar<14>();
    X(6); W(6);
    QSTAMP(5);
    wait_bar<14>();
    if (wt == 0 && lane < 8) *(LAS unsigned*)(lds + L_CTRL + lane * 4) = 0u;
    L(0, LEA);
    QSTAMP(6);
    ep = wait_bar_epoch<7 + LEA>(P.ws + FL_OFF);
    L(LEA, LEB);
    QSTAMP(7);
    wait_bar<LEB>();
    L(LEB, LE0);
    QSTAMP(8);
    bar_lds();
    __builtin_amdgcn_s_sleep(FDELAY);
  }
  STAMP(2);
  const u64 tag = (ep + 1) << 16;
  unsigned char* const wsb = P.ws + SET_BASE + (((unsigned)ep + 1u) & 3u) * SET_STRIDE;
  const __amdgpu_buffer_rsrc_t rsWs = mk_rsrc(wsb, WS_END);
  unsigned xoff[4];
#pragma unroll
  for (int ks = 0; ks < 4; ++ks) xoff[ks] = (unsigned)(L_X + r * 128 + (((2 * ks + h) ^ ((r >> 1) & 7)) << 4));
  if (ffn) {
    LAS unsigned char* ring = lds + L_W1 + wt * 8192;
    const unsigned troff = (unsigned)((8 * h + ((lane & 15) >> 2)) * 64 + ((lane >> 4) & 1) * 32 + (lane & 3) * 8);
#pragma unroll
    for (int i = 0; i < 4; ++i) acc[i] = f32x16{};
    RSTAMP(1);
    constexpr int LA = F1LA;
    bf16x8 xf[LA + 1][4], wf[LA + 1];
    auto wr = [&](int u) { *(LAS u32x4*)(ring + (u & 7) * 1024 + lo) = wl[u]; };
    auto rdw = [&](int u) { wf[u % (LA + 1)] = tr_frag(ring + (u & 7) * 1024 + troff, 4 * 64); };
    auto rdx = [&](int u, int t) { xf[u % (LA + 1)][t] = *(const LAS bf16x8*)(lds + xoff[u & 3] + (u >> 2) * 16384 + t * 4096); };
#pragma unroll
    for (int u = 0; u < LA + 2; ++u) wr(u);
#pragma unroll
    for (int u = 0; u < LA; ++u) {
      rdw(u);
#pragma unroll
      for (int t = 0; t < 4; ++t) rdx(u, t);
    }
    SBAR();
#pragma unroll
    for (int u = 0; u < 28; ++u) {
      if ((u & 3) == 0) RSTAMP(2 + (u >> 2));
#pragma unroll
      for (int t = 0; t < 4; ++t) {
        if (u + LA < 28) {
          if (t == 1) rdw(u + LA);
          rdx(u + LA, t);
          if (t == 3 && u + LA + 2 < 28) wr(u + LA + 2);
        }
        SBAR();
        acc[t] = MFMA(wf[u % (LA + 1)], xf[u % (LA + 1)][t], acc[t]);
        SBAR();
        if (u >= LU0 && LE0 + (u - LU0) * 4 + t < 28) L(LE0 + (u - LU0) * 4 + t, LE0 + (u - LU0) * 4 + t + 1);
      }
    }
    lds_arrive(lds + L_CTRL);
    RSTAMP(9);
    STAMP(3);
    __builtin_amdgcn_s_setprio(3);
#pragma unroll
    for (int t = 0; t < 4; ++t) {
      f32x16 z = acc[t];
#pragma unroll
      for (int i = 0; i < 16; ++i) z[i] = z[i] * __builtin_amdgcn_rcpf(1.f + __builtin_amdgcn_exp2f(-LOG2E * z[i]));
      const unsigned so = HF_OFF + ((rb * 4 + t) * 56 + j * 4 + wt) * 2048;
      st16(rsWs, as_u4(pack8(z, 0)), lo, so);
      st16(rsWs, as_u4(pack8(z, 1)), lo + 1024, so);
    }
    drain();
    if (lane == 0) put_granule(wsb, GH_OFF + (rb * 16 + j) * 128 + wt * 8, tag, 0);
  } else {
    __builtin_amdgcn_s_setprio(3);
    if (j == 14) {
      L(LE0, 28);
      acc[0] = f32x16{};
#pragma unroll
      for (int u = 0; u < 28; ++u) {
        const bf16x8 xf = *(const LAS bf16x8*)(lds + xoff[u & 3] + (u >> 2) * 16384 + wt * 4096);
        acc[0] = MFMA(as_frag(wl[u]), xf, acc[0]);
      }
      const float g = __builtin_amdgcn_rcpf(1.f + __builtin_amdgcn_exp2f(-LOG2E * acc[0][0]));
      if (h == 0) put_granule(wsb, GG_OFF + ((rb * 4 + wt) * 32 + r) * 8, tag, cvtpk(g, 0.f));
    }
    lds_arrive(lds + L_CTRL);
    const __amdgpu_buffer_rsrc_t rsO = mk_rsrc(P.wo, DM * DM * 2);
    const int id0 = ((j - 14) * 16 + rb) * 4 + wt;
    for (int id = id0; id < 224; id += 128) {
      const int nt = id >> 4, hd = id & 15;
      unsigned rp[16];
#pragma unroll
      for (int e = 0; e < 16; ++e)
        rp[e] = __builtin_amdgcn_raw_buffer_load_b16(rsO, (unsigned)(((4 * h + 8 * (e >> 2) + (e & 3)) * DM + r) * 2), (unsigned)((DH * hd * DM + 32 * nt) * 2), 0);
      u32x4 a, c2;
#pragma unroll
      for (int i = 0; i < 4; ++i) {
        a[i] = (rp[2 * i] & 0xffffu) | (rp[2 * i + 1] << 16);
        c2[i] = (rp[8 + 2 * i] & 0xffffu) | (rp[8 + 2 * i + 1] << 16);
      }
      c2[2] = h ? 0u : c2[2];
      c2[3] = h ? 0u : c2[3];
      st16(rsWs, a, lo, WOF_OFF + id * 2048);
      st16(rsWs, c2, lo + 1024, WOF_OFF + id * 2048);
      drain();
      if (lane == 0) put_granule(wsb, GW_OFF + id * 8, tag, 0);
    }
  }
  STAMP(4);
  if (idx >= 28) {
    if (b == 255 && wt == 0) {
      __builtin_amdgcn_s_sleep(127);
#pragma unroll
      for (int i = 0; i < 4; ++i) (void)poll_granule<64>(wsb, GQ_OFF + (i * 64 + lane) * GQW, tag);
      if (lane == 0) __hip_atomic_store((gu64*)(P.ws + FL_OFF), ep + 1, __ATOMIC_RELAXED, __HIP_MEMORY_SCOPE_AGENT);
    }
    STAMP(10);
    STAMP_FLUSH(P, 0);
    return;
  }
  const int mb = 4 * xcd + (idx & 3), nb = idx >> 2;
  const __amdgpu_buffer_rsrc_t rsW2 = mk_rsrc(P.w2, FF * DM * 2), rsH = rsWs;
  const unsigned w2src = (unsigned)((wt * 448 + (lane >> 3)) * (DM * 2) + nb * 128 + ((((lane >> 2) & 1) ^ ((lane >> 4) & 1)) * 64) + (lane & 3) * 16);
  const unsigned ring0 = lds0 + L_W2 + wt * 24576;
  const int rot2 = ROT2;
  auto fr = [&](int f) { const int v = f + rot2; return v >= 14 ? v - 14 : v; };
  auto D = [&](int f) {
#pragma unroll
    for (int i = 0; i < 4; ++i) glds16(rsW2, ring0 + (f % W2S) * 4096 + i * 1024, w2src, (fr(f) * 32 + i * 8) * DM * 2);
  };
  (void)wait_lds_word(lds + L_CTRL, 4);
#pragma unroll
  for (int f = 0; f < W2S; ++f) { D(f); __builtin_amdgcn_s_sleep(PACE); }
  const int ftl = wt * 14 + ((lane & 15) < 14 ? (lane & 15) : 0);
  (void)poll_granule<8>(wsb, (lane & 16) ? GW_OFF + ((nb * 2 + ((lane >> 2) & 1)) * 16 + wt * 4 + (lane & 3)) * 8
                                        : GH_OFF + ((mb >> 1) * 16 + (ftl >> 2)) * 128 + (ftl & 3) * 8, tag);
  STAMP(5);
  u32x4 hb[HPRE][4], wo[16];
  const unsigned hbase = HF_OFF + (mb * 2 * 56 + wt * 14) * 2048;
  auto H = [&](int f) {
#pragma unroll
    for (int e = 0; e < 4; ++e) hb[f % HPRE][e] = ld16c(rsH, lo + (e & 1) * 1024, hbase + ((e >> 1) * 56 + fr(f)) * 2048);
  };
#pragma unroll
  for (int f = 0; f < HPRE; ++f) { H(f); SBAR(); }
  const float gl = __uint_as_float(poll_granule<8>(wsb, GG_OFF + (mb * 64 + lane) * 8, tag) << 16), gs = swap_half(gl);
  const float og0 = 1.f - (h ? gs : gl), og1 = 1.f - (h ? gl : gs);
  const int q4 = (lane & 15) >> 2;
  unsigned woff[2];
#pragma unroll
  for (int nt = 0; nt < 2; ++nt) woff[nt] = (unsigned)(L_W2 + wt * 24576 + (4 * h + q4) * 128 + ((nt ^ (q4 >> 1)) * 64) + ((lane >> 4) & 1) * 32 + (lane & 3) * 8);
#pragma unroll
  for (int i = 0; i < 4; ++i) acc[i] = f32x16{};
  STAMP(6);
  __builtin_amdgcn_s_setprio(0);
  bf16x8 wf[2][2][2];
  auto RW = [&](int f) {
#pragma unroll
    for (int s = 0; s < 2; ++s)
#pragma unroll
      for (int nt = 0; nt < 2; ++nt) wf[f & 1][s][nt] = tr_frag(lds + woff[nt] + (f % W2S) * 4096 + s * 2048, 1024);
  };
  RW(0);
  static_for<0, 14>([&](auto fc) {
    constexpr int f = decltype(fc)::value;
    asm volatile("s_waitcnt lgkmcnt(0)" ::: "memory");
    SBAR();
    if constexpr (f + 1 < 14) {
      if constexpr (f + 1 >= W2S) wait_vm<ffn2_younger(f + 1, f)>();
      RW(f + 1);
    }
    SBAR();
#pragma unroll
    for (int s = 0; s < 2; ++s)
#pragma unroll
      for (int nt = 0; nt < 2; ++nt)
#pragma unroll
        for (int mt = 0; mt < 2; ++mt) {
          acc[nt * 2 + mt] = MFMA(wf[f & 1][s][nt], as_frag(hb[f % HPRE][mt * 2 + s]), acc[nt * 2 + mt]);
        }
    SBAR();
    if constexpr (f + W2S < 14) D(f + W2S);
    if constexpr (f + HPRE < 14) H(f + HPRE);
    else if constexpr (f + HPRE < 18) {
      constexpr int e0 = (f + HPRE - 14) * 4;
#pragma unroll
      for (int e = e0; e < e0 + 4; ++e) wo[e] = ld16c(rsWs, lo, WOF_OFF + ((nb * 2 + (e >> 3)) * 16 + wt * 4) * 2048 + (e & 7) * 1024);
    }
    SBAR();
    __builtin_amdgcn_s_sleep(PACE2);
    SBAR();
  });
  STAMP(7);
  __builtin_amdgcn_s_setprio(3);
#pragma unroll
  for (int i = 0; i < 16; ++i) { acc[0][i] *= og0; acc[1][i] *= og1; acc[2][i] *= og0; acc[3][i] *= og1; }
  (void)wait_lds_word(lds + L_CTRL + 20, 1);
  __builtin_amdgcn_s_sleep(ODELAY);
  (void)poll_granule<POLL_A>(wsb, GO_OFF + ((mb * 2 + ((lane >> 2) & 1)) * 16 + wt * 4 + (lane & 3)) * 128, tag);
  STAMP(8);
  u32x4 of[16];
#pragma unroll
  for (int e = 0; e < 8; ++e)
#pragma unroll
    for (int mt = 0; mt < 2; ++mt) of[mt * 8 + e] = ld16c(rsWs, lo, OF_OFF + ((mb * 2 + mt) * 16 + wt * 4) * 2048 + e * 1024);
  SBAR();
#pragma unroll
  for (int e = 0; e < 8; ++e)
#pragma unroll
    for (int nt = 0; nt < 2; ++nt)
#pragma unroll
      for (int mt = 0; mt < 2; ++mt) acc[nt * 2 + mt] = MFMA(as_frag(wo[nt * 8 + e]), as_frag(of[mt * 8 + e]), acc[nt * 2 + mt]);
#pragma unroll
  for (int t = 0; t < 4; ++t) st_tile(lds + L_W2 + wt * 24576 + t * 4096 + lo, acc[t]);
  STAMP(9);
  bar_lds();
  f32x16 a = f32x16{};
#pragma unroll
  for (int f = 0; f < 4; ++f) add_tile(a, lds + L_W2 + f * 24576 + wt * 4096 + lo);
  unsigned short* dst = P.out + (size_t)((mb * 2 + (wt & 1)) * 32 + r) * DM + (nb * 2 + (wt >> 1)) * 32 + 8 * h;
#pragma unroll
  for (int k = 0; k < 2; ++k) {
    const auto x = __builtin_amdgcn_permlane32_swap(cvtpk(a[8 * k], a[8 * k + 1]), cvtpk(a[8 * k + 4], a[8 * k + 5]), false, false);
    const auto y = __builtin_amdgcn_permlane32_swap(cvtpk(a[8 * k + 2], a[8 * k + 3]), cvtpk(a[8 * k + 6], a[8 * k + 7]), false, false);
    *(u32x4*)(dst + 16 * k) = u32x4{x[0], y[0], x[1], y[1]};
  }
  STAMP(10);
  STAMP_FLUSH(P, 0);
}

extern __shared__ __attribute__((aligned(16))) unsigned char g_lds[];

__global__ __launch_bounds__(512, 2) void k_fused(Params P) {
  u64 pc;
  asm volatile("s_getpc_b64 %0" : "=s"(pc));
  const unsigned code_word = __builtin_amdgcn_raw_buffer_load_b32(mk_rsrc((const void*)(pc & ~63ull), CODE_PF),
      (threadIdx.x & 256) * 4096 + (((blockIdx.x >> 3) * 32 + ((threadIdx.x >> 3) & 24) + (threadIdx.x & 7)) * 64), 0, 0);
  SBAR();
  LAS unsigned char* lds = (LAS unsigned char*)g_lds;
  const int w = __builtin_amdgcn_readfirstlane(threadIdx.x >> 6);
  if (w < 4) role_a(P, lds, w, code_word);
  else role_f(P, lds, w - 4);
}

extern "C" void kernel_launch(void* const* d_in, const int* in_sizes, int n_in, void* d_out, int out_size, void* d_ws,
                              size_t ws_size, hipStream_t stream) {
  Params P;
  P.x = (const unsigned short*)d_in[0];
  P.wqkv = (const unsigned short*)d_in[1];
  P.wo = (const unsigned short*)d_in[2];
  P.w1 = (const unsigned short*)d_in[4];
  P.w2 = (const unsigned short*)d_in[5];
  P.gw = (const unsigned short*)d_in[6];
  P.out = (unsigned short*)d_out;
  P.ws = (unsigned char*)d_ws;
  (void)hipFuncSetAttribute((const void*)k_fused, hipFuncAttributeMaxDynamicSharedMemorySize, LDS_BYTES);
  k_fused<<<256, 512, LDS_BYTES, stream>>>(P);
}
```

```cpp
#include <hip/hip_runtime.h>
#include <hip/hip_bf16.h>
#include <stdint.h>

typedef short bf16x8 __attribute__((ext_vector_type(8)));
typedef short s16x4 __attribute__((ext_vector_type(4)));
typedef float f32x16 __attribute__((ext_vector_type(16)));
typedef float f32x4 __attribute__((ext_vector_type(4)));
typedef unsigned u32x4 __attribute__((ext_vector_type(4)));
typedef unsigned u32x2 __attribute__((ext_vector_type(2)));
#define LAS __attribute__((address_space(3)))
#define DI __device__ __forceinline__
#define MFMA(a, b, c) __builtin_amdgcn_mfma_f32_32x32x16_bf16((a), (b), (c), 0, 0, 0)
#define SBAR() __builtin_amdgcn_sched_barrier(0)

constexpr int SEQ = 2048, DM = 448, NH = 16, DH = 28, FF = 1792;
constexpr unsigned FL_OFF = 0;
#define SETB 1u
constexpr unsigned SET_BASE = SETB << 20, SET_STRIDE = 24u << 20;
#define GQS 16u
constexpr unsigned GQW = GQS == 16u ? 128u : 4u * GQS;
constexpr unsigned GQ_OFF = 0;
constexpr unsigned GH_OFF = GQ_OFF + 256 * GQW;
constexpr unsigned GO_OFF = GH_OFF + 256 * 128;
constexpr unsigned GW_OFF = GO_OFF + 1024 * 128;
constexpr unsigned GG_OFF = GW_OFF + 256 * 8;
constexpr unsigned QF_OFF = 524288;
constexpr unsigned KV_OFF = QF_OFF + 16 * 64 * 2048;
constexpr unsigned HF_OFF = KV_OFF + 16 * 64 * 4096;
constexpr unsigned WOF_OFF = HF_OFF + 64 * 56 * 2048;
constexpr unsigned OF_OFF = WOF_OFF + 14 * 16 * 2048;
constexpr unsigned WS_END = OF_OFF + 64 * 16 * 2048;
static_assert(GG_OFF + 2048 * 8 <= QF_OFF && WS_END <= SET_STRIDE, "workspace map");

constexpr int L_X = 0;
constexpr int L_WQ = 114688;
constexpr int L_W1 = 114688;
constexpr int L_CTRL = 163776;
constexpr int L_W2 = 0;
constexpr int L_CMB = 98304;
constexpr int LDS_BYTES = 163840;

constexpr float QSCALE = 0.18898223650461362f * 1.4426950408889634f;
constexpr float LOG2E = 1.4426950408889634f;
constexpr float SAFE_BOUND = 60.0f;
constexpr unsigned SPIN_CAP = 1u << 18;
#define ROT1(rb, j) ((((rb) + (j)) & 1) * 3)
#define KVD 4
#define ROTA 0
#define ROT2 0
#define PDELAY 16
#define ODELAY 10
#define FDELAY 18
#define CODE_PF 44032
#define MPF 1
#define HDELAY 1
#define TRIG 3
#define WDELAY 100
#define FTHR 8
#define DEARLY W2S
#define F1PRIO 3
#define POLL_A 4

struct Params {
  const unsigned short* x;
  const unsigned short* wqkv;
  const unsigned short* wo;
  const unsigned short* w1;
  const unsigned short* w2;
  const unsigned short* gw;
  unsigned short* out;
  unsigned char* ws;
};
typedef unsigned long long u64;
typedef __attribute__((address_space(1))) u64 gu64;

DI unsigned cvtpk(float lo, float hi) {
  typedef __bf16 bf2 __attribute__((ext_vector_type(2)));
  typedef float f2 __attribute__((ext_vector_type(2)));
  f2 v = {lo, hi};
  bf2 r = __builtin_convertvector(v, bf2);
  return __builtin_bit_cast(unsigned, r);
}
DI bf16x8 pack8(const f32x16& x, int s) {
  u32x4 p;
  p[0] = cvtpk(x[8 * s + 0], x[8 * s + 1]);
  p[1] = cvtpk(x[8 * s + 2], x[8 * s + 3]);
  p[2] = cvtpk(x[8 * s + 4], x[8 * s + 5]);
  p[3] = cvtpk(x[8 * s + 6], x[8 * s + 7]);
  return __builtin_bit_cast(bf16x8, p);
}
DI float swap_half(float v) {
  auto r = __builtin_amdgcn_permlane32_swap(__float_as_uint(v), __float_as_uint(v), false, false);
  return (threadIdx.x & 32) ? __uint_as_float(r[0]) : __uint_as_float(r[1]);
}
template <int CTRL> DI float dpp_mov(float v) {
  return __uint_as_float(__builtin_amdgcn_update_dpp(__float_as_uint(v), __float_as_uint(v), CTRL, 0xf, 0xf, false));
}
DI float row16_max(float v) {
  v = fmaxf(v, dpp_mov<0x128>(v));
  v = fmaxf(v, dpp_mov<0x124>(v));
  v = fmaxf(v, dpp_mov<0x122>(v));
  v = fmaxf(v, dpp_mov<0x121>(v));
  return v;
}
DI unsigned row16_umax(unsigned v) {
  v = max(v, (unsigned)__builtin_amdgcn_update_dpp(v, v, 0x128, 0xf, 0xf, false));
  v = max(v, (unsigned)__builtin_amdgcn_update_dpp(v, v, 0x124, 0xf, 0xf, false));
  v = max(v, (unsigned)__builtin_amdgcn_update_dpp(v, v, 0x122, 0xf, 0xf, false));
  v = max(v, (unsigned)__builtin_amdgcn_update_dpp(v, v, 0x121, 0xf, 0xf, false));
  return v;
}
DI float rdlane(float v, int l) { return __uint_as_float(__builtin_amdgcn_readlane(__float_as_uint(v), l)); }
DI s16x4 tr_read(const LAS unsigned char* p) {
  typedef short v4i16_t __attribute__((ext_vector_type(4)));
  return __builtin_bit_cast(s16x4, __builtin_amdgcn_ds_read_tr16_b64_v4i16((LAS v4i16_t*)p));
}
DI bf16x8 tr_frag(const LAS unsigned char* p, int row_stride4) {
  s16x4 lo = tr_read(p), hi = tr_read(p + row_stride4);
  return __builtin_shufflevector(lo, hi, 0, 1, 2, 3, 4, 5, 6, 7);
}
DI __amdgpu_buffer_rsrc_t mk_rsrc(const void* p, unsigned bytes) {
  return __builtin_amdgcn_make_buffer_rsrc((void*)p, (short)0, (int)bytes, 0x00020000);
}
DI void glds16(__amdgpu_buffer_rsrc_t rs, unsigned dst, unsigned voff, unsigned soff) {
  asm volatile("s_mov_b32 m0, %0\n\ts_nop 0\n\tbuffer_load_dwordx4 %1, %2, %3 offen lds" ::"s"(dst), "v"(voff), "s"(rs), "s"(soff) : "memory");
}
DI u32x4 ld16(__amdgpu_buffer_rsrc_t rs, unsigned voff, unsigned soff) { return __builtin_amdgcn_raw_buffer_load_b128(rs, voff, soff, 0); }
DI u32x4 ld16c(__amdgpu_buffer_rsrc_t rs, unsigned voff, unsigned soff) { return __builtin_amdgcn_raw_buffer_load_b128(rs, voff, soff, 16); }
DI void st16(__amdgpu_buffer_rsrc_t rs, u32x4 v, unsigned voff, unsigned soff) { __builtin_amdgcn_raw_buffer_store_b128(v, rs, voff + soff, 0, 16); }
DI bf16x8 as_frag(u32x4 v) { return __builtin_bit_cast(bf16x8, v); }
DI u32x4 as_u4(bf16x8 v) { return __builtin_bit_cast(u32x4, v); }
template <int N> DI void wait_bar() { asm volatile("s_waitcnt vmcnt(%0) lgkmcnt(0)\n\ts_barrier" ::"n"(N) : "memory"); }

template <int V> struct IC { static constexpr int value = V; };
template <int I, int N, class Fn> DI void static_for(Fn&& fn) {
  if constexpr (I < N) { fn(IC<I>{}); static_for<I + 1, N>(fn); }
}
DI void drain() { asm volatile("s_waitcnt vmcnt(0)" ::: "memory"); }
template <int N> DI void wait_vm() { asm volatile("s_waitcnt vmcnt(%0)" ::"n"(N) : "memory"); }
DI void put_granule(unsigned char* ws, unsigned off, u64 tag16, unsigned payload) {
  __hip_atomic_store((gu64*)(ws + off), tag16 | (u64)(payload & 0xffffu), __ATOMIC_RELAXED, __HIP_MEMORY_SCOPE_AGENT);
}
template <int SLEEP> DI unsigned poll_granule(unsigned char* ws, unsigned off, u64 tag16) {
  u64 v = ~tag16;
  for (unsigned spins = 0;; ++spins) {
    if (v - tag16 > 0xffffull) v = __hip_atomic_load((gu64*)(ws + off), __ATOMIC_RELAXED, __HIP_MEMORY_SCOPE_AGENT);
    if (__all(v - tag16 <= 0xffffull) || spins > SPIN_CAP) break;
    __builtin_amdgcn_s_sleep(SLEEP);
  }
  __builtin_amdgcn_fence(__ATOMIC_ACQUIRE, "wavefront");
  return (unsigned)v & 0xffffu;
}
template <int SLEEP = 1> DI unsigned wait_lds_word(LAS unsigned char* p, unsigned want) {
  unsigned v = 0;
#pragma nounroll
  for (unsigned spins = 0; spins < (1u << 24); ++spins) {
    v = __builtin_amdgcn_readfirstlane(__hip_atomic_load((LAS unsigned*)p, __ATOMIC_RELAXED, __HIP_MEMORY_SCOPE_WORKGROUP));
    if (v >= want) break;
    __builtin_amdgcn_s_sleep(SLEEP);
  }
  asm volatile("" ::: "memory");
  return v;
}
template <int N> DI u64 wait_bar_epoch(const unsigned char* p) {
  u64 ep;
  asm volatile("s_load_dwordx2 %0, %1, 0x0\n\ts_waitcnt vmcnt(%2) lgkmcnt(0)\n\ts_barrier" : "=&s"(ep) : "s"(p), "n"(N) : "memory");
  return ep;
}
DI void bar_lds() { asm volatile("s_waitcnt lgkmcnt(0)\n\ts_barrier" ::: "memory"); }
DI void st_tile(LAS unsigned char* p, const f32x16& a) {
#pragma unroll
  for (int g = 0; g < 4; ++g) *(LAS f32x4*)(p + g * 1024) = f32x4{a[4 * g], a[4 * g + 1], a[4 * g + 2], a[4 * g + 3]};
}
DI void add_tile(f32x16& a, const LAS unsigned char* p) {
#pragma unroll
  for (int g = 0; g < 4; ++g) {
    const f32x4 v = *(const LAS f32x4*)(p + g * 1024);
    a[4 * g] += v[0]; a[4 * g + 1] += v[1]; a[4 * g + 2] += v[2]; a[4 * g + 3] += v[3];
  }
}

#define STAMP_DECL do {} while (0)
#define STAMP(id) do {} while (0)
#define QSTAMP(id) do {} while (0)
#define RSTAMP(id) do {} while (0)
#define STAMP_FLUSH(P, ph) do {} while (0)

DI void lds_arrive(LAS unsigned char* p) {
  asm volatile("s_mov_b64 exec, 1\n\tds_add_u32 %0, %1\n\ts_mov_b64 exec, -1" ::"v"((unsigned)(uintptr_t)p), "v"(1u) : "memory");
}

#define PINV(x) asm volatile("" : "+v"(x))
#define PINS(x) asm volatile("" : "+s"(x))
DI float sumsq(const u32x4& a, const u32x4& b) {
  typedef __bf16 bf2 __attribute__((ext_vector_type(2)));
  float n0 = 0.f, n1 = 0.f;
#pragma unroll
  for (int i = 0; i < 4; ++i) {
    n0 = __builtin_amdgcn_fdot2_f32_bf16(__builtin_bit_cast(bf2, a[i]), __builtin_bit_cast(bf2, a[i]), n0, false);
    n1 = __builtin_amdgcn_fdot2_f32_bf16(__builtin_bit_cast(bf2, b[i]), __builtin_bit_cast(bf2, b[i]), n1, false);
  }
  return n0 + n1;
}

DI void role_a(const Params& P, LAS unsigned char* lds, const int wt, const unsigned code_word) {
  const int lane = threadIdx.x & 63, r = lane & 31, h = lane >> 5;
  const int b = blockIdx.x, xcd = b & 7, idx = b >> 3;
  const unsigned lo = lane * 16;
  STAMP_DECL;
  STAMP(0);
  __builtin_amdgcn_s_setprio(2);
  unsigned xtouch = 0;
  if (wt < 2) xtouch = __builtin_amdgcn_raw_buffer_load_b32(wt ? mk_rsrc(P.wqkv, DM * 3 * DM * 2) : mk_rsrc(P.x, SEQ * DM * 2),
                                                            (unsigned)(wt ? b * 37 + (lane < 37 ? lane : 0) : b * 56 + (lane < 56 ? lane : 0)) * 128, 0, 0);
  SBAR();
  f32x16 acc[4];
  const int rb = (xcd & 1) * 8 + (idx & 7), j = (xcd >> 1) * 4 + (idx >> 3);
  const int head = xcd * 2 + (idx & 1), qb = idx >> 1;
  unsigned sto[3];
#pragma unroll
  for (int ct = 0; ct < 3; ++ct) {
    const int u = 3 * j + ct, part = u >> 4, t = (u & 15) * 64 + rb * 4 + wt;
    sto[ct] = lo + (part == 0 ? QF_OFF + t * 2048 : KV_OFF + t * 4096 + (part - 1) * 2048);
    PINV(sto[ct]);
  }
  unsigned padv = r == DH ? 0x3f803f80u : 0u;
  PINV(padv);
  const int gq = head / 3, gk = (16 + head) / 3, gvv = (32 + head) / 3;
  unsigned pub_off = GQ_OFF + (j * 16 + rb) * GQW + wt * GQS + (lane & 1) * 8;
  unsigned poll_off = GQ_OFF + (h ? gq * 16 + qb : (lane < 16 ? gk : gvv) * 16 + wt * 4 + ((lane >> 2) & 3)) * GQW + (lane & 3) * GQS + h * 8;
  unsigned chk_off = GQ_OFF + (gk * 16 + (lane >> 2)) * GQW + (lane & 3) * GQS;
  unsigned g_off = GG_OFF + ((qb * 4 + wt) * 32 + r) * 8;
  unsigned o_off = lo + OF_OFF + ((qb * 4 + wt) * 16 + head) * 2048;
  PINV(pub_off); PINV(poll_off); PINV(chk_off); PINV(g_off); PINV(o_off);
  unsigned kvbase = KV_OFF + (head * 64 + wt * 16) * 4096, go_off = GO_OFF + ((qb * 4 + wt) * 16 + head) * 128;
  unsigned qofs[4];
#pragma unroll
  for (int t = 0; t < 4; ++t) { qofs[t] = QF_OFF + (unsigned)(head * 64 + qb * 4 + ((wt + 1 + t) & 3)) * 2048; PINS(qofs[t]); }
  PINS(kvbase); PINS(go_off);
  unsigned xoff[4];
#pragma unroll
  for (int ks = 0; ks < 4; ++ks) xoff[ks] = (unsigned)(L_X + wt * 4096 + r * 128 + (((2 * ks + h) ^ ((r >> 1) & 7)) << 4));
  const unsigned wqoff = (unsigned)(L_WQ + (8 * h + ((lane & 15) >> 2)) * 192 + ((lane >> 4) & 1) * 32 + (lane & 3) * 8 + (j & 1) * 8);
  u64 ep;
  asm volatile("s_load_dwordx2 %0, %1, 0x0\n\ts_waitcnt lgkmcnt(0)" : "=&s"(ep) : "s"(P.ws + FL_OFF) : "memory");
  u64 tag16 = (ep + 1) << 16;
  unsigned char* wsb = P.ws + SET_BASE + (((unsigned)ep + 1u) & 3u) * SET_STRIDE;
  PINS(tag16); PINS(wsb);
  const __amdgpu_buffer_rsrc_t rsWs = mk_rsrc(wsb, WS_END);
#pragma unroll
  for (int i = 0; i < 3; ++i) acc[i] = f32x16{};
  unsigned wtouch = 0;
  auto kloop = [&](auto nvc) {
    constexpr int NV = decltype(nvc)::value;
    bf16x8 xf[4], wf[4][3];
    auto mm = [&](int ks, int ct) { acc[ct] = ct >= 3 - NV ? MFMA(xf[ks], wf[ks][ct], acc[ct]) : MFMA(wf[ks][ct], xf[ks], acc[ct]); };
    auto step = [&](int c, int ks, bool domm) {
      const int km = (ks + 2) & 3;
#pragma unroll
      for (int ct = 0; ct < 3; ++ct) {
        if (domm) mm(km, ct);
        SBAR();
        if (ct == 0) xf[ks] = *(const LAS bf16x8*)(lds + xoff[ks] + c * 16384);
        wf[ks][ct] = tr_frag(lds + wqoff + (c & 3) * 12288 + ks * 16 * 192 + ct * 56, 4 * 192);
        SBAR();
      }
    };
#pragma unroll
    for (int c = 0; c < 7; ++c) {
      bar_lds();
      if (c == 0) STAMP(1);
      if (c == MPF) {
        const unsigned l = (unsigned)(b * 128 + (wt & 1) * 64 + lane) * 128;
        if (wt < 2) wtouch = l < DM * FF * 2 ? __builtin_amdgcn_raw_buffer_load_b32(mk_rsrc(P.w1, DM * FF * 2), l, 0, 0)
                           : l < 2 * DM * FF * 2 ? __builtin_amdgcn_raw_buffer_load_b32(mk_rsrc(P.w2, DM * FF * 2), l - DM * FF * 2, 0, 0)
                                                 : __builtin_amdgcn_raw_buffer_load_b32(mk_rsrc(P.wo, DM * DM * 2), l - 2 * DM * FF * 2, 0, 0);
        SBAR();
      }
      QSTAMP(1 + c);
      step(c, 0, c > 0);
      step(c, 1, c > 0);
      step(c, 2, true);
      step(c, 3, true);
    }
    bar_lds();
    QSTAMP(8);
#pragma unroll
    for (int ct = 0; ct < 3; ++ct) { mm(2, ct); mm(3, ct); SBAR(); }
  };
  auto epi = [&](auto nqc, auto nvc) {
    constexpr int NQ = decltype(nqc)::value, NV = decltype(nvc)::value;
    STAMP(2);
    asm volatile("" ::"v"(code_word), "v"(wtouch), "v"(xtouch));
    u32x4 t0[3], t1[3];
#pragma unroll
    for (int ct = 0; ct < 3; ++ct) {
      if (ct < NQ) acc[ct] *= QSCALE;
      t0[ct] = as_u4(pack8(acc[ct], 0));
      if (ct >= 3 - NV) {
#pragma unroll
        for (int i = 0; i < 4; ++i) t0[ct][i] = r < DH ? t0[ct][i] : padv;
      }
      st16(rsWs, t0[ct], sto[ct], 0);
      t1[ct] = as_u4(pack8(acc[ct], 1));
      if (ct >= 3 - NV) {
#pragma unroll
        for (int i = 0; i < 4; ++i) t1[ct][i] = r < DH ? t1[ct][i] : padv;
      } else {
        t1[ct][2] = h ? (ct < NQ ? 0u : 0x3f803f80u) : t1[ct][2];
        t1[ct][3] = h ? (ct < NQ ? 0u : 0x00003f80u) : t1[ct][3];
      }
      st16(rsWs, t1[ct], sto[ct], 1024);
      SBAR();
    }
    STAMP(8);
    unsigned mq = 0, mk = 0;
#pragma unroll
    for (int ct = 0; ct < 3; ++ct) {
      const unsigned n = __float_as_uint(sumsq(t0[ct], t1[ct])) & 0x7fffffffu;
      if (ct < NQ) mq = max(mq, n); else mk = max(mk, n);
    }
    auto bound = [&](unsigned m) {
      m = row16_umax(m);
      const float lo2 = __uint_as_float(max((unsigned)__builtin_amdgcn_readlane(m, 0), (unsigned)__builtin_amdgcn_readlane(m, 16)));
      const float hi2 = __uint_as_float(max((unsigned)__builtin_amdgcn_readlane(m, 32), (unsigned)__builtin_amdgcn_readlane(m, 48)));
      return ((__float_as_uint(lo2 + hi2) >> 16) + 1u) & 0xffffu;
    };
    const unsigned pq = NQ > 0 ? bound(mq) : 0u, pk = NQ < 3 ? bound(mk) : 0u;
    u64 gran = tag16 | ((lane & 1) ? pq : pk);
    PINV(gran);
    drain();
    STAMP(9);
    if (lane < 2) __hip_atomic_store((gu64*)(wsb + pub_off), gran, __ATOMIC_RELAXED, __HIP_MEMORY_SCOPE_AGENT);
  };
  if (j < 10) {
    kloop(IC<0>{});
    if (j < 5) epi(IC<3>{}, IC<0>{});
    else if (j == 5) epi(IC<1>{}, IC<0>{});
    else epi(IC<0>{}, IC<0>{});
  } else if (j == 10) {
    kloop(IC<1>{});
    epi(IC<0>{}, IC<1>{});
  } else {
    kloop(IC<3>{});
    epi(IC<0>{}, IC<3>{});
  }
  STAMP(3);
  __builtin_amdgcn_s_sleep(PDELAY);
  const unsigned pay = poll_granule<POLL_A>(wsb, poll_off, tag16);
  STAMP(4);
  u32x4 q[4][2];
  LAS unsigned char* const cmb = lds + L_CMB + wt * 12288 + lo;
  u64 gv = 0;
  unsigned thr = 0;
#pragma nounroll
  for (int pass = 0;; ++pass) {
    u32x4 kv[KVD][4];
    kv[0][0] = ld16c(rsWs, lo, kvbase);
    SBAR();
    if (pass == 0) { q[0][0] = ld16c(rsWs, lo, qofs[0]); q[0][1] = ld16c(rsWs, lo + 1024, qofs[0]); }
    SBAR();
    kv[0][1] = ld16c(rsWs, lo + 1024, kvbase);
    SBAR();
    if (pass == 0) {
#pragma unroll
      for (int t = 1; t < 4; ++t) { q[t][0] = ld16c(rsWs, lo, qofs[t]); q[t][1] = ld16c(rsWs, lo + 1024, qofs[t]); SBAR(); }
    }
#pragma unroll
    for (int e = 2; e < 4 * (KVD - 1); ++e) { kv[e >> 2][e & 3] = ld16c(rsWs, lo + (e & 3) * 1024, kvbase + (e >> 2) * 4096); SBAR(); }
#pragma unroll
    for (int i = 0; i < 4; ++i) acc[i] = f32x16{};
    {
      const float q2 = __uint_as_float(__builtin_amdgcn_readlane(row16_umax(pay), 32) << 16);
      const float lim = SAFE_BOUND * SAFE_BOUND * __builtin_amdgcn_rcpf(q2);
      thr = lim < 1e30f ? __float_as_uint(lim) >> 16 : (lim >= 1e30f ? 0x7000u : 0u);
      PINV(thr);
    }
    __builtin_amdgcn_s_setprio(1);
    f32x16 sc = MFMA(as_frag(kv[0][0]), as_frag(q[0][0]), f32x16{});
    sc = MFMA(as_frag(kv[0][1]), as_frag(q[0][1]), sc);
    STAMP(5);
    u32x4 p0, p1 = {0, 0, 0, 0};
    float e0 = 0.f, e1 = 0.f, e2 = 0.f, e3 = 0.f;
    u64 ck = 0, cv = 0;
#define EXP4(i) do { e0 = __builtin_amdgcn_exp2f(sc[i]); e1 = __builtin_amdgcn_exp2f(sc[(i) + 1]); e2 = __builtin_amdgcn_exp2f(sc[(i) + 2]); e3 = __builtin_amdgcn_exp2f(sc[(i) + 3]); \
    asm volatile("" : "+v"(e0), "+v"(e1), "+v"(e2), "+v"(e3)); SBAR(); } while (0)
#define CVT2(P, i) do { P[i] = cvtpk(e0, e1); P[(i) + 1] = cvtpk(e2, e3); asm volatile("" : "+v"(P)); SBAR(); } while (0)
#define PART(t, g) do { *(LAS f32x4*)(cmb + (t) * 4096 + (g) * 1024) = f32x4{acc[t][4 * (g)], acc[t][4 * (g) + 1], acc[t][4 * (g) + 2], acc[t][4 * (g) + 3]}; SBAR(); } while (0)
    static_for<0, 64>([&](auto ic) {
      constexpr int i = decltype(ic)::value;
      constexpr int kt = i >> 2, t = i & 3, kn_ = (i + 1) >> 2, tn = (i + 1) & 3, kp = (i - 1) >> 2, tp = (i - 1) & 3;
      constexpr int kl = kt + KVD - 1;
      if (i == 24) STAMP(11);
      if (i == 56) (void)wait_lds_word(lds + L_CTRL, 4);
      f32x16 sn;
      SBAR();
      if (i < 63) sn = MFMA(as_frag(kv[kn_ % KVD][0]), as_frag(q[tn][0]), f32x16{});
      SBAR();
      if constexpr (i >= 62) PART(i - 62, 0);
      if (i > 0) CVT2(p1, 2);
      EXP4(0);
      if (i > 0) acc[tp] = MFMA(as_frag(kv[(kp + KVD) % KVD][3]), as_frag(p1), acc[tp]);
      SBAR();
      if constexpr (t == 0 && kl < 16) {
#pragma unroll
        for (int e = 0; e < 4; ++e) kv[kl % KVD][e] = ld16c(rsWs, lo + e * 1024, kvbase + kl * 4096);
      }
      if constexpr (t == 2 && kt + 1 < 16) {
        constexpr int n = 4 * ((kl < 16 ? kl : 15) - (kt + 1)) + (i > 53 ? 3 : 0);
        __builtin_amdgcn_s_waitcnt(0x0f70 | (n & 15) | ((n >> 4) << 14));
      }
      if (i == 51) gv = __hip_atomic_load((gu64*)(wsb + g_off), __ATOMIC_RELAXED, __HIP_MEMORY_SCOPE_AGENT);
      if (i == 52) ck = __hip_atomic_load((gu64*)(wsb + chk_off), __ATOMIC_RELAXED, __HIP_MEMORY_SCOPE_AGENT);
      if (i == 53) cv = __hip_atomic_load((gu64*)(wsb + (gvv - gk) * 16 * GQW + chk_off), __ATOMIC_RELAXED, __HIP_MEMORY_SCOPE_AGENT);
      SBAR();
      if constexpr (i >= 62) PART(i - 62, 1);
      CVT2(p0, 0);
      EXP4(4);
      if (i < 63) sn = MFMA(as_frag(kv[kn_ % KVD][1]), as_frag(q[tn][1]), sn);
      SBAR();
      if constexpr (i >= 62) PART(i - 62, 2);
      CVT2(p0, 2);
      EXP4(8);
      acc[t] = MFMA(as_frag(kv[kt % KVD][2]), as_frag(p0), acc[t]);
      SBAR();
      if constexpr (i >= 62) PART(i - 62, 3);
      CVT2(p1, 0);
      EXP4(12);
      if (i < 63) sc = sn;
    });
    CVT2(p1, 2);
    acc[3] = MFMA(as_frag(kv[15 % KVD][3]), as_frag(p1), acc[3]);
    SBAR();
    PART(2, 0); PART(2, 1); PART(2, 2); PART(2, 3);
#undef EXP4
#undef CVT2
#undef PART
    __builtin_amdgcn_s_setprio(2);
    if (__builtin_expect(__all(ck - tag16 <= (u64)thr && cv - tag16 <= 0x7000ull && gv - tag16 <= 0xffffull), 1)) break;
    gv = poll_granule<2>(wsb, g_off, tag16);
    if (pass) break;
    const unsigned pk = poll_granule<2>(wsb, chk_off, tag16), pv = poll_granule<2>(wsb, (gvv - gk) * 16 * GQW + chk_off, tag16);
    if (__all(pk <= thr && pv <= 0x7000u)) break;
    float mx[4];
#pragma unroll
    for (int t = 0; t < 4; ++t) mx[t] = -INFINITY;
    for (int kt = 0; kt < 64; ++kt) {
      const u32x4 k0 = ld16c(rsWs, lo, KV_OFF + (head * 64 + kt) * 4096), k1 = ld16c(rsWs, lo + 1024, KV_OFF + (head * 64 + kt) * 4096);
#pragma unroll
      for (int t = 0; t < 4; ++t) {
        f32x16 s = MFMA(as_frag(k0), as_frag(q[t][0]), f32x16{});
        s = MFMA(as_frag(k1), as_frag(q[t][1]), s);
#pragma unroll
        for (int i = 0; i < 16; ++i) mx[t] = fmaxf(mx[t], s[i]);
      }
    }
#pragma unroll
    for (int t = 0; t < 4; ++t) {
      const float nm = -fmaxf(mx[t], swap_half(mx[t]));
      const unsigned hi = __float_as_uint(nm) & 0xffff0000u;
      const float r1 = nm - __uint_as_float(hi);
      const unsigned mid = __float_as_uint(r1) & 0xffff0000u;
      const float r2 = r1 - __uint_as_float(mid);
      if (h) { q[t][1][2] = (hi >> 16) | mid; q[t][1][3] = __float_as_uint(r2) >> 16; }
    }
  }
  STAMP(6);
  asm volatile("s_waitcnt lgkmcnt(0)" ::: "memory");
  lds_arrive(lds + L_CTRL + 20);
  (void)wait_lds_word(lds + L_CTRL + 20, 4);
  f32x16 o = acc[3];
#pragma unroll
  for (int u = 0; u < 3; ++u) add_tile(o, lds + L_CMB + ((wt + 3 - u) & 3) * 12288 + u * 4096 + lo);
  const auto lsw = __builtin_amdgcn_permlane32_swap(__float_as_uint(o[12]), __float_as_uint(o[12]), false, false);
  const float inv = __uint_as_float((unsigned)gv << 16) * __builtin_amdgcn_rcpf(__uint_as_float(lsw[1]));
  o *= inv;
  st16(rsWs, as_u4(pack8(o, 0)), o_off, 0);
  u32x4 o1 = as_u4(pack8(o, 1));
  o1[2] = h ? 0u : o1[2];
  o1[3] = h ? 0u : o1[3];
  st16(rsWs, o1, o_off, 1024);
  STAMP(7);
  drain();
  if (lane == 0) __hip_atomic_store((gu64*)(wsb + go_off), tag16, __ATOMIC_RELAXED, __HIP_MEMORY_SCOPE_AGENT);
  STAMP(10);
  STAMP_FLUSH(P, 0);
}

#define W2S 6
#define HPRE 6
#define NPRE 14
#define LEA 5
#define LEB 11
#define LE0 16
#define LU0 3
#define F1LA 2
#define PACE 2
#define PACE2 2
constexpr int ffn2_tail(int f) { return f + HPRE < 18 ? 4 : 0; }
constexpr int ffn2_ops(int f) { return (f + W2S < 14 ? 4 : 0) + ffn2_tail(f); }
constexpr int ffn2_younger(int f, int upto) {
  int n = ffn2_tail(f - W2S);
  for (int i = f - W2S + 1; i < upto; ++i) n += ffn2_ops(i);
  return n;
}

DI void role_f(const Params& P, LAS unsigned char* lds, const int wt) {
  u64 ep;
  const int lane = threadIdx.x & 63, r = lane & 31, h = lane >> 5;
  const int b = blockIdx.x, xcd = b & 7, idx = b >> 3;
  const int rb = (xcd & 1) * 8 + (idx & 7), j = (xcd >> 1) * 4 + (idx >> 3);
  const unsigned lo = lane * 16, lds0 = (unsigned)(uintptr_t)lds;
  const bool ffn = j < 14;
  STAMP_DECL;
  STAMP(0);
  f32x16 acc[4];
  u32x4 wl[28];
  const __amdgpu_buffer_rsrc_t rsL = mk_rsrc(ffn ? P.w1 : P.gw, ffn ? DM * FF * 2 : DM * 2);
  const unsigned lsrc = ffn ? (unsigned)((lane >> 2) * (FF * 2) + (j * 4 + wt) * 64 + (lane & 3) * 16) : (unsigned)(h * 16);
  const unsigned lstep = ffn ? 16 * FF * 2 : 32;
  const int rot = ROT1(rb, j);
  auto kc = [&](int c) { const int v = c + rot; return v >= 7 ? v - 7 : v; };
  auto L = [&](int u0, int u1) {
#pragma unroll
    for (int u = u0; u < u1; ++u) wl[u] = ld16(rsL, lsrc, (kc(u >> 2) * 4 + (u & 3)) * lstep);
    SBAR();
  };
  {
    const __amdgpu_buffer_rsrc_t rsX = mk_rsrc(P.x, SEQ * DM * 2);
    const __amdgpu_buffer_rsrc_t rsW = mk_rsrc(P.wqkv, DM * 3 * DM * 2);
    unsigned xsrc[2], wsrc[3];
#pragma unroll
    for (int i = 0; i < 2; ++i) {
      const int row = 32 * wt + 8 * i + (lane >> 3), ch = (lane & 7) ^ ((row >> 1) & 7);
      xsrc[i] = (unsigned)((rb * 128 + row) * (DM * 2) + ch * 16);
    }
#pragma unroll
    for (int i = 0; i < 3; ++i) {
      const int p = 64 * (3 * wt + i) + lane, kr = p / 12, ch = p % 12;
      wsrc[i] = (unsigned)(kr * (3 * DM * 2) + j * 168 - (j & 1) * 8 + (ch < 11 ? ch : 10) * 16);
    }
    auto X = [&](int c) {
#pragma unroll
      for (int i = 0; i < 4; ++i) glds16(rsX, lds0 + L_X + c * 16384 + (4 * wt + i) * 1024, xsrc[i & 1], kc(c) * 128 + (i >> 1) * 16 * DM * 2);
    };
    auto W = [&](int c) {
#pragma unroll
      for (int i = 0; i < 3; ++i) glds16(rsW, lds0 + L_WQ + (c & 3) * 12288 + (3 * wt + i) * 1024, wsrc[i], kc(c) * 64 * 3 * DM * 2);
    };
    X(0); W(0); X(1); W(1); X(2);
    QSTAMP(1);
    wait_bar<11>();
    STAMP(1);
    W(2); X(3); W(3);
    QSTAMP(2);
    wait_bar<14>();
    X(4); W(4);
    QSTAMP(3);
    wait_bar<14>();
    X(5); W(5);
    QSTAMP(4);
    wait_bar<14>();
    X(6); W(6);
    QSTAMP(5);
    wait_bar<14>();
    if (wt == 0 && lane < 8) *(LAS unsigned*)(lds + L_CTRL + lane * 4) = 0u;
    L(0, LEA);
    QSTAMP(6);
    ep = wait_bar_epoch<7 + LEA>(P.ws + FL_OFF);
    L(LEA, LEB);
    QSTAMP(7);
    wait_bar<LEB>();
    L(LEB, LE0);
    QSTAMP(8);
    bar_lds();
    __builtin_amdgcn_s_sleep(FDELAY);
    __builtin_amdgcn_s_setprio(F1PRIO);
  }
  STAMP(2);
  const u64 tag = (ep + 1) << 16;
  unsigned char* const wsb = P.ws + SET_BASE + (((unsigned)ep + 1u) & 3u) * SET_STRIDE;
  const __amdgpu_buffer_rsrc_t rsWs = mk_rsrc(wsb, WS_END);
  unsigned xoff[4];
#pragma unroll
  for (int ks = 0; ks < 4; ++ks) xoff[ks] = (unsigned)(L_X + r * 128 + (((2 * ks + h) ^ ((r >> 1) & 7)) << 4));
  const int mb = 4 * xcd + (idx & 3), nb = idx >> 2;
  const __amdgpu_buffer_rsrc_t rsW2 = mk_rsrc(P.w2, FF * DM * 2), rsH = rsWs;
  const unsigned w2src = (unsigned)((wt * 448 + (lane >> 3)) * (DM * 2) + nb * 128 + ((((lane >> 2) & 1) ^ ((lane >> 4) & 1)) * 64) + (lane & 3) * 16);
  const unsigned ring0 = lds0 + L_W2 + wt * (W2S * 4096);
  const int rot2 = ROT2;
  auto fr = [&](int f) { const int v = f + rot2; return v >= 14 ? v - 14 : v; };
  auto D = [&](int f) {
#pragma unroll
    for (int i = 0; i < 4; ++i) glds16(rsW2, ring0 + (f % W2S) * 4096 + i * 1024, w2src, (fr(f) * 32 + i * 8) * DM * 2);
  };
  const bool has_tile = idx < 28;
  auto fill_ring = [&]() {
    if (has_tile) {
      (void)wait_lds_word(lds + L_CTRL, 4);
#pragma unroll
      for (int f = 0; f < DEARLY; ++f) { D(f); wait_vm<FTHR>(); __builtin_amdgcn_s_sleep(PACE); }
    }
  };
  if (ffn) {
    LAS unsigned char* ring = lds + L_W1 + wt * 8192;
    const unsigned troff = (unsigned)((8 * h + ((lane & 15) >> 2)) * 64 + ((lane >> 4) & 1) * 32 + (lane & 3) * 8);
#pragma unroll
    for (int i = 0; i < 4; ++i) acc[i] = f32x16{};
    RSTAMP(1);
    constexpr int LA = F1LA;
    bf16x8 xf[LA + 1][4], wf[LA + 1];
    auto wr = [&](int u) { *(LAS u32x4*)(ring + (u & 7) * 1024 + lo) = wl[u]; };
    auto rdw = [&](int u) { wf[u % (LA + 1)] = tr_frag(ring + (u & 7) * 1024 + troff, 4 * 64); };
    auto rdx = [&](int u, int t) { xf[u % (LA + 1)][t] = *(const LAS bf16x8*)(lds + xoff[u & 3] + (u >> 2) * 16384 + t * 4096); };
#pragma unroll
    for (int u = 0; u < LA + 2; ++u) wr(u);
#pragma unroll
    for (int u = 0; u < LA; ++u) {
      rdw(u);
#pragma unroll
      for (int t = 0; t < 4; ++t) rdx(u, t);
    }
    SBAR();
#pragma unroll
    for (int u = 0; u < 28; ++u) {
      if ((u & 3) == 0) RSTAMP(2 + (u >> 2));
#pragma unroll
      for (int t = 0; t < 4; ++t) {
        if (u + LA < 28) {
          if (t == 1) rdw(u + LA);
          rdx(u + LA, t);
          if (t == 3 && u + LA + 2 < 28) wr(u + LA + 2);
        }
        SBAR();
        acc[t] = MFMA(wf[u % (LA + 1)], xf[u % (LA + 1)][t], acc[t]);
        SBAR();
        if (u >= LU0 && LE0 + (u - LU0) * 4 + t < 28) L(LE0 + (u - LU0) * 4 + t, LE0 + (u - LU0) * 4 + t + 1);
      }
    }
    lds_arrive(lds + L_CTRL);
    RSTAMP(9);
    STAMP(3);
    __builtin_amdgcn_s_setprio(3);
#pragma unroll
    for (int t = 0; t < 4; ++t) {
      f32x16 z = acc[t];
#pragma unroll
      for (int i = 0; i < 16; ++i) z[i] = z[i] * __builtin_amdgcn_rcpf(1.f + __builtin_amdgcn_exp2f(-LOG2E * z[i]));
      const unsigned so = HF_OFF + ((rb * 4 + t) * 56 + j * 4 + wt) * 2048;
      st16(rsWs, as_u4(pack8(z, 0)), lo, so);
      st16(rsWs, as_u4(pack8(z, 1)), lo + 1024, so);
    }
    STAMP(11);
    drain();
    if (lane == 0) put_granule(wsb, GH_OFF + (rb * 16 + j) * 128 + wt * 8, tag, 0);
  } else {
    __builtin_amdgcn_s_setprio(3);
    if (j == 14) {
      L(LE0, 28);
      acc[0] = f32x16{};
#pragma unroll
      for (int u = 0; u < 28; ++u) {
        const bf16x8 xf = *(const LAS bf16x8*)(lds + xoff[u & 3] + (u >> 2) * 16384 + wt * 4096);
        acc[0] = MFMA(as_frag(wl[u]), xf, acc[0]);
      }
      const float g = __builtin_amdgcn_rcpf(1.f + __builtin_amdgcn_exp2f(-LOG2E * acc[0][0]));
      if (h == 0) put_granule(wsb, GG_OFF + ((rb * 4 + wt) * 32 + r) * 8, tag, cvtpk(g, 0.f));
    }
    lds_arrive(lds + L_CTRL);
    __builtin_amdgcn_s_sleep(WDELAY);
    const __amdgpu_buffer_rsrc_t rsO = mk_rsrc(P.wo, DM * DM * 2);
    const int id0 = ((j - 14) * 16 + rb) * 4 + wt;
    for (int id = id0; id < 224; id += 128) {
      const int nt = id >> 4, hd = id & 15;
      unsigned rp[16];
#pragma unroll
      for (int e = 0; e < 16; ++e)
        rp[e] = __builtin_amdgcn_raw_buffer_load_b16(rsO, (unsigned)(((4 * h + 8 * (e >> 2) + (e & 3)) * DM + r) * 2), (unsigned)((DH * hd * DM + 32 * nt) * 2), 0);
      u32x4 a, c2;
#pragma unroll
      for (int i = 0; i < 4; ++i) {
        a[i] = (rp[2 * i] & 0xffffu) | (rp[2 * i + 1] << 16);
        c2[i] = (rp[8 + 2 * i] & 0xffffu) | (rp[8 + 2 * i + 1] << 16);
      }
      c2[2] = h ? 0u : c2[2];
      c2[3] = h ? 0u : c2[3];
      st16(rsWs, a, lo, WOF_OFF + id * 2048);
      st16(rsWs, c2, lo + 1024, WOF_OFF + id * 2048);
      drain();
      if (lane == 0) put_granule(wsb, GW_OFF + id * 8, tag, 0);
    }
  }
  STAMP(4);
  if (idx >= 28) {
    if (b == 255 && wt == 0) {
      __builtin_amdgcn_s_sleep(127);
#pragma unroll
      for (int i = 0; i < 4; ++i) (void)poll_granule<64>(wsb, GQ_OFF + (i * 64 + lane) * GQW, tag);
      if (lane == 0) __hip_atomic_store((gu64*)(P.ws + FL_OFF), ep + 1, __ATOMIC_RELAXED, __HIP_MEMORY_SCOPE_AGENT);
    }
    STAMP(10);
    STAMP_FLUSH(P, 0);
    return;
  }
  fill_ring();
  const float gl = __uint_as_float(poll_granule<8>(wsb, GG_OFF + (mb * 64 + lane) * 8, tag) << 16), gs = swap_half(gl);
  const float og0 = 1.f - (h ? gs : gl), og1 = 1.f - (h ? gl : gs);
  __builtin_amdgcn_s_sleep(HDELAY);
  const int ftl = wt * 14 + ((lane & 15) < 14 ? (lane & 15) : 0);
  (void)poll_granule<8>(wsb, GH_OFF + ((mb >> 1) * 16 + (ftl >> 2)) * 128 + (ftl & 3) * 8, tag);
  STAMP(5);
  u32x4 hb[HPRE][4], wo[16];
  const unsigned hbase = HF_OFF + (mb * 2 * 56 + wt * 14) * 2048;
  auto H = [&](int f) {
#pragma unroll
    for (int e = 0; e < 4; ++e) hb[f % HPRE][e] = ld16c(rsH, lo + (e & 1) * 1024, hbase + ((e >> 1) * 56 + fr(f)) * 2048);
  };
#pragma unroll
  for (int f = 0; f < (HPRE > W2S ? HPRE : W2S); ++f) {
    if (f >= DEARLY && f < W2S) D(f);
    if (f < HPRE) H(f);
    SBAR(); wait_vm<FTHR>(); __builtin_amdgcn_s_sleep(PACE); SBAR();
  }
  if (DEARLY < W2S) drain();
  const int q4 = (lane & 15) >> 2;
  unsigned woff[2];
#pragma unroll
  for (int nt = 0; nt < 2; ++nt) woff[nt] = (unsigned)(L_W2 + wt * (W2S * 4096) + (4 * h + q4) * 128 + ((nt ^ (q4 >> 1)) * 64) + ((lane >> 4) & 1) * 32 + (lane & 3) * 8);
#pragma unroll
  for (int i = 0; i < 4; ++i) acc[i] = f32x16{};
  STAMP(6);
  __builtin_amdgcn_s_setprio(0);
  bf16x8 wf[2][2][2];
  auto RW = [&](int f) {
#pragma unroll
    for (int s = 0; s < 2; ++s)
#pragma unroll
      for (int nt = 0; nt < 2; ++nt) wf[f & 1][s][nt] = tr_frag(lds + woff[nt] + (f % W2S) * 4096 + s * 2048, 1024);
  };
  RW(0);
  static_for<0, 14>([&](auto fc) {
    constexpr int f = decltype(fc)::value;
    if constexpr (f == 14 - HPRE) {
      PINV(acc[0]); PINV(acc[1]); PINV(acc[2]); PINV(acc[3]);
      (void)poll_granule<8>(wsb, GW_OFF + ((nb * 2 + ((lane >> 2) & 1)) * 16 + wt * 4 + (lane & 3)) * 8, tag);
    }
    if constexpr (f == NPRE) {
      PINV(acc[0]); PINV(acc[1]); PINV(acc[2]); PINV(acc[3]);
      STAMP(11);
      (void)wait_lds_word<2>(lds + L_CTRL + 20, TRIG);
    }
    asm volatile("s_waitcnt lgkmcnt(0)" ::: "memory");
    SBAR();
    if constexpr (f + 1 < 14) {
      if constexpr (f + 1 >= W2S) wait_vm<ffn2_younger(f + 1, f)>();
      RW(f + 1);
    }
    SBAR();
#pragma unroll
    for (int u = 0; u < 8; ++u) {
      const int s = u >> 2, nt = (u >> 1) & 1, mt = u & 1;
      acc[nt * 2 + mt] = MFMA(wf[f & 1][s][nt], as_frag(hb[f % HPRE][mt * 2 + s]), acc[nt * 2 + mt]);
      SBAR();
      if (u < 4) {
        if constexpr (f + W2S < 14) glds16(rsW2, ring0 + (f % W2S) * 4096 + u * 1024, w2src, (fr(f + W2S) * 32 + u * 8) * DM * 2);
      } else {
        const int e = u == 4 ? 0 : u == 5 ? 2 : u == 6 ? 1 : 3;
        if constexpr (f + HPRE < 14) hb[f % HPRE][e] = ld16c(rsH, lo + (e & 1) * 1024, hbase + ((e >> 1) * 56 + fr(f + HPRE)) * 2048);
        else if constexpr (f + HPRE < 18) {
          const int x = (f + HPRE - 14) * 4 + u - 4;
          wo[x] = ld16c(rsWs, lo, WOF_OFF + ((nb * 2 + (x >> 3)) * 16 + wt * 4) * 2048 + (x & 7) * 1024);
        }
      }
      SBAR();
    }
    if constexpr (f < NPRE) { wait_vm<FTHR>(); __builtin_amdgcn_s_sleep(PACE2); }
    SBAR();
  });
  STAMP(7);
  __builtin_amdgcn_s_setprio(3);
#pragma unroll
  for (int i = 0; i < 16; ++i) { acc[0][i] *= og0; acc[1][i] *= og1; acc[2][i] *= og0; acc[3][i] *= og1; }
  __builtin_amdgcn_s_sleep(ODELAY);
  (void)poll_granule<POLL_A>(wsb, GO_OFF + ((mb * 2 + ((lane >> 2) & 1)) * 16 + wt * 4 + (lane & 3)) * 128, tag);
  STAMP(8);
  u32x4 of[16];
#pragma unroll
  for (int e = 0; e < 8; ++e)
#pragma unroll
    for (int mt = 0; mt < 2; ++mt) of[mt * 8 + e] = ld16c(rsWs, lo, OF_OFF + ((mb * 2 + mt) * 16 + wt * 4) * 2048 + e * 1024);
  SBAR();
#pragma unroll
  for (int e = 0; e < 8; ++e)
#pragma unroll
    for (int nt = 0; nt < 2; ++nt)
#pragma unroll
      for (int mt = 0; mt < 2; ++mt) acc[nt * 2 + mt] = MFMA(as_frag(wo[nt * 8 + e]), as_frag(of[mt * 8 + e]), acc[nt * 2 + mt]);
#pragma unroll
  for (int t = 0; t < 4; ++t) st_tile(lds + L_W2 + wt * (W2S * 4096) + t * 4096 + lo, acc[t]);
  STAMP(9);
  bar_lds();
  f32x16 a = f32x16{};
#pragma unroll
  for (int f = 0; f < 4; ++f) add_tile(a, lds + L_W2 + f * (W2S * 4096) + wt * 4096 + lo);
  unsigned short* dst = P.out + (size_t)((mb * 2 + (wt & 1)) * 32 + r) * DM + (nb * 2 + (wt >> 1)) * 32 + 8 * h;
#pragma unroll
  for (int k = 0; k < 2; ++k) {
    const auto x = __builtin_amdgcn_permlane32_swap(cvtpk(a[8 * k], a[8 * k + 1]), cvtpk(a[8 * k + 4], a[8 * k + 5]), false, false);
    const auto y = __builtin_amdgcn_permlane32_swap(cvtpk(a[8 * k + 2], a[8 * k + 3]), cvtpk(a[8 * k + 6], a[8 * k + 7]), false, false);
    *(u32x4*)(dst + 16 * k) = u32x4{x[0], y[0], x[1], y[1]};
  }
  STAMP(10);
  STAMP_FLUSH(P, 0);
}

extern __shared__ __attribute__((aligned(16))) unsigned char g_lds[];

__global__ __launch_bounds__(512, 2) void k_fused(Params P) {
  u64 pc;
  asm volatile("s_getpc_b64 %0" : "=s"(pc));
  const unsigned code_word = __builtin_amdgcn_raw_buffer_load_b32(mk_rsrc((const void*)(pc & ~63ull), CODE_PF),
      (threadIdx.x & 256) * 4096 + (((blockIdx.x >> 3) * 32 + ((threadIdx.x >> 3) & 24) + (threadIdx.x & 7)) * 64), 0, 0);
  SBAR();
  LAS unsigned char* lds = (LAS unsigned char*)g_lds;
  const int w = __builtin_amdgcn_readfirstlane(threadIdx.x >> 6);
  if (w < 4) role_a(P, lds, w, code_word);
  else role_f(P, lds, w - 4);
}

extern "C" void kernel_launch(void* const* d_in, const int* in_sizes, int n_in, void* d_out, int out_size, void* d_ws,
                              size_t ws_size, hipStream_t stream) {
  Params P;
  P.x = (const unsigned short*)d_in[0];
  P.wqkv = (const unsigned short*)d_in[1];
  P.wo = (const unsigned short*)d_in[2];
  P.w1 = (const unsigned short*)d_in[4];
  P.w2 = (const unsigned short*)d_in[5];
  P.gw = (const unsigned short*)d_in[6];
  P.out = (unsigned short*)d_out;
  P.ws = (unsigned char*)d_ws;
  (void)hipFuncSetAttribute((const void*)k_fused, hipFuncAttributeMaxDynamicSharedMemorySize, LDS_BYTES);
  k_fused<<<256, 512, LDS_BYTES, stream>>>(P);
}
```

```cpp
#define XSTAMP 0
#define XWAVE 1
#define XBLK 9
#include <hip/hip_runtime.h>
#include <hip/hip_bf16.h>
#include <stdint.h>

typedef short bf16x8 __attribute__((ext_vector_type(8)));
typedef short s16x4 __attribute__((ext_vector_type(4)));
typedef float f32x16 __attribute__((ext_vector_type(16)));
typedef float f32x4 __attribute__((ext_vector_type(4)));
typedef unsigned u32x4 __attribute__((ext_vector_type(4)));
typedef unsigned u32x2 __attribute__((ext_vector_type(2)));
#define LAS __attribute__((address_space(3)))
#define DI __device__ __forceinline__
#define MFMA(a, b, c) __builtin_amdgcn_mfma_f32_32x32x16_bf16((a), (b), (c), 0, 0, 0)
#define SBAR() __builtin_amdgcn_sched_barrier(0)

constexpr int SEQ = 2048, DM = 448, NH = 16, DH = 28, FF = 1792;
constexpr unsigned FL_OFF = 0;
#define SETB 1u
constexpr unsigned SET_BASE = SETB << 20, SET_STRIDE = 24u << 20;
#define GQS 16u
constexpr unsigned GQW = GQS == 16u ? 128u : 4u * GQS;
constexpr unsigned GQ_OFF = 0;
constexpr unsigned GH_OFF = GQ_OFF + 256 * GQW;
constexpr unsigned GO_OFF = GH_OFF + 256 * 128;
constexpr unsigned GW_OFF = GO_OFF + 1024 * 128;
constexpr unsigned GG_OFF = GW_OFF + 256 * 8;
constexpr unsigned QF_OFF = 524288;
constexpr unsigned KV_OFF = QF_OFF + 16 * 64 * 2048;
constexpr unsigned HF_OFF = KV_OFF + 16 * 64 * 4096;
constexpr unsigned WOF_OFF = HF_OFF + 64 * 56 * 2048;
constexpr unsigned OF_OFF = WOF_OFF + 14 * 16 * 2048;
constexpr unsigned WS_END = OF_OFF + 64 * 16 * 2048;
static_assert(GG_OFF + 2048 * 8 <= QF_OFF && WS_END <= SET_STRIDE, "workspace map");

constexpr int L_X = 0;
constexpr int L_WQ = 114688;
constexpr int L_W1 = 114688;
constexpr int L_CTRL = 163776;
constexpr int L_W2 = 0;
constexpr int L_CMB = 98304;
constexpr int LDS_BYTES = 163840;

constexpr float QSCALE = 0.18898223650461362f * 1.4426950408889634f;
constexpr float LOG2E = 1.4426950408889634f;
constexpr float SAFE_BOUND = 60.0f;
constexpr unsigned SPIN_CAP = 1u << 18;
#define ROT1(rb, j) ((((rb) + (j)) & 1) * 3)
#define KVD 4
#define ROTA 0
#define ROT2 0
#define PDELAY 16
#define ODELAY 28
#define FDELAY 18
#define CODE_PF 44032
#define MPF 1
#define POLL_A 4

struct Params {
  const unsigned short* x;
  const unsigned short* wqkv;
  const unsigned short* wo;
  const unsigned short* w1;
  const unsigned short* w2;
  const unsigned short* gw;
  unsigned short* out;
  unsigned char* ws;
};
typedef unsigned long long u64;
typedef __attribute__((address_space(1))) u64 gu64;

DI unsigned cvtpk(float lo, float hi) {
  typedef __bf16 bf2 __attribute__((ext_vector_type(2)));
  typedef float f2 __attribute__((ext_vector_type(2)));
  f2 v = {lo, hi};
  bf2 r = __builtin_convertvector(v, bf2);
  return __builtin_bit_cast(unsigned, r);
}
DI bf16x8 pack8(const f32x16& x, int s) {
  u32x4 p;
  p[0] = cvtpk(x[8 * s + 0], x[8 * s + 1]);
  p[1] = cvtpk(x[8 * s + 2], x[8 * s + 3]);
  p[2] = cvtpk(x[8 * s + 4], x[8 * s + 5]);
  p[3] = cvtpk(x[8 * s + 6], x[8 * s + 7]);
  return __builtin_bit_cast(bf16x8, p);
}
DI float swap_half(float v) {
  auto r = __builtin_amdgcn_permlane32_swap(__float_as_uint(v), __float_as_uint(v), false, false);
  return (threadIdx.x & 32) ? __uint_as_float(r[0]) : __uint_as_float(r[1]);
}
template <int CTRL> DI float dpp_mov(float v) {
  return __uint_as_float(__builtin_amdgcn_update_dpp(__float_as_uint(v), __float_as_uint(v), CTRL, 0xf, 0xf, false));
}
DI float row16_max(float v) {
  v = fmaxf(v, dpp_mov<0x128>(v));
  v = fmaxf(v, dpp_mov<0x124>(v));
  v = fmaxf(v, dpp_mov<0x122>(v));
  v = fmaxf(v, dpp_mov<0x121>(v));
  return v;
}
DI unsigned row16_umax(unsigned v) {
  v = max(v, (unsigned)__builtin_amdgcn_update_dpp(v, v, 0x128, 0xf, 0xf, false));
  v = max(v, (unsigned)__builtin_amdgcn_update_dpp(v, v, 0x124, 0xf, 0xf, false));
  v = max(v, (unsigned)__builtin_amdgcn_update_dpp(v, v, 0x122, 0xf, 0xf, false));
  v = max(v, (unsigned)__builtin_amdgcn_update_dpp(v, v, 0x121, 0xf, 0xf, false));
  return v;
}
DI float rdlane(float v, int l) { return __uint_as_float(__builtin_amdgcn_readlane(__float_as_uint(v), l)); }
DI s16x4 tr_read(const LAS unsigned char* p) {
  typedef short v4i16_t __attribute__((ext_vector_type(4)));
  return __builtin_bit_cast(s16x4, __builtin_amdgcn_ds_read_tr16_b64_v4i16((LAS v4i16_t*)p));
}
DI bf16x8 tr_frag(const LAS unsigned char* p, int row_stride4) {
  s16x4 lo = tr_read(p), hi = tr_read(p + row_stride4);
  return __builtin_shufflevector(lo, hi, 0, 1, 2, 3, 4, 5, 6, 7);
}
DI __amdgpu_buffer_rsrc_t mk_rsrc(const void* p, unsigned bytes) {
  return __builtin_amdgcn_make_buffer_rsrc((void*)p, (short)0, (int)bytes, 0x00020000);
}
DI void glds16(__amdgpu_buffer_rsrc_t rs, unsigned dst, unsigned voff, unsigned soff) {
  asm volatile("s_mov_b32 m0, %0\n\ts_nop 0\n\tbuffer_load_dwordx4 %1, %2, %3 offen lds" ::"s"(dst), "v"(voff), "s"(rs), "s"(soff) : "memory");
}
DI u32x4 ld16(__amdgpu_buffer_rsrc_t rs, unsigned voff, unsigned soff) { return __builtin_amdgcn_raw_buffer_load_b128(rs, voff, soff, 0); }
DI u32x4 ld16c(__amdgpu_buffer_rsrc_t rs, unsigned voff, unsigned soff) { return __builtin_amdgcn_raw_buffer_load_b128(rs, voff, soff, 16); }
DI void st16(__amdgpu_buffer_rsrc_t rs, u32x4 v, unsigned voff, unsigned soff) { __builtin_amdgcn_raw_buffer_store_b128(v, rs, voff + soff, 0, 16); }
DI bf16x8 as_frag(u32x4 v) { return __builtin_bit_cast(bf16x8, v); }
DI u32x4 as_u4(bf16x8 v) { return __builtin_bit_cast(u32x4, v); }
template <int N> DI void wait_bar() { asm volatile("s_waitcnt vmcnt(%0) lgkmcnt(0)\n\ts_barrier" ::"n"(N) : "memory"); }

template <int V> struct IC { static constexpr int value = V; };
template <int I, int N, class Fn> DI void static_for(Fn&& fn) {
  if constexpr (I < N) { fn(IC<I>{}); static_for<I + 1, N>(fn); }
}
DI void drain() { asm volatile("s_waitcnt vmcnt(0)" ::: "memory"); }
template <int N> DI void wait_vm() { asm volatile("s_waitcnt vmcnt(%0)" ::"n"(N) : "memory"); }
DI void put_granule(unsigned char* ws, unsigned off, u64 tag16, unsigned payload) {
  __hip_atomic_store((gu64*)(ws + off), tag16 | (u64)(payload & 0xffffu), __ATOMIC_RELAXED, __HIP_MEMORY_SCOPE_AGENT);
}
template <int SLEEP> DI unsigned poll_granule(unsigned char* ws, unsigned off, u64 tag16) {
  u64 v = ~tag16;
  for (unsigned spins = 0;; ++spins) {
    if (v - tag16 > 0xffffull) v = __hip_atomic_load((gu64*)(ws + off), __ATOMIC_RELAXED, __HIP_MEMORY_SCOPE_AGENT);
    if (__all(v - tag16 <= 0xffffull) || spins > SPIN_CAP) break;
    __builtin_amdgcn_s_sleep(SLEEP);
  }
  __builtin_amdgcn_fence(__ATOMIC_ACQUIRE, "wavefront");
  return (unsigned)v & 0xffffu;
}
DI unsigned wait_lds_word(LAS unsigned char* p, unsigned want) {
  unsigned v = 0;
#pragma nounroll
  for (unsigned spins = 0; spins < (1u << 24); ++spins) {
    v = __builtin_amdgcn_readfirstlane(__hip_atomic_load((LAS unsigned*)p, __ATOMIC_RELAXED, __HIP_MEMORY_SCOPE_WORKGROUP));
    if (v >= want) break;
    __builtin_amdgcn_s_sleep(1);
  }
  asm volatile("" ::: "memory");
  return v;
}
template <int N> DI u64 wait_bar_epoch(const unsigned char* p) {
  u64 ep;
  asm volatile("s_load_dwordx2 %0, %1, 0x0\n\ts_waitcnt vmcnt(%2) lgkmcnt(0)\n\ts_barrier" : "=&s"(ep) : "s"(p), "n"(N) : "memory");
  return ep;
}
DI void bar_lds() { asm volatile("s_waitcnt lgkmcnt(0)\n\ts_barrier" ::: "memory"); }
DI void st_tile(LAS unsigned char* p, const f32x16& a) {
#pragma unroll
  for (int g = 0; g < 4; ++g) *(LAS f32x4*)(p + g * 1024) = f32x4{a[4 * g], a[4 * g + 1], a[4 * g + 2], a[4 * g + 3]};
}
DI void add_tile(f32x16& a, const LAS unsigned char* p) {
#pragma unroll
  for (int g = 0; g < 4; ++g) {
    const f32x4 v = *(const LAS f32x4*)(p + g * 1024);
    a[4 * g] += v[0]; a[4 * g + 1] += v[1]; a[4 * g + 2] += v[2]; a[4 * g + 3] += v[3];
  }
}

#define STAMP_DECL unsigned long long rt0_ = __builtin_amdgcn_s_memrealtime(), rtx_ = rt0_
#define STAMP(id) do { if ((id) == XSTAMP) { SBAR(); asm volatile("s_memrealtime %0\n\ts_waitcnt lgkmcnt(0)" : "=s"(rtx_) :: "memory"); SBAR(); } } while (0)
#define QSTAMP(id) do {} while (0)
#define RSTAMP(id) do {} while (0)
#define STAMP_FLUSH(P, ph) do { if (blockIdx.x == XBLK && __builtin_amdgcn_readfirstlane(threadIdx.x >> 6) == XWAVE) { const unsigned long long until_ = rt0_ + 5000 + 4 * (rtx_ - rt0_); \
  while (__builtin_amdgcn_s_memrealtime() < until_) __builtin_amdgcn_s_sleep(2); } } while (0)

DI void lds_arrive(LAS unsigned char* p) {
  asm volatile("s_mov_b64 exec, 1\n\tds_add_u32 %0, %1\n\ts_mov_b64 exec, -1" ::"v"((unsigned)(uintptr_t)p), "v"(1u) : "memory");
}

#define PINV(x) asm volatile("" : "+v"(x))
#define PINS(x) asm volatile("" : "+s"(x))
DI float sumsq(const u32x4& a, const u32x4& b) {
  typedef __bf16 bf2 __attribute__((ext_vector_type(2)));
  float n0 = 0.f, n1 = 0.f;
#pragma unroll
  for (int i = 0; i < 4; ++i) {
    n0 = __builtin_amdgcn_fdot2_f32_bf16(__builtin_bit_cast(bf2, a[i]), __builtin_bit_cast(bf2, a[i]), n0, false);
    n1 = __builtin_amdgcn_fdot2_f32_bf16(__builtin_bit_cast(bf2, b[i]), __builtin_bit_cast(bf2, b[i]), n1, false);
  }
  return n0 + n1;
}

DI void role_a(const Params& P, LAS unsigned char* lds, const int wt, const unsigned code_word) {
  const int lane = threadIdx.x & 63, r = lane & 31, h = lane >> 5;
  const int b = blockIdx.x, xcd = b & 7, idx = b >> 3;
  const unsigned lo = lane * 16;
  STAMP_DECL;
  STAMP(0);
  __builtin_amdgcn_s_setprio(2);
  unsigned xtouch = 0;
  if (wt < 2) xtouch = __builtin_amdgcn_raw_buffer_load_b32(wt ? mk_rsrc(P.wqkv, DM * 3 * DM * 2) : mk_rsrc(P.x, SEQ * DM * 2),
                                                            (unsigned)(wt ? b * 37 + (lane < 37 ? lane : 0) : b * 56 + (lane < 56 ? lane : 0)) * 128, 0, 0);
  SBAR();
  f32x16 acc[4];
  const int rb = (xcd & 1) * 8 + (idx & 7), j = (xcd >> 1) * 4 + (idx >> 3);
  const int head = xcd * 2 + (idx & 1), qb = idx >> 1;
  unsigned sto[3];
#pragma unroll
  for (int ct = 0; ct < 3; ++ct) {
    const int u = 3 * j + ct, part = u >> 4, t = (u & 15) * 64 + rb * 4 + wt;
    sto[ct] = lo + (part == 0 ? QF_OFF + t * 2048 : KV_OFF + t * 4096 + (part - 1) * 2048);
    PINV(sto[ct]);
  }
  unsigned padv = r == DH ? 0x3f803f80u : 0u;
  PINV(padv);
  const int gq = head / 3, gk = (16 + head) / 3, gvv = (32 + head) / 3;
  unsigned pub_off = GQ_OFF + (j * 16 + rb) * GQW + wt * GQS + (lane & 1) * 8;
  unsigned poll_off = GQ_OFF + (h ? gq * 16 + qb : (lane < 16 ? gk : gvv) * 16 + wt * 4 + ((lane >> 2) & 3)) * GQW + (lane & 3) * GQS + h * 8;
  unsigned chk_off = GQ_OFF + (gk * 16 + (lane >> 2)) * GQW + (lane & 3) * GQS;
  unsigned g_off = GG_OFF + ((qb * 4 + wt) * 32 + r) * 8;
  unsigned o_off = lo + OF_OFF + ((qb * 4 + wt) * 16 + head) * 2048;
  PINV(pub_off); PINV(poll_off); PINV(chk_off); PINV(g_off); PINV(o_off);
  unsigned kvbase = KV_OFF + (head * 64 + wt * 16) * 4096, go_off = GO_OFF + ((qb * 4 + wt) * 16 + head) * 128;
  unsigned qofs[4];
#pragma unroll
  for (int t = 0; t < 4; ++t) { qofs[t] = QF_OFF + (unsigned)(head * 64 + qb * 4 + ((wt + 1 + t) & 3)) * 2048; PINS(qofs[t]); }
  PINS(kvbase); PINS(go_off);
  unsigned xoff[4];
#pragma unroll
  for (int ks = 0; ks < 4; ++ks) xoff[ks] = (unsigned)(L_X + wt * 4096 + r * 128 + (((2 * ks + h) ^ ((r >> 1) & 7)) << 4));
  const unsigned wqoff = (unsigned)(L_WQ + (8 * h + ((lane & 15) >> 2)) * 192 + ((lane >> 4) & 1) * 32 + (lane & 3) * 8);
  u64 ep;
  asm volatile("s_load_dwordx2 %0, %1, 0x0\n\ts_waitcnt lgkmcnt(0)" : "=&s"(ep) : "s"(P.ws + FL_OFF) : "memory");
  u64 tag16 = (ep + 1) << 16;
  unsigned char* wsb = P.ws + SET_BASE + (((unsigned)ep + 1u) & 3u) * SET_STRIDE;
  PINS(tag16); PINS(wsb);
  const __amdgpu_buffer_rsrc_t rsWs = mk_rsrc(wsb, WS_END);
#pragma unroll
  for (int i = 0; i < 3; ++i) acc[i] = f32x16{};
  unsigned wtouch = 0;
  auto kloop = [&](auto nvc) {
    constexpr int NV = decltype(nvc)::value;
    bf16x8 xf[4], wf[4][3];
    auto mm = [&](int ks, int ct) { acc[ct] = ct >= 3 - NV ? MFMA(xf[ks], wf[ks][ct], acc[ct]) : MFMA(wf[ks][ct], xf[ks], acc[ct]); };
    auto step = [&](int c, int ks, bool domm) {
      const int km = (ks + 2) & 3;
#pragma unroll
      for (int ct = 0; ct < 3; ++ct) {
        if (domm) mm(km, ct);
        SBAR();
        if (ct == 0) xf[ks] = *(const LAS bf16x8*)(lds + xoff[ks] + c * 16384);
        wf[ks][ct] = tr_frag(lds + wqoff + (c & 3) * 12288 + ks * 16 * 192 + ct * 56, 4 * 192);
        SBAR();
      }
    };
#pragma unroll
    for (int c = 0; c < 7; ++c) {
      bar_lds();
      if (c == 0) STAMP(1);
      if (c == MPF) {
        const unsigned l = (unsigned)(b * 128 + (wt & 1) * 64 + lane) * 128;
        if (wt < 2) wtouch = l < DM * FF * 2 ? __builtin_amdgcn_raw_buffer_load_b32(mk_rsrc(P.w1, DM * FF * 2), l, 0, 0)
                           : l < 2 * DM * FF * 2 ? __builtin_amdgcn_raw_buffer_load_b32(mk_rsrc(P.w2, DM * FF * 2), l - DM * FF * 2, 0, 0)
                                                 : __builtin_amdgcn_raw_buffer_load_b32(mk_rsrc(P.wo, DM * DM * 2), l - 2 * DM * FF * 2, 0, 0);
        SBAR();
      }
      QSTAMP(1 + c);
      step(c, 0, c > 0);
      step(c, 1, c > 0);
      step(c, 2, true);
      step(c, 3, true);
    }
    bar_lds();
    QSTAMP(8);
#pragma unroll
    for (int ct = 0; ct < 3; ++ct) { mm(2, ct); mm(3, ct); SBAR(); }
  };
  auto epi = [&](auto nqc, auto nvc) {
    constexpr int NQ = decltype(nqc)::value, NV = decltype(nvc)::value;
    STAMP(2);
    asm volatile("" ::"v"(code_word), "v"(wtouch), "v"(xtouch));
    u32x4 t0[3], t1[3];
#pragma unroll
    for (int ct = 0; ct < 3; ++ct) {
      if (ct < NQ) acc[ct] *= QSCALE;
      t0[ct] = as_u4(pack8(acc[ct], 0));
      if (ct >= 3 - NV) {
#pragma unroll
        for (int i = 0; i < 4; ++i) t0[ct][i] = r < DH ? t0[ct][i] : padv;
      }
      st16(rsWs, t0[ct], sto[ct], 0);
      t1[ct] = as_u4(pack8(acc[ct], 1));
      if (ct >= 3 - NV) {
#pragma unroll
        for (int i = 0; i < 4; ++i) t1[ct][i] = r < DH ? t1[ct][i] : padv;
      } else {
        t1[ct][2] = h ? (ct < NQ ? 0u : 0x3f803f80u) : t1[ct][2];
        t1[ct][3] = h ? (ct < NQ ? 0u : 0x00003f80u) : t1[ct][3];
      }
      st16(rsWs, t1[ct], sto[ct], 1024);
      SBAR();
    }
    STAMP(8);
    unsigned mq = 0, mk = 0;
#pragma unroll
    for (int ct = 0; ct < 3; ++ct) {
      const unsigned n = __float_as_uint(sumsq(t0[ct], t1[ct])) & 0x7fffffffu;
      if (ct < NQ) mq = max(mq, n); else mk = max(mk, n);
    }
    auto bound = [&](unsigned m) {
      m = row16_umax(m);
      const float lo2 = __uint_as_float(max((unsigned)__builtin_amdgcn_readlane(m, 0), (unsigned)__builtin_amdgcn_readlane(m, 16)));
      const float hi2 = __uint_as_float(max((unsigned)__builtin_amdgcn_readlane(m, 32), (unsigned)__builtin_amdgcn_readlane(m, 48)));
      return ((__float_as_uint(lo2 + hi2) >> 16) + 1u) & 0xffffu;
    };
    const unsigned pq = NQ > 0 ? bound(mq) : 0u, pk = NQ < 3 ? bound(mk) : 0u;
    u64 gran = tag16 | ((lane & 1) ? pq : pk);
    PINV(gran);
    drain();
    STAMP(9);
    if (lane < 2) __hip_atomic_store((gu64*)(wsb + pub_off), gran, __ATOMIC_RELAXED, __HIP_MEMORY_SCOPE_AGENT);
  };
  if (j < 10) {
    kloop(IC<0>{});
    if (j < 5) epi(IC<3>{}, IC<0>{});
    else if (j == 5) epi(IC<1>{}, IC<0>{});
    else epi(IC<0>{}, IC<0>{});
  } else if (j == 10) {
    kloop(IC<1>{});
    epi(IC<0>{}, IC<1>{});
  } else {
    kloop(IC<3>{});
    epi(IC<0>{}, IC<3>{});
  }
  STAMP(3);
  __builtin_amdgcn_s_sleep(PDELAY);
  const unsigned pay = poll_granule<POLL_A>(wsb, poll_off, tag16);
  STAMP(4);
  u32x4 q[4][2];
  LAS unsigned char* const cmb = lds + L_CMB + wt * 12288 + lo;
  u64 gv = 0;
  unsigned thr = 0;
#pragma nounroll
  for (int pass = 0;; ++pass) {
    u32x4 kv[KVD][4];
    kv[0][0] = ld16c(rsWs, lo, kvbase);
    SBAR();
    if (pass == 0) { q[0][0] = ld16c(rsWs, lo, qofs[0]); q[0][1] = ld16c(rsWs, lo + 1024, qofs[0]); }
    SBAR();
    kv[0][1] = ld16c(rsWs, lo + 1024, kvbase);
    SBAR();
    if (pass == 0) {
#pragma unroll
      for (int t = 1; t < 4; ++t) { q[t][0] = ld16c(rsWs, lo, qofs[t]); q[t][1] = ld16c(rsWs, lo + 1024, qofs[t]); SBAR(); }
    }
#pragma unroll
    for (int e = 2; e < 4 * (KVD - 1); ++e) { kv[e >> 2][e & 3] = ld16c(rsWs, lo + (e & 3) * 1024, kvbase + (e >> 2) * 4096); SBAR(); }
#pragma unroll
    for (int i = 0; i < 4; ++i) acc[i] = f32x16{};
    {
      const float q2 = __uint_as_float(__builtin_amdgcn_readlane(row16_umax(pay), 32) << 16);
      const float lim = SAFE_BOUND * SAFE_BOUND * __builtin_amdgcn_rcpf(q2);
      thr = lim < 1e30f ? __float_as_uint(lim) >> 16 : (lim >= 1e30f ? 0x7000u : 0u);
      PINV(thr);
    }
    __builtin_amdgcn_s_setprio(1);
    f32x16 sc = MFMA(as_frag(kv[0][0]), as_frag(q[0][0]), f32x16{});
    sc = MFMA(as_frag(kv[0][1]), as_frag(q[0][1]), sc);
    STAMP(5);
    u32x4 p0, p1 = {0, 0, 0, 0};
    float e0 = 0.f, e1 = 0.f, e2 = 0.f, e3 = 0.f;
    u64 ck = 0, cv = 0;
#define EXP4(i) do { e0 = __builtin_amdgcn_exp2f(sc[i]); e1 = __builtin_amdgcn_exp2f(sc[(i) + 1]); e2 = __builtin_amdgcn_exp2f(sc[(i) + 2]); e3 = __builtin_amdgcn_exp2f(sc[(i) + 3]); \
    asm volatile("" : "+v"(e0), "+v"(e1), "+v"(e2), "+v"(e3)); SBAR(); } while (0)
#define CVT2(P, i) do { P[i] = cvtpk(e0, e1); P[(i) + 1] = cvtpk(e2, e3); asm volatile("" : "+v"(P)); SBAR(); } while (0)
#define PART(t, g) do { *(LAS f32x4*)(cmb + (t) * 4096 + (g) * 1024) = f32x4{acc[t][4 * (g)], acc[t][4 * (g) + 1], acc[t][4 * (g) + 2], acc[t][4 * (g) + 3]}; SBAR(); } while (0)
    static_for<0, 64>([&](auto ic) {
      constexpr int i = decltype(ic)::value;
      constexpr int kt = i >> 2, t = i & 3, kn_ = (i + 1) >> 2, tn = (i + 1) & 3, kp = (i - 1) >> 2, tp = (i - 1) & 3;
      constexpr int kl = kt + KVD - 1;
      if (i == 24) STAMP(11);
      if (i == 56) (void)wait_lds_word(lds + L_CTRL, 4);
      f32x16 sn;
      SBAR();
      if (i < 63) sn = MFMA(as_frag(kv[kn_ % KVD][0]), as_frag(q[tn][0]), f32x16{});
      SBAR();
      if constexpr (i >= 62) PART(i - 62, 0);
      if (i > 0) CVT2(p1, 2);
      EXP4(0);
      if (i > 0) acc[tp] = MFMA(as_frag(kv[(kp + KVD) % KVD][3]), as_frag(p1), acc[tp]);
      SBAR();
      if (kl < 16) kv[kl % KVD][t] = ld16c(rsWs, lo + t * 1024, kvbase + kl * 4096);
      if (i == 52) gv = __hip_atomic_load((gu64*)(wsb + g_off), __ATOMIC_RELAXED, __HIP_MEMORY_SCOPE_AGENT);
      if (i == 53) ck = __hip_atomic_load((gu64*)(wsb + chk_off), __ATOMIC_RELAXED, __HIP_MEMORY_SCOPE_AGENT);
      if (i == 54) cv = __hip_atomic_load((gu64*)(wsb + (gvv - gk) * 16 * GQW + chk_off), __ATOMIC_RELAXED, __HIP_MEMORY_SCOPE_AGENT);
      SBAR();
      if constexpr (i >= 62) PART(i - 62, 1);
      CVT2(p0, 0);
      EXP4(4);
      if (i < 63) sn = MFMA(as_frag(kv[kn_ % KVD][1]), as_frag(q[tn][1]), sn);
      SBAR();
      if constexpr (i >= 62) PART(i - 62, 2);
      CVT2(p0, 2);
      EXP4(8);
      acc[t] = MFMA(as_frag(kv[kt % KVD][2]), as_frag(p0), acc[t]);
      SBAR();
      if constexpr (i >= 62) PART(i - 62, 3);
      CVT2(p1, 0);
      EXP4(12);
      if (i < 63) sc = sn;
    });
    CVT2(p1, 2);
    acc[3] = MFMA(as_frag(kv[15 % KVD][3]), as_frag(p1), acc[3]);
    SBAR();
    PART(2, 0); PART(2, 1); PART(2, 2); PART(2, 3);
#undef EXP4
#undef CVT2
#undef PART
    __builtin_amdgcn_s_setprio(2);
    if (__builtin_expect(__all(ck - tag16 <= (u64)thr && cv - tag16 <= 0x7000ull && gv - tag16 <= 0xffffull), 1)) break;
    gv = poll_granule<2>(wsb, g_off, tag16);
    if (pass) break;
    const unsigned pk = poll_granule<2>(wsb, chk_off, tag16), pv = poll_granule<2>(wsb, (gvv - gk) * 16 * GQW + chk_off, tag16);
    if (__all(pk <= thr && pv <= 0x7000u)) break;
    float mx[4];
#pragma unroll
    for (int t = 0; t < 4; ++t) mx[t] = -INFINITY;
    for (int kt = 0; kt < 64; ++kt) {
      const u32x4 k0 = ld16c(rsWs, lo, KV_OFF + (head * 64 + kt) * 4096), k1 = ld16c(rsWs, lo + 1024, KV_OFF + (head * 64 + kt) * 4096);
#pragma unroll
      for (int t = 0; t < 4; ++t) {
        f32x16 s = MFMA(as_frag(k0), as_frag(q[t][0]), f32x16{});
        s = MFMA(as_frag(k1), as_frag(q[t][1]), s);
#pragma unroll
        for (int i = 0; i < 16; ++i) mx[t] = fmaxf(mx[t], s[i]);
      }
    }
#pragma unroll
    for (int t = 0; t < 4; ++t) {
      const float nm = -fmaxf(mx[t], swap_half(mx[t]));
      const unsigned hi = __float_as_uint(nm) & 0xffff0000u;
      const float r1 = nm - __uint_as_float(hi);
      const unsigned mid = __float_as_uint(r1) & 0xffff0000u;
      const float r2 = r1 - __uint_as_float(mid);
      if (h) { q[t][1][2] = (hi >> 16) | mid; q[t][1][3] = __float_as_uint(r2) >> 16; }
    }
  }
  STAMP(6);
  asm volatile("s_waitcnt lgkmcnt(0)" ::: "memory");
  lds_arrive(lds + L_CTRL + 20);
  (void)wait_lds_word(lds + L_CTRL + 20, 4);
  f32x16 o = acc[3];
#pragma unroll
  for (int u = 0; u < 3; ++u) add_tile(o, lds + L_CMB + ((wt + 3 - u) & 3) * 12288 + u * 4096 + lo);
  const auto lsw = __builtin_amdgcn_permlane32_swap(__float_as_uint(o[12]), __float_as_uint(o[12]), false, false);
  const float inv = __uint_as_float((unsigned)gv << 16) * __builtin_amdgcn_rcpf(__uint_as_float(lsw[1]));
  o *= inv;
  st16(rsWs, as_u4(pack8(o, 0)), o_off, 0);
  u32x4 o1 = as_u4(pack8(o, 1));
  o1[2] = h ? 0u : o1[2];
  o1[3] = h ? 0u : o1[3];
  st16(rsWs, o1, o_off, 1024);
  STAMP(7);
  drain();
  if (lane == 0) __hip_atomic_store((gu64*)(wsb + go_off), tag16, __ATOMIC_RELAXED, __HIP_MEMORY_SCOPE_AGENT);
  STAMP(10);
  STAMP_FLUSH(P, 0);
}

constexpr int W2S = 6;
constexpr int HPRE = 5;
#define LEA 5
#define LEB 11
#define LE0 16
#define LU0 3
#define F1LA 2
#define PACE 4
#define PACE2 2
constexpr int ffn2_ops(int f) { return (f + W2S < 14 ? 4 : 0) + 4; }
constexpr int ffn2_younger(int f, int upto) {
  int n = 4;
  for (int i = f - W2S + 1; i < upto; ++i) n += ffn2_ops(i);
  return n;
}

DI void role_f(const Params& P, LAS unsigned char* lds, const int wt) {
  u64 ep;
  const int lane = threadIdx.x & 63, r = lane & 31, h = lane >> 5;
  const int b = blockIdx.x, xcd = b & 7, idx = b >> 3;
  const int rb = (xcd & 1) * 8 + (idx & 7), j = (xcd >> 1) * 4 + (idx >> 3);
  const unsigned lo = lane * 16, lds0 = (unsigned)(uintptr_t)lds;
  const bool ffn = j < 14;
  STAMP_DECL;
  STAMP(0);
  f32x16 acc[4];
  u32x4 wl[28];
  const __amdgpu_buffer_rsrc_t rsL = mk_rsrc(ffn ? P.w1 : P.gw, ffn ? DM * FF * 2 : DM * 2);
  const unsigned lsrc = ffn ? (unsigned)((lane >> 2) * (FF * 2) + (j * 4 + wt) * 64 + (lane & 3) * 16) : (unsigned)(h * 16);
  const unsigned lstep = ffn ? 16 * FF * 2 : 32;
  const int rot = ROT1(rb, j);
  auto kc = [&](int c) { const int v = c + rot; return v >= 7 ? v - 7 : v; };
  auto L = [&](int u0, int u1) {
#pragma unroll
    for (int u = u0; u < u1; ++u) wl[u] = ld16(rsL, lsrc, (kc(u >> 2) * 4 + (u & 3)) * lstep);
    SBAR();
  };
  {
    const __amdgpu_buffer_rsrc_t rsX = mk_rsrc(P.x, SEQ * DM * 2);
    const __amdgpu_buffer_rsrc_t rsW = mk_rsrc(P.wqkv, DM * 3 * DM * 2);
    unsigned xsrc[2], wsrc[3];
#pragma unroll
    for (int i = 0; i < 2; ++i) {
      const int row = 32 * wt + 8 * i + (lane >> 3), ch = (lane & 7) ^ ((row >> 1) & 7);
      xsrc[i] = (unsigned)((rb * 128 + row) * (DM * 2) + ch * 16);
    }
#pragma unroll
    for (int i = 0; i < 3; ++i) {
      const int p = 64 * (3 * wt + i) + lane, kr = p / 12, ch = p % 12;
      wsrc[i] = (unsigned)(kr * (3 * DM * 2) + j * 168 + (ch < 11 ? ch : 10) * 16);
    }
    auto X = [&](int c) {
#pragma unroll
      for (int i = 0; i < 4; ++i) glds16(rsX, lds0 + L_X + c * 16384 + (4 * wt + i) * 1024, xsrc[i & 1], kc(c) * 128 + (i >> 1) * 16 * DM * 2);
    };
    auto W = [&](int c) {
#pragma unroll
      for (int i = 0; i < 3; ++i) glds16(rsW, lds0 + L_WQ + (c & 3) * 12288 + (3 * wt + i) * 1024, wsrc[i], kc(c) * 64 * 3 * DM * 2);
    };
    X(0); W(0); X(1); W(1); X(2);
    QSTAMP(1);
    wait_bar<11>();
    STAMP(1);
    W(2); X(3); W(3);
    QSTAMP(2);
    wait_bar<14>();
    X(4); W(4);
    QSTAMP(3);
    wait_bar<14>();
    X(5); W(5);
    QSTAMP(4);
    wait_bar<14>();
    X(6); W(6);
    QSTAMP(5);
    wait_bar<14>();
    if (wt == 0 && lane < 8) *(LAS unsigned*)(lds + L_CTRL + lane * 4) = 0u;
    L(0, LEA);
    QSTAMP(6);
    ep = wait_bar_epoch<7 + LEA>(P.ws + FL_OFF);
    L(LEA, LEB);
    QSTAMP(7);
    wait_bar<LEB>();
    L(LEB, LE0);
    QSTAMP(8);
    bar_lds();
    __builtin_amdgcn_s_sleep(FDELAY);
  }
  STAMP(2);
  const u64 tag = (ep + 1) << 16;
  unsigned char* const wsb = P.ws + SET_BASE + (((unsigned)ep + 1u) & 3u) * SET_STRIDE;
  const __amdgpu_buffer_rsrc_t rsWs = mk_rsrc(wsb, WS_END);
  unsigned xoff[4];
#pragma unroll
  for (int ks = 0; ks < 4; ++ks) xoff[ks] = (unsigned)(L_X + r * 128 + (((2 * ks + h) ^ ((r >> 1) & 7)) << 4));
  if (ffn) {
    LAS unsigned char* ring = lds + L_W1 + wt * 8192;
    const unsigned troff = (unsigned)((8 * h + ((lane & 15) >> 2)) * 64 + ((lane >> 4) & 1) * 32 + (lane & 3) * 8);
#pragma unroll
    for (int i = 0; i < 4; ++i) acc[i] = f32x16{};
    RSTAMP(1);
    constexpr int LA = F1LA;
    bf16x8 xf[LA + 1][4], wf[LA + 1];
    auto wr = [&](int u) { *(LAS u32x4*)(ring + (u & 7) * 1024 + lo) = wl[u]; };
    auto rdw = [&](int u) { wf[u % (LA + 1)] = tr_frag(ring + (u & 7) * 1024 + troff, 4 * 64); };
    auto rdx = [&](int u, int t) { xf[u % (LA + 1)][t] = *(const LAS bf16x8*)(lds + xoff[u & 3] + (u >> 2) * 16384 + t * 4096); };
#pragma unroll
    for (int u = 0; u < LA + 2; ++u) wr(u);
#pragma unroll
    for (int u = 0; u < LA; ++u) {
      rdw(u);
#pragma unroll
      for (int t = 0; t < 4; ++t) rdx(u, t);
    }
    SBAR();
#pragma unroll
    for (int u = 0; u < 28; ++u) {
      if ((u & 3) == 0) RSTAMP(2 + (u >> 2));
#pragma unroll
      for (int t = 0; t < 4; ++t) {
        if (u + LA < 28) {
          if (t == 1) rdw(u + LA);
          rdx(u + LA, t);
          if (t == 3 && u + LA + 2 < 28) wr(u + LA + 2);
        }
        SBAR();
        acc[t] = MFMA(wf[u % (LA + 1)], xf[u % (LA + 1)][t], acc[t]);
        SBAR();
        if (u >= LU0 && LE0 + (u - LU0) * 4 + t < 28) L(LE0 + (u - LU0) * 4 + t, LE0 + (u - LU0) * 4 + t + 1);
      }
    }
    lds_arrive(lds + L_CTRL);
    RSTAMP(9);
    STAMP(3);
    __builtin_amdgcn_s_setprio(3);
#pragma unroll
    for (int t = 0; t < 4; ++t) {
      f32x16 z = acc[t];
#pragma unroll
      for (int i = 0; i < 16; ++i) z[i] = z[i] * __builtin_amdgcn_rcpf(1.f + __builtin_amdgcn_exp2f(-LOG2E * z[i]));
      const unsigned so = HF_OFF + ((rb * 4 + t) * 56 + j * 4 + wt) * 2048;
      st16(rsWs, as_u4(pack8(z, 0)), lo, so);
      st16(rsWs, as_u4(pack8(z, 1)), lo + 1024, so);
    }
    drain();
    if (lane == 0) put_granule(wsb, GH_OFF + (rb * 16 + j) * 128 + wt * 8, tag, 0);
  } else {
    __builtin_amdgcn_s_setprio(3);
    if (j == 14) {
      L(LE0, 28);
      acc[0] = f32x16{};
#pragma unroll
      for (int u = 0; u < 28; ++u) {
        const bf16x8 xf = *(const LAS bf16x8*)(lds + xoff[u & 3] + (u >> 2) * 16384 + wt * 4096);
        acc[0] = MFMA(as_frag(wl[u]), xf, acc[0]);
      }
      const float g = __builtin_amdgcn_rcpf(1.f + __builtin_amdgcn_exp2f(-LOG2E * acc[0][0]));
      if (h == 0) put_granule(wsb, GG_OFF + ((rb * 4 + wt) * 32 + r) * 8, tag, cvtpk(g, 0.f));
    }
    lds_arrive(lds + L_CTRL);
    const __amdgpu_buffer_rsrc_t rsO = mk_rsrc(P.wo, DM * DM * 2);
    const int id0 = ((j - 14) * 16 + rb) * 4 + wt;
    for (int id = id0; id < 224; id += 128) {
      const int nt = id >> 4, hd = id & 15;
      unsigned rp[16];
#pragma unroll
      for (int e = 0; e < 16; ++e)
        rp[e] = __builtin_amdgcn_raw_buffer_load_b16(rsO, (unsigned)(((4 * h + 8 * (e >> 2) + (e & 3)) * DM + r) * 2), (unsigned)((DH * hd * DM + 32 * nt) * 2), 0);
      u32x4 a, c2;
#pragma unroll
      for (int i = 0; i < 4; ++i) {
        a[i] = (rp[2 * i] & 0xffffu) | (rp[2 * i + 1] << 16);
        c2[i] = (rp[8 + 2 * i] & 0xffffu) | (rp[8 + 2 * i + 1] << 16);
      }
      c2[2] = h ? 0u : c2[2];
      c2[3] = h ? 0u : c2[3];
      st16(rsWs, a, lo, WOF_OFF + id * 2048);
      st16(rsWs, c2, lo + 1024, WOF_OFF + id * 2048);
      drain();
      if (lane == 0) put_granule(wsb, GW_OFF + id * 8, tag, 0);
    }
  }
  STAMP(4);
  if (idx >= 28) {
    if (b == 255 && wt == 0) {
      __builtin_amdgcn_s_sleep(127);
#pragma unroll
      for (int i = 0; i < 4; ++i) (void)poll_granule<64>(wsb, GQ_OFF + (i * 64 + lane) * GQW, tag);
      if (lane == 0) __hip_atomic_store((gu64*)(P.ws + FL_OFF), ep + 1, __ATOMIC_RELAXED, __HIP_MEMORY_SCOPE_AGENT);
    }
    STAMP(10);
    STAMP_FLUSH(P, 0);
    return;
  }
  const int mb = 4 * xcd + (idx & 3), nb = idx >> 2;
  const __amdgpu_buffer_rsrc_t rsW2 = mk_rsrc(P.w2, FF * DM * 2), rsH = rsWs;
  const unsigned w2src = (unsigned)((wt * 448 + (lane >> 3)) * (DM * 2) + nb * 128 + ((((lane >> 2) & 1) ^ ((lane >> 4) & 1)) * 64) + (lane & 3) * 16);
  const unsigned ring0 = lds0 + L_W2 + wt * 24576;
  const int rot2 = ROT2;
  auto fr = [&](int f) { const int v = f + rot2; return v >= 14 ? v - 14 : v; };
  auto D = [&](int f) {
#pragma unroll
    for (int i = 0; i < 4; ++i) glds16(rsW2, ring0 + (f % W2S) * 4096 + i * 1024, w2src, (fr(f) * 32 + i * 8) * DM * 2);
  };
  (void)wait_lds_word(lds + L_CTRL, 4);
#pragma unroll
  for (int f = 0; f < W2S; ++f) { D(f); __builtin_amdgcn_s_sleep(PACE); }
  const int ftl = wt * 14 + ((lane & 15) < 14 ? (lane & 15) : 0);
  (void)poll_granule<8>(wsb, (lane & 16) ? GW_OFF + ((nb * 2 + ((lane >> 2) & 1)) * 16 + wt * 4 + (lane & 3)) * 8
                                        : GH_OFF + ((mb >> 1) * 16 + (ftl >> 2)) * 128 + (ftl & 3) * 8, tag);
  STAMP(5);
  u32x4 hb[HPRE][4], wo[16];
  const unsigned hbase = HF_OFF + (mb * 2 * 56 + wt * 14) * 2048;
  auto H = [&](int f) {
#pragma unroll
    for (int e = 0; e < 4; ++e) hb[f % HPRE][e] = ld16c(rsH, lo + (e & 1) * 1024, hbase + ((e >> 1) * 56 + fr(f)) * 2048);
  };
#pragma unroll
  for (int f = 0; f < HPRE; ++f) { H(f); SBAR(); }
  const float gl = __uint_as_float(poll_granule<8>(wsb, GG_OFF + (mb * 64 + lane) * 8, tag) << 16), gs = swap_half(gl);
  const float og0 = 1.f - (h ? gs : gl), og1 = 1.f - (h ? gl : gs);
  const int q4 = (lane & 15) >> 2;
  unsigned woff[2];
#pragma unroll
  for (int nt = 0; nt < 2; ++nt) woff[nt] = (unsigned)(L_W2 + wt * 24576 + (4 * h + q4) * 128 + ((nt ^ (q4 >> 1)) * 64) + ((lane >> 4) & 1) * 32 + (lane & 3) * 8);
#pragma unroll
  for (int i = 0; i < 4; ++i) acc[i] = f32x16{};
  STAMP(6);
  __builtin_amdgcn_s_setprio(0);
  bf16x8 wf[2][2][2];
  auto RW = [&](int f) {
#pragma unroll
    for (int s = 0; s < 2; ++s)
#pragma unroll
      for (int nt = 0; nt < 2; ++nt) wf[f & 1][s][nt] = tr_frag(lds + woff[nt] + (f % W2S) * 4096 + s * 2048, 1024);
  };
  RW(0);
  static_for<0, 14>([&](auto fc) {
    constexpr int f = decltype(fc)::value;
    asm volatile("s_waitcnt lgkmcnt(0)" ::: "memory");
    SBAR();
    if constexpr (f + 1 < 14) {
      if constexpr (f + 1 >= W2S) wait_vm<ffn2_younger(f + 1, f)>();
      RW(f + 1);
    }
    SBAR();
#pragma unroll
    for (int s = 0; s < 2; ++s)
#pragma unroll
      for (int nt = 0; nt < 2; ++nt)
#pragma unroll
        for (int mt = 0; mt < 2; ++mt) {
          acc[nt * 2 + mt] = MFMA(wf[f & 1][s][nt], as_frag(hb[f % HPRE][mt * 2 + s]), acc[nt * 2 + mt]);
        }
    SBAR();
    if constexpr (f + W2S < 14) D(f + W2S);
    if constexpr (f + HPRE < 14) H(f + HPRE);
    else if constexpr (f + HPRE < 18) {
      constexpr int e0 = (f + HPRE - 14) * 4;
#pragma unroll
      for (int e = e0; e < e0 + 4; ++e) wo[e] = ld16c(rsWs, lo, WOF_OFF + ((nb * 2 + (e >> 3)) * 16 + wt * 4) * 2048 + (e & 7) * 1024);
    }
    SBAR();
    __builtin_amdgcn_s_sleep(PACE2);
    SBAR();
  });
  STAMP(7);
  __builtin_amdgcn_s_setprio(3);
#pragma unroll
  for (int i = 0; i < 16; ++i) { acc[0][i] *= og0; acc[1][i] *= og1; acc[2][i] *= og0; acc[3][i] *= og1; }
  (void)wait_lds_word(lds + L_CTRL + 20, 1);
  __builtin_amdgcn_s_sleep(ODELAY);
  (void)poll_granule<POLL_A>(wsb, GO_OFF + ((mb * 2 + ((lane >> 2) & 1)) * 16 + wt * 4 + (lane & 3)) * 128, tag);
  STAMP(8);
  u32x4 of[16];
#pragma unroll
  for (int e = 0; e < 8; ++e)
#pragma unroll
    for (int mt = 0; mt < 2; ++mt) of[mt * 8 + e] = ld16c(rsWs, lo, OF_OFF + ((mb * 2 + mt) * 16 + wt * 4) * 2048 + e * 1024);
  SBAR();
#pragma unroll
  for (int e = 0; e < 8; ++e)
#pragma unroll
    for (int nt = 0; nt < 2; ++nt)
#pragma unroll
      for (int mt = 0; mt < 2; ++mt) acc[nt * 2 + mt] = MFMA(as_frag(wo[nt * 8 + e]), as_frag(of[mt * 8 + e]), acc[nt * 2 + mt]);
#pragma unroll
  for (int t = 0; t < 4; ++t) st_tile(lds + L_W2 + wt * 24576 + t * 4096 + lo, acc[t]);
  STAMP(9);
  bar_lds();
  f32x16 a = f32x16{};
#pragma unroll
  for (int f = 0; f < 4; ++f) add_tile(a, lds + L_W2 + f * 24576 + wt * 4096 + lo);
  unsigned short* dst = P.out + (size_t)((mb * 2 + (wt & 1)) * 32 + r) * DM + (nb * 2 + (wt >> 1)) * 32 + 8 * h;
#pragma unroll
  for (int k = 0; k < 2; ++k) {
    const auto x = __builtin_amdgcn_permlane32_swap(cvtpk(a[8 * k], a[8 * k + 1]), cvtpk(a[8 * k + 4], a[8 * k + 5]), false, false);
    const auto y = __builtin_amdgcn_permlane32_swap(cvtpk(a[8 * k + 2], a[8 * k + 3]), cvtpk(a[8 * k + 6], a[8 * k + 7]), false, false);
    *(u32x4*)(dst + 16 * k) = u32x4{x[0], y[0], x[1], y[1]};
  }
  STAMP(10);
  STAMP_FLUSH(P, 0);
}

extern __shared__ __attribute__((aligned(16))) unsigned char g_lds[];

__global__ __launch_bounds__(512, 2) void k_fused(Params P) {
  u64 pc;
  asm volatile("s_getpc_b64 %0" : "=s"(pc));
  const unsigned code_word = __builtin_amdgcn_raw_buffer_load_b32(mk_rsrc((const void*)(pc & ~63ull), CODE_PF),
      (threadIdx.x & 256) * 4096 + (((blockIdx.x >> 3) * 32 + ((threadIdx.x >> 3) & 24) + (threadIdx.x & 7)) * 64), 0, 0);
  SBAR();
  LAS unsigned char* lds = (LAS unsigned char*)g_lds;
  const int w = __builtin_amdgcn_readfirstlane(threadIdx.x >> 6);
  if (w < 4) role_a(P, lds, w, code_word);
  else role_f(P, lds, w - 4);
}

extern "C" void kernel_launch(void* const* d_in, const int* in_sizes, int n_in, void* d_out, int out_size, void* d_ws,
                              size_t ws_size, hipStream_t stream) {
  Params P;
  P.x = (const unsigned short*)d_in[0];
  P.wqkv = (const unsigned short*)d_in[1];
  P.wo = (const unsigned short*)d_in[2];
  P.w1 = (const unsigned short*)d_in[4];
  P.w2 = (const unsigned short*)d_in[5];
  P.gw = (const unsigned short*)d_in[6];
  P.out = (unsigned short*)d_out;
  P.ws = (unsigned char*)d_ws;
  (void)hipFuncSetAttribute((const void*)k_fused, hipFuncAttributeMaxDynamicSharedMemorySize, LDS_BYTES);
  k_first<<<256, 64, 0, stream>>>(P);
  k_fused<<<256, 512, LDS_BYTES, stream>>>(P);
}
```

```cpp
#include <hip/hip_runtime.h>
#include <hip/hip_bf16.h>
#include <stdint.h>

#if defined(__HIP_DEVICE_COMPILE__)
#pragma clang attribute push(__attribute__((target("no-packed-fp32-ops"))), apply_to = function)
#endif

typedef short bf16x8 __attribute__((ext_vector_type(8)));
typedef short s16x4 __attribute__((ext_vector_type(4)));
typedef float f32x16 __attribute__((ext_vector_type(16)));
typedef float f32x4 __attribute__((ext_vector_type(4)));
typedef unsigned u32x4 __attribute__((ext_vector_type(4)));
#define LAS __attribute__((address_space(3)))
#define DI __device__ __forceinline__
#define MFMA(a, b, c) __builtin_amdgcn_mfma_f32_32x32x16_bf16((a), (b), (c), 0, 0, 0)
#define SBAR() __builtin_amdgcn_sched_barrier(0)

constexpr int SEQ = 2048, DM = 448, DH = 28, FF = 1792;
constexpr unsigned FL_OFF = 0;
constexpr unsigned SET_BASE = 1u << 20, SET_STRIDE = 24u << 20;
constexpr unsigned GQS = 16u, GQW = 128u;
constexpr unsigned GQ_OFF = 0;
constexpr unsigned GH_OFF = GQ_OFF + 256 * GQW;
constexpr unsigned GO_OFF = GH_OFF + 256 * 128;
constexpr unsigned GW_OFF = GO_OFF + 1024 * 128;
constexpr unsigned GG_OFF = GW_OFF + 256 * 8;
constexpr unsigned QF_OFF = 524288;
constexpr unsigned KV_OFF = QF_OFF + 16 * 64 * 2048;
constexpr unsigned HF_OFF = KV_OFF + 16 * 64 * 4096;
constexpr unsigned WOF_OFF = HF_OFF + 64 * 56 * 2048;
constexpr unsigned OF_OFF = WOF_OFF + 14 * 16 * 2048;
constexpr unsigned WS_END = OF_OFF + 64 * 16 * 2048;
static_assert(GG_OFF + 2048 * 8 <= QF_OFF && WS_END <= SET_STRIDE, "workspace map");

constexpr int L_X = 0;
constexpr int L_WQ = 114688;
constexpr int L_W1 = 114688;
constexpr int L_CTRL = 163792;
constexpr int PAIR = 40960;
constexpr int LDS_BYTES = 163840;

constexpr float QSCALE = 0.18898223650461362f * 1.4426950408889634f;
constexpr float LOG2E = 1.4426950408889634f;
constexpr float SAFE_BOUND = 100.0f;
constexpr unsigned V_LIMIT = 0x4e80u;
constexpr unsigned SPIN_CAP = 1u << 18;
#define ROT1(rb, j) ((((rb) + (j)) & 1) * 3)
constexpr int PDELAY = 16;
constexpr int ODELAY = 1;
constexpr int CODE_PF = 45056;
constexpr int F1PRIO = 2;
constexpr int LSTEP = 2;
constexpr int NR0 = 5;
constexpr int APRIO = 3;
constexpr int POLL_A = 2;

struct Params {
  const unsigned short* x;
  const unsigned short* wqkv;
  const unsigned short* wo;
  const unsigned short* w1;
  const unsigned short* w2;
  const unsigned short* gw;
  unsigned short* out;
  unsigned char* ws;
};
typedef unsigned long long u64;
typedef __attribute__((address_space(1))) u64 gu64;

#define TID __builtin_amdgcn_workitem_id_x()
#define BID __builtin_amdgcn_workgroup_id_x()
DI unsigned f2u(float x) { return __builtin_bit_cast(unsigned, x); }
DI float u2f(unsigned x) { return __builtin_bit_cast(float, x); }
DI unsigned umax(unsigned a, unsigned b) { return a > b ? a : b; }
DI bool wave_all(bool p) { return __builtin_amdgcn_ballot_w64(!p) == 0; }
DI unsigned cvtpk(float lo, float hi) {
  typedef __bf16 bf2 __attribute__((ext_vector_type(2)));
  typedef float f2 __attribute__((ext_vector_type(2)));
  f2 v = {lo, hi};
  bf2 r = __builtin_convertvector(v, bf2);
  return __builtin_bit_cast(unsigned, r);
}
DI bf16x8 pack8(const f32x16& x, int s) {
  u32x4 p;
  p[0] = cvtpk(x[8 * s + 0], x[8 * s + 1]);
  p[1] = cvtpk(x[8 * s + 2], x[8 * s + 3]);
  p[2] = cvtpk(x[8 * s + 4], x[8 * s + 5]);
  p[3] = cvtpk(x[8 * s + 6], x[8 * s + 7]);
  return __builtin_bit_cast(bf16x8, p);
}
DI float swap_half(float v) {
  auto r = __builtin_amdgcn_permlane32_swap(f2u(v), f2u(v), false, false);
  return (TID & 32) ? u2f(r[0]) : u2f(r[1]);
}
DI unsigned row16_umax(unsigned v) {
  v = umax(v, (unsigned)__builtin_amdgcn_update_dpp(v, v, 0x128, 0xf, 0xf, false));
  v = umax(v, (unsigned)__builtin_amdgcn_update_dpp(v, v, 0x124, 0xf, 0xf, false));
  v = umax(v, (unsigned)__builtin_amdgcn_update_dpp(v, v, 0x122, 0xf, 0xf, false));
  v = umax(v, (unsigned)__builtin_amdgcn_update_dpp(v, v, 0x121, 0xf, 0xf, false));
  return v;
}
DI s16x4 tr_read(const LAS unsigned char* p) {
  typedef short v4i16_t __attribute__((ext_vector_type(4)));
  return __builtin_bit_cast(s16x4, __builtin_amdgcn_ds_read_tr16_b64_v4i16((LAS v4i16_t*)p));
}
DI bf16x8 tr_frag(const LAS unsigned char* p, int row_stride4) {
  s16x4 lo = tr_read(p), hi = tr_read(p + row_stride4);
  return __builtin_shufflevector(lo, hi, 0, 1, 2, 3, 4, 5, 6, 7);
}
DI __amdgpu_buffer_rsrc_t mk_rsrc(const void* p, unsigned bytes) {
  return __builtin_amdgcn_make_buffer_rsrc((void*)p, (short)0, (int)bytes, 0x00020000);
}
DI void glds16x4(__amdgpu_buffer_rsrc_t rs, unsigned dst, unsigned v0, unsigned v1, unsigned v2, unsigned v3, unsigned soff) {
  asm volatile("s_mov_b32 m0, %0\n\ts_nop 0\n\tbuffer_load_dwordx4 %1, %5, %6 offen lds\n\tbuffer_load_dwordx4 %2, %5, %6 offen offset:1024 lds\n\t"
               "buffer_load_dwordx4 %3, %5, %6 offen offset:2048 lds\n\tbuffer_load_dwordx4 %4, %5, %6 offen offset:3072 lds"
               ::"s"(dst), "v"(v0), "v"(v1), "v"(v2), "v"(v3), "s"(rs), "s"(soff) : "memory");
}
DI void glds16x3(__amdgpu_buffer_rsrc_t rs, unsigned dst, unsigned v0, unsigned v1, unsigned v2, unsigned soff) {
  asm volatile("s_mov_b32 m0, %0\n\ts_nop 0\n\tbuffer_load_dwordx4 %1, %4, %5 offen lds\n\tbuffer_load_dwordx4 %2, %4, %5 offen offset:1024 lds\n\t"
               "buffer_load_dwordx4 %3, %4, %5 offen offset:2048 lds"
               ::"s"(dst), "v"(v0), "v"(v1), "v"(v2), "s"(rs), "s"(soff) : "memory");
}
DI void glds_tile_sc1(__amdgpu_buffer_rsrc_t rs, unsigned dst, unsigned voff, unsigned soff) {
  asm volatile("s_mov_b32 m0, %0\n\ts_nop 0\n\tbuffer_load_dwordx4 %1, %2, %3 offen sc1 lds\n\tbuffer_load_dwordx4 %1, %2, %3 offen offset:1024 sc1 lds\n\t"
               "buffer_load_dwordx4 %1, %2, %3 offen offset:2048 sc1 lds\n\ts_mov_b32 exec_hi, 0x1fffffff\n\tbuffer_load_dwordx4 %1, %2, %3 offen offset:3072 sc1 lds\n\ts_mov_b32 exec_hi, -1"
               ::"s"(dst), "v"(voff), "s"(rs), "s"(soff) : "memory");
}
template <int OFF> DI u32x4 ld16c_raw(__amdgpu_buffer_rsrc_t rs, unsigned voff, unsigned soff) {
  u32x4 v;
  asm volatile("buffer_load_dwordx4 %0, %1, %2, %3 offen offset:%4 sc1" : "=v"(v) : "v"(voff), "s"(rs), "s"(soff), "n"(OFF) : "memory");
  return v;
}
DI u64 ld_granule_raw(unsigned char* ws, unsigned off) {
  u64 v;
  asm volatile("global_load_dwordx2 %0, %1, %2 sc1" : "=v"(v) : "v"(off), "s"(ws) : "memory");
  return v;
}
DI u32x4 ld16(__amdgpu_buffer_rsrc_t rs, unsigned voff, unsigned soff) { return __builtin_amdgcn_raw_buffer_load_b128(rs, voff, soff, 0); }
DI u32x4 ld16c(__amdgpu_buffer_rsrc_t rs, unsigned voff, unsigned soff) { return __builtin_amdgcn_raw_buffer_load_b128(rs, voff, soff, 16); }
DI void st16(__amdgpu_buffer_rsrc_t rs, u32x4 v, unsigned voff, unsigned soff) { __builtin_amdgcn_raw_buffer_store_b128(v, rs, voff + soff, 0, 16); }
DI bf16x8 as_frag(u32x4 v) { return __builtin_bit_cast(bf16x8, v); }
DI u32x4 as_u4(bf16x8 v) { return __builtin_bit_cast(u32x4, v); }
template <int N> DI void wait_bar() { asm volatile("s_waitcnt vmcnt(%0) lgkmcnt(0)\n\ts_barrier" ::"n"(N) : "memory"); }

template <int V> struct IC { static constexpr int value = V; };
template <int I, int N, class Fn> DI void static_for(Fn&& fn) {
  if constexpr (I < N) { fn(IC<I>{}); static_for<I + 1, N>(fn); }
}
DI void drain() { asm volatile("s_waitcnt vmcnt(0)" ::: "memory"); }
template <int N> DI void wait_vm() { asm volatile("s_waitcnt vmcnt(%0)" ::"n"(N) : "memory"); }
DI void put_granule(unsigned char* ws, unsigned off, u64 tag16, unsigned payload) {
  __hip_atomic_store((gu64*)(ws + off), tag16 | (u64)(payload & 0xffffu), __ATOMIC_RELAXED, __HIP_MEMORY_SCOPE_AGENT);
}
template <int SLEEP> DI unsigned poll_granule(unsigned char* ws, unsigned off, u64 tag16) {
  u64 v = ~tag16;
  for (unsigned spins = 0;; ++spins) {
    if (v - tag16 > 0xffffull) v = __hip_atomic_load((gu64*)(ws + off), __ATOMIC_RELAXED, __HIP_MEMORY_SCOPE_AGENT);
    if (wave_all(v - tag16 <= 0xffffull) || spins > SPIN_CAP) break;
    __builtin_amdgcn_s_sleep(SLEEP);
  }
  __builtin_amdgcn_fence(__ATOMIC_ACQUIRE, "wavefront");
  return (unsigned)v & 0xffffu;
}
template <int SLEEP = 1> DI unsigned wait_lds_word(LAS unsigned char* p, unsigned want) {
  unsigned v = 0;
#pragma nounroll
  for (unsigned spins = 0; spins < (1u << 22); ++spins) {
    v = __builtin_amdgcn_readfirstlane(__hip_atomic_load((LAS unsigned*)p, __ATOMIC_RELAXED, __HIP_MEMORY_SCOPE_WORKGROUP));
    if (v >= want) break;
    if (SLEEP) __builtin_amdgcn_s_sleep(SLEEP);
  }
  asm volatile("" ::: "memory");
  return v;
}
template <int N> DI u64 wait_bar_epoch(const unsigned char* p) {
  u64 ep;
  asm volatile("s_load_dwordx2 %0, %1, 0x0\n\ts_waitcnt vmcnt(%2) lgkmcnt(0)\n\ts_barrier" : "=&s"(ep) : "s"(p), "n"(N) : "memory");
  return ep;
}
DI void bar_lds() { asm volatile("s_waitcnt lgkmcnt(0)\n\ts_barrier" ::: "memory"); }
DI void st_tile(LAS unsigned char* p, const f32x16& a) {
#pragma unroll
  for (int g = 0; g < 4; ++g) *(LAS f32x4*)(p + g * 1024) = f32x4{a[4 * g], a[4 * g + 1], a[4 * g + 2], a[4 * g + 3]};
}
DI void add_tiles3(f32x16& a, const LAS unsigned char* p0, const LAS unsigned char* p1, const LAS unsigned char* p2) {
  f32x4 v[3][4];
#pragma unroll
  for (int g = 0; g < 4; ++g) {
    v[0][g] = *(const LAS f32x4*)(p0 + g * 1024);
    v[1][g] = *(const LAS f32x4*)(p1 + g * 1024);
    v[2][g] = *(const LAS f32x4*)(p2 + g * 1024);
  }
  SBAR();
#pragma unroll
  for (int g = 0; g < 4; ++g)
#pragma unroll
    for (int i = 0; i < 4; ++i) a[4 * g + i] = (a[4 * g + i] + v[0][g][i]) + (v[1][g][i] + v[2][g][i]);
}
DI bool tag_in(u64 v, u64 tag16, unsigned lim) { return (unsigned)(v >> 32) == (unsigned)(tag16 >> 32) && (unsigned)v - (unsigned)tag16 <= lim; }

DI void lds_arrive(LAS unsigned char* p) {
  asm volatile("s_mov_b64 exec, 1\n\tds_add_u32 %0, %1\n\ts_mov_b64 exec, -1" ::"v"((unsigned)(uintptr_t)p), "v"(1u) : "memory");
}

#define PINV(x) asm volatile("" : "+v"(x))
#define PINS(x) asm volatile("" : "+s"(x))
DI float sumsq(const f32x16& a) {
  typedef float f2 __attribute__((ext_vector_type(2)));
  f2 n0 = {0.f, 0.f}, n1 = {0.f, 0.f};
#pragma unroll
  for (int i = 0; i < 4; ++i) {
    const f2 u = {a[4 * i], a[4 * i + 1]}, v = {a[4 * i + 2], a[4 * i + 3]};
    n0 = __builtin_elementwise_fma(u, u, n0);
    n1 = __builtin_elementwise_fma(v, v, n1);
  }
  n0 += n1;
  return (n0[0] + n0[1]) * 1.02f;
}

DI void role_a(const Params& P, LAS unsigned char* lds, const int wt, const unsigned code_word) {
  const int lane = TID & 63, r = lane & 31, h = lane >> 5;
  const int b = BID, xcd = b & 7, idx = b >> 3;
  const unsigned lo = lane * 16;
  __builtin_amdgcn_s_setprio(APRIO);
  unsigned xtouch;
  {
    const unsigned xr = xcd & 1, xh = xcd >> 1, l7 = idx * 49 + (lane < 49 ? lane : 0), l8 = idx * 56 + (lane < 56 ? lane : 0);
    const unsigned row7 = (l7 * 9363u) >> 16, c7 = l7 - row7 * 7;
    const unsigned c8 = l8 & 7, w1c = xh < 2 ? xh * 768 + (c8 < 6 ? c8 : 5) * 128 : xh * 1024 - 512 + c8 * 128;
    const unsigned off = wt == 0 ? (1024 * xr + 256 * xh) * (DM * 2) + l8 * 128
                       : wt == 1 ? (224 * xr + row7) * (3 * DM * 2) + 672 * xh + (c7 < 6 ? c7 * 128 : 668)
                       : wt == 2 ? (224 * xr + (l8 >> 3)) * (FF * 2) + w1c
                                 : (xcd * 1568 + l7) * 128;
    xtouch = __builtin_amdgcn_raw_buffer_load_b32(mk_rsrc(wt == 0 ? P.x : wt == 1 ? P.wqkv : wt == 2 ? P.w1 : P.w2,
                                                                        wt == 0 ? SEQ * DM * 2 : wt == 1 ? 3 * DM * DM * 2 : DM * FF * 2), off, 0, 0);
    SBAR();
  }
  f32x16 acc[4];
  const int rb = (xcd & 1) * 8 + (idx & 7), j = (xcd >> 1) * 4 + (idx >> 3);
  const int head = xcd * 2 + (idx & 1), qb = idx >> 1;
  unsigned sto[3];
#pragma unroll
  for (int ct = 0; ct < 3; ++ct) {
    const int u = 3 * j + ct, part = u >> 4, t = (u & 15) * 64 + rb * 4 + wt;
    sto[ct] = lo + (part == 0 ? QF_OFF + t * 2048 : KV_OFF + t * 4096 + (part - 1) * 2048);
    PINV(sto[ct]);
  }
  unsigned padv = r == DH ? 0x3f803f80u : 0u;
  PINV(padv);
  const int gq = head / 3, gk = (16 + head) / 3, gvv = (32 + head) / 3;
  unsigned pub_off = GQ_OFF + (j * 16 + rb) * GQW + wt * GQS + (lane & 1) * 8;
  unsigned poll_off = GQ_OFF + (h ? gq * 16 + qb : (lane < 16 ? gk : gvv) * 16 + wt * 4 + ((lane >> 2) & 3)) * GQW + (lane & 3) * GQS + h * 8;
  unsigned chk_off = GQ_OFF + (gk * 16 + (lane >> 2)) * GQW + (lane & 3) * GQS;
  unsigned g_off = GG_OFF + ((qb * 4 + wt) * 32 + r) * 8;
  unsigned o_off = lo + OF_OFF + ((qb * 4 + wt) * 16 + head) * 2048;
  PINV(pub_off); PINV(poll_off); PINV(chk_off); PINV(g_off); PINV(o_off);
  unsigned kvbase = KV_OFF + (head * 64 + wt * 16) * 4096, go_off = GO_OFF + ((qb * 4 + wt) * 16 + head) * 128;
  unsigned qofs[4];
#pragma unroll
  for (int t = 0; t < 4; ++t) { qofs[t] = QF_OFF + (unsigned)(head * 64 + qb * 4 + ((wt + 1 + t) & 3)) * 2048; PINS(qofs[t]); }
  PINS(kvbase); PINS(go_off);
  unsigned xoff[4];
#pragma unroll
  for (int ks = 0; ks < 4; ++ks) xoff[ks] = (unsigned)(L_X + wt * 4096 + r * 128 + (((2 * ks + h) ^ ((r >> 1) & 7)) << 4));
  const unsigned wqoff = (unsigned)(L_WQ + (8 * h + ((lane & 15) >> 2)) * 192 + ((lane >> 4) & 1) * 32 + (lane & 3) * 8 + (j & 1) * 8);
  u64 ep;
  asm volatile("s_load_dwordx2 %0, %1, 0x0\n\ts_waitcnt lgkmcnt(0)" : "=&s"(ep) : "s"(P.ws + FL_OFF) : "memory");
  u64 tag16 = (ep + 1) << 16;
  unsigned char* wsb = P.ws + SET_BASE + (((unsigned)ep + 1u) & 3u) * SET_STRIDE;
  PINS(tag16); PINS(wsb);
  const __amdgpu_buffer_rsrc_t rsWs = mk_rsrc(wsb, WS_END);
#pragma unroll
  for (int i = 0; i < 3; ++i) acc[i] = f32x16{};
  auto kloop = [&](auto nvc) {
    constexpr int NV = decltype(nvc)::value;
    bf16x8 xf[4], wf[4][3];
    auto mm = [&](int ks, int ct) { acc[ct] = ct >= 3 - NV ? MFMA(xf[ks], wf[ks][ct], acc[ct]) : MFMA(wf[ks][ct], xf[ks], acc[ct]); };
    auto step = [&](int c, int ks, bool domm) {
      const int km = (ks + 2) & 3;
#pragma unroll
      for (int ct = 0; ct < 3; ++ct) {
        if (domm) mm(km, ct);
        SBAR();
        if (ct == 0) xf[ks] = *(const LAS bf16x8*)(lds + xoff[ks] + c * 16384);
        wf[ks][ct] = tr_frag(lds + wqoff + (c & 3) * 12288 + ks * 16 * 192 + ct * 56, 4 * 192);
        SBAR();
      }
    };
#pragma unroll
    for (int c = 0; c < 7; ++c) {
      bar_lds();
      step(c, 0, c > 0);
      step(c, 1, c > 0);
      step(c, 2, true);
      step(c, 3, true);
    }
    bar_lds();
#pragma unroll
    for (int ct = 0; ct < 3; ++ct) { mm(2, ct); mm(3, ct); SBAR(); }
  };
  auto epi = [&](auto nqc, auto nvc) {
    constexpr int NQ = decltype(nqc)::value, NV = decltype(nvc)::value;
    asm volatile("" ::"v"(code_word), "v"(xtouch));
    u32x4 t0[3], t1[3];
#pragma unroll
    for (int ct = 0; ct < 3; ++ct) {
      if (ct < NQ) acc[ct] *= QSCALE;
      t0[ct] = as_u4(pack8(acc[ct], 0));
      if (ct >= 3 - NV) {
#pragma unroll
        for (int i = 0; i < 4; ++i) t0[ct][i] = r < DH ? t0[ct][i] : padv;
      }
      st16(rsWs, t0[ct], sto[ct], 0);
      t1[ct] = as_u4(pack8(acc[ct], 1));
      if (ct >= 3 - NV) {
#pragma unroll
        for (int i = 0; i < 4; ++i) t1[ct][i] = r < DH ? t1[ct][i] : padv;
      } else {
        t1[ct][2] = h ? (ct < NQ ? 0u : 0x3f803f80u) : t1[ct][2];
        t1[ct][3] = h ? (ct < NQ ? 0u : 0x00003f80u) : t1[ct][3];
      }
      st16(rsWs, t1[ct], sto[ct], 1024);
      SBAR();
    }
    unsigned mq = 0, mk = 0;
#pragma unroll
    for (int ct = 0; ct < 3; ++ct) {
      const float hs = sumsq(acc[ct]);
      const auto sw = __builtin_amdgcn_permlane32_swap(f2u(hs), f2u(hs), false, false);
      const unsigned n = f2u(u2f(sw[0]) + u2f(sw[1])) & 0x7fffffffu;
      if (ct < NQ) mq = umax(mq, n); else mk = umax(mk, n);
    }
    auto bound = [&](unsigned m) {
      m = row16_umax(m);
      return ((umax((unsigned)__builtin_amdgcn_readlane(m, 0), (unsigned)__builtin_amdgcn_readlane(m, 16)) >> 16) + 1u) & 0xffffu;
    };
    const unsigned pq = NQ > 0 ? bound(mq) : 0u, pk = NQ < 3 ? bound(mk) : 0u;
    u64 gran = tag16 | ((lane & 1) ? pq : pk);
    PINV(gran);
    drain();
    if (lane < 2) __hip_atomic_store((gu64*)(wsb + pub_off), gran, __ATOMIC_RELAXED, __HIP_MEMORY_SCOPE_AGENT);
  };
  if (j < 10) {
    kloop(IC<0>{});
    if (j < 5) epi(IC<3>{}, IC<0>{});
    else if (j == 5) epi(IC<1>{}, IC<0>{});
    else epi(IC<0>{}, IC<0>{});
  } else if (j == 10) {
    kloop(IC<1>{});
    epi(IC<0>{}, IC<1>{});
  } else {
    kloop(IC<3>{});
    epi(IC<0>{}, IC<3>{});
  }
  __builtin_amdgcn_s_sleep(PDELAY);
  const unsigned pay = poll_granule<POLL_A>(wsb, poll_off, tag16);
  u32x4 q[4][2], kv[16][4];
  auto kvo = [&](int k) { return kvbase + k * 4096; };
  LAS unsigned char* const slot = lds + wt * PAIR + lo;
  kv[0][0] = ld16c(rsWs, lo, kvo(0));
  q[0][0] = ld16c(rsWs, lo, qofs[0]);
  SBAR();
  kv[0][1] = ld16c(rsWs, lo + 1024, kvo(0));
  q[0][1] = ld16c(rsWs, lo + 1024, qofs[0]);
  SBAR();
  q[1][0] = ld16c(rsWs, lo, qofs[1]);
  q[1][1] = ld16c(rsWs, lo + 1024, qofs[1]);
  SBAR();
  kv[0][2] = ld16c(rsWs, lo + 2048, kvo(0));
  kv[0][3] = ld16c(rsWs, lo + 3072, kvo(0));
  SBAR();
#pragma unroll
  for (int t = 2; t < 4; ++t) { q[t][0] = ld16c(rsWs, lo, qofs[t]); q[t][1] = ld16c(rsWs, lo + 1024, qofs[t]); SBAR(); }
#pragma unroll
  for (int e = 0; e < 4; ++e) { kv[1][e] = ld16c(rsWs, lo + e * 1024, kvo(1)); SBAR(); }
#pragma unroll
  for (int i = 0; i < 4; ++i) acc[i] = f32x16{};
  unsigned thr;
  {
    const float q2 = u2f(__builtin_amdgcn_readlane(row16_umax(pay), 32) << 16);
    const float lim = SAFE_BOUND * SAFE_BOUND * __builtin_amdgcn_rcpf(q2);
    thr = lim < 1e30f ? f2u(lim) >> 16 : (lim >= 1e30f ? 0x7000u : 0u);
    PINV(thr);
  }
  __builtin_amdgcn_s_setprio(1);
  u64 gv = 0, ck = 0, cv = 0;
  u64 off_range = 0;
  unsigned landed = 0;
  auto check_loads = [&]() {
    gv = __hip_atomic_load((gu64*)(wsb + g_off), __ATOMIC_RELAXED, __HIP_MEMORY_SCOPE_AGENT);
    ck = __hip_atomic_load((gu64*)(wsb + chk_off), __ATOMIC_RELAXED, __HIP_MEMORY_SCOPE_AGENT);
    cv = __hip_atomic_load((gu64*)(wsb + (gvv - gk) * 16 * GQW + chk_off), __ATOMIC_RELAXED, __HIP_MEMORY_SCOPE_AGENT);
  };
#define PART(t, g) do { *(LAS f32x4*)(slot + (6 + (t)) * 4096 + (g) * 1024) = f32x4{acc[t][4 * (g)], acc[t][4 * (g) + 1], acc[t][4 * (g) + 2], acc[t][4 * (g) + 3]}; SBAR(); } while (0)
  {
    f32x16 sc = MFMA(as_frag(kv[0][0]), as_frag(q[0][0]), f32x16{});
    sc = MFMA(as_frag(kv[0][1]), as_frag(q[0][1]), sc);
    u32x4 p0, p1 = {0, 0, 0, 0};
    float e0 = 0.f, e1 = 0.f, e2 = 0.f, e3 = 0.f;
#define EXP4(i) do { e0 = __builtin_amdgcn_exp2f(sc[i]); e1 = __builtin_amdgcn_exp2f(sc[(i) + 1]); e2 = __builtin_amdgcn_exp2f(sc[(i) + 2]); e3 = __builtin_amdgcn_exp2f(sc[(i) + 3]); \
    asm volatile("" : "+v"(e0), "+v"(e1), "+v"(e2), "+v"(e3)); SBAR(); } while (0)
#define CVT2(P, i) do { P[i] = cvtpk(e0, e1); P[(i) + 1] = cvtpk(e2, e3); asm volatile("" : "+v"(P)); SBAR(); } while (0)
#define RDKV(k, e) do { kv[k][e] = *(const LAS u32x4*)(slot + ((k) - NR0) * 4096 + (e) * 1024); SBAR(); } while (0)
    static_for<0, 64>([&](auto ic) {
      constexpr int i = decltype(ic)::value;
      constexpr int kt = i >> 2, t = i & 3, kn_ = (i + 1) >> 2, tn = (i + 1) & 3, kp = (i - 1) >> 2, tp = (i - 1) & 3;
      constexpr bool nl = kt + 1 >= NR0 && kt + 1 <= NR0 + 9, cl = kt >= NR0 && kt <= NR0 + 9;
      if constexpr (i == 4 * NR0 - 2 || i == 4 * NR0 + 10) {
        PINV(acc[0]); PINV(acc[1]); PINV(acc[2]); PINV(acc[3]); PINV(sc);
        if (__builtin_expect((unsigned)__builtin_amdgcn_readfirstlane(landed) < (i == 4 * NR0 - 2 ? 1u : 2u), 0)) (void)wait_lds_word(lds + L_CTRL + 20 + wt * 4, i == 4 * NR0 - 2 ? 1 : 2);
        asm volatile("" ::: "memory");
      }
      f32x16 sn;
      SBAR();
      if (i < 63) sn = MFMA(as_frag(kv[kn_ & 15][0]), as_frag(q[tn][0]), f32x16{});
      SBAR();
      if constexpr (i >= 62) PART(i - 62, 0);
      if constexpr (t == 2 && nl) RDKV(kt + 1, 0);
      if (i > 0) CVT2(p1, 2);
      EXP4(0);
      if (i > 0) acc[tp] = MFMA(as_frag(kv[kp & 15][3]), as_frag(p1), acc[tp]);
      SBAR();
      if constexpr (t == 0 && cl) RDKV(kt, 3);
      if constexpr (i < 4 * LSTEP && i % LSTEP == 0) {
        constexpr int k = 2 + i / LSTEP < NR0 ? 2 + i / LSTEP : 12 + i / LSTEP;
#pragma unroll
        for (int e = 0; e < 4; ++e) kv[k][e] = ld16c(rsWs, lo + e * 1024, kvo(k));
        if constexpr (i == 3 * LSTEP) {
          check_loads();
          lds_arrive(lds + L_CTRL + 4 + wt * 4);
        }
      }
      if constexpr (t == 2 && kt < NR0 - 1) {
        constexpr int n = 4 * ((i / LSTEP + 1 < 4 ? i / LSTEP + 1 : 4) - kt) + (i >= 3 * LSTEP ? 3 : 0);
        __builtin_amdgcn_s_waitcnt(0x0f70 | (n & 15) | ((n >> 4) << 14));
      }
      if constexpr (i == 4 * NR0 + 6 || i == 4 * NR0 + 14) lds_arrive(lds + L_CTRL + 4 + wt * 4);
      if constexpr (i == 50) {
        __builtin_amdgcn_s_waitcnt(0x0f70);
        off_range = __builtin_amdgcn_ballot_w64(!(tag_in(ck, tag16, thr) && tag_in(cv, tag16, V_LIMIT) && tag_in(gv, tag16, 0xffffu)));
        PINS(off_range);
      }
      SBAR();
      if constexpr (i >= 62) PART(i - 62, 1);
      CVT2(p0, 0);
      EXP4(4);
      if (i < 63) sn = MFMA(as_frag(kv[kn_ & 15][1]), as_frag(q[tn][1]), sn);
      SBAR();
      if constexpr (i >= 62) PART(i - 62, 2);
      if constexpr (t == 2 && nl) RDKV(kt + 1, 1);
      CVT2(p0, 2);
      EXP4(8);
      acc[t] = MFMA(as_frag(kv[kt][2]), as_frag(p0), acc[t]);
      SBAR();
      if constexpr (i >= 62) PART(i - 62, 3);
      if constexpr (t == 3 && nl) RDKV(kt + 1, 2);
      if constexpr (i == 4 * NR0 - 3 || i == 4 * NR0 + 9) { landed = __hip_atomic_load((LAS unsigned*)(lds + L_CTRL + 20 + wt * 4), __ATOMIC_RELAXED, __HIP_MEMORY_SCOPE_WORKGROUP); SBAR(); }
      CVT2(p1, 0);
      EXP4(12);
      if (i < 63) sc = sn;
    });
    CVT2(p1, 2);
    acc[3] = MFMA(as_frag(kv[15][3]), as_frag(p1), acc[3]);
    SBAR();
    PART(2, 0); PART(2, 1); PART(2, 2); PART(2, 3);
#undef EXP4
#undef CVT2
#undef RDKV
  }
  __builtin_amdgcn_s_setprio(APRIO);
  if (__builtin_expect(off_range != 0, 0)) {
    gv = poll_granule<2>(wsb, g_off, tag16);
    const unsigned pk = poll_granule<2>(wsb, chk_off, tag16), pv = poll_granule<2>(wsb, (gvv - gk) * 16 * GQW + chk_off, tag16);
    if (!wave_all(pk <= thr && pv <= V_LIMIT)) {
      float mx[4];
#pragma unroll
      for (int t = 0; t < 4; ++t) mx[t] = -INFINITY;
#pragma nounroll
      for (int kt = 0; kt < 64; ++kt) {
        const u32x4 k0 = ld16c(rsWs, lo, KV_OFF + (head * 64 + kt) * 4096), k1 = ld16c(rsWs, lo + 1024, KV_OFF + (head * 64 + kt) * 4096);
#pragma unroll
        for (int t = 0; t < 4; ++t) {
          f32x16 s = MFMA(as_frag(k0), as_frag(q[t][0]), f32x16{});
          s = MFMA(as_frag(k1), as_frag(q[t][1]), s);
#pragma unroll
          for (int i = 0; i < 16; ++i) mx[t] = __builtin_fmaxf(mx[t], s[i]);
        }
      }
#pragma unroll
      for (int t = 0; t < 4; ++t) {
        const float nm = -__builtin_fmaxf(mx[t], swap_half(mx[t]));
        const unsigned hi = f2u(nm) & 0xffff0000u;
        const float r1 = nm - u2f(hi);
        const unsigned mid = f2u(r1) & 0xffff0000u;
        const float r2 = r1 - u2f(mid);
        if (h) { q[t][1][2] = (hi >> 16) | mid; q[t][1][3] = f2u(r2) >> 16; }
        acc[t] = f32x16{};
      }
#pragma nounroll
      for (int kt = 0; kt < 16; ++kt) {
        u32x4 f[4];
#pragma unroll
        for (int e = 0; e < 4; ++e) f[e] = ld16c(rsWs, lo + e * 1024, kvbase + kt * 4096);
#pragma unroll
        for (int t = 0; t < 4; ++t) {
          f32x16 s = MFMA(as_frag(f[0]), as_frag(q[t][0]), f32x16{});
          s = MFMA(as_frag(f[1]), as_frag(q[t][1]), s);
#pragma unroll
          for (int i = 0; i < 16; ++i) s[i] = __builtin_amdgcn_exp2f(s[i]);
          acc[t] = MFMA(as_frag(f[2]), pack8(s, 0), acc[t]);
          acc[t] = MFMA(as_frag(f[3]), pack8(s, 1), acc[t]);
        }
      }
#pragma unroll
      for (int t = 0; t < 3; ++t) st_tile(slot + (6 + t) * 4096, acc[t]);
    }
  }
#undef PART
  const LAS unsigned char *cs0 = lds + ((wt + 3) & 3) * PAIR + 6 * 4096 + lo, *cs1 = lds + (wt ^ 2) * PAIR + 7 * 4096 + lo, *cs2 = lds + ((wt + 1) & 3) * PAIR + 8 * 4096 + lo;
  lds_arrive(lds + L_CTRL + 36);
  PINV(cs0); PINV(cs1); PINV(cs2);
  (void)wait_lds_word<0>(lds + L_CTRL + 36, 4);
  f32x16 o = acc[3];
  add_tiles3(o, cs0, cs1, cs2);
  const auto lsw = __builtin_amdgcn_permlane32_swap(f2u(o[12]), f2u(o[12]), false, false);
  const float inv = u2f((unsigned)gv << 16) * __builtin_amdgcn_rcpf(u2f(lsw[1]));
  o *= inv;
  st16(rsWs, as_u4(pack8(o, 0)), o_off, 0);
  u32x4 o1 = as_u4(pack8(o, 1));
  o1[2] = h ? 0u : o1[2];
  o1[3] = h ? 0u : o1[3];
  st16(rsWs, o1, o_off, 1024);
  drain();
  if (lane == 0) __hip_atomic_store((gu64*)(wsb + go_off), tag16, __ATOMIC_RELAXED, __HIP_MEMORY_SCOPE_AGENT);
  lds_arrive(lds + L_CTRL + 40);
}

constexpr int W2S = 4;
constexpr int HPRE = 5;
constexpr int LEA = 6;
constexpr int LEB = 12;
constexpr int LE0 = 18;
constexpr int LU0 = 11;
constexpr int F1LA = 1;
constexpr int WOCHK = 14 - HPRE - 3;
constexpr int ffn2_tail(int f) { return (f + HPRE < 18 ? 4 : 0) + (f == WOCHK); }
constexpr int ffn2_ops(int f) { return (f + W2S < 14 ? 4 : 0) + ffn2_tail(f); }
constexpr int ffn2_since(int from, int upto) { int n = 0; for (int i = from; i < upto; ++i) n += ffn2_ops(i); return n; }
constexpr int cap63(int n) { return n < 63 ? n : 63; }
constexpr int ffn2_younger(int f, int upto) {
  if (f >= W2S) return cap63(ffn2_tail(f - W2S) + ffn2_since(f - W2S + 1, upto));
  return cap63((f < 2 ? 4 * (1 - f) + 4 * (HPRE - 2) + 8 : 4 * (3 - f)) + ffn2_since(0, upto));
}
constexpr int ffn2_younger_h(int f) {
  if (f >= HPRE) return cap63((f - HPRE == WOCHK) + ffn2_since(f - HPRE + 1, f));
  return cap63(4 * (HPRE - 1 - f) + (f < 2 ? 16 : 8) + ffn2_since(0, f));
}
static_assert(W2S == 4, "slots 0-3");

DI void role_f(const Params& P, LAS unsigned char* lds, const int wt) {
  u64 ep, tag;
  unsigned char* wsb;
  const int lane = TID & 63, r = lane & 31, h = lane >> 5;
  const int b = BID, xcd = b & 7, idx = b >> 3;
  const int rb = (xcd & 1) * 8 + (idx & 7), j = (xcd >> 1) * 4 + (idx >> 3);
  const unsigned lo = lane * 16, lds0 = (unsigned)(uintptr_t)lds;
  const bool ffn = j != 3 && j != 6;
  const int jf = j - (j > 3) - (j > 6);
  f32x16 acc[4];
  const int rot = ROT1(rb, j);
  auto kc = [&](int c) { const int v = c + rot; return v >= 7 ? v - 7 : v; };
  const __amdgpu_buffer_rsrc_t rsX = mk_rsrc(P.x, SEQ * DM * 2);
  constexpr unsigned XP = 8 * DM * 2 - 1024;
  const unsigned xs0 = (unsigned)((rb * 128 + 32 * wt + (lane >> 3)) * (DM * 2) + (((lane & 7) ^ (lane >> 4)) << 4));
  const unsigned xs1 = (xs0 ^ 64u) + XP;
  auto X = [&](int c) { glds16x4(rsX, lds0 + L_X + c * 16384 + wt * 4096, xs0, xs1, xs0 + 2 * XP, xs1 + 2 * XP, kc(c) * 128); };
  X(0);
  SBAR();
  u32x4 wl[28];
  const __amdgpu_buffer_rsrc_t rsL = mk_rsrc(ffn ? P.w1 : P.gw, ffn ? DM * FF * 2 : DM * 2);
  const unsigned lsrc = ffn ? (unsigned)((lane >> 2) * (FF * 2) + (jf * 4 + wt) * 64 + (lane & 3) * 16) : (unsigned)(h * 16);
  const unsigned lstep = ffn ? 16 * FF * 2 : 32;
  auto L = [&](int u0, int u1) {
#pragma unroll
    for (int u = u0; u < u1; ++u) wl[u] = ld16(rsL, lsrc, (kc(u >> 2) * 4 + (u & 3)) * lstep);
    SBAR();
  };
  const bool has_tile = j != 9 && j != 12;
  const int tl = xcd < 4 ? xcd * 32 + idx : 32 + xcd * 24 + idx - ((j & 3) > (xcd < 6 ? 1 : 0) ? 8 : 0);
  int mb = has_tile ? tl / 7 : 0, nb = has_tile ? tl % 7 : 0;
  const int ftl = wt * 14 + ((lane & 15) < 14 ? (lane & 15) : 0);
  unsigned gg_off = GG_OFF + (mb * 64 + lane) * 8, gh_off = GH_OFF + ((mb >> 1) * 16 + (ftl >> 2)) * 128 + (ftl & 3) * 8;
  LAS unsigned char* xad[8];
  auto xfrag = [&](int u, int t) { return *(const LAS bf16x8*)(xad[(u & 3) + (u >> 4) * 4] + ((u >> 2) & 3) * 16384 + t * 4096); };
  LAS unsigned char* tw = lds + L_W1 + wt * 8192 + lo;
  LAS unsigned char* trd = lds + L_W1 + wt * 8192 + (8 * h + ((lane & 15) >> 2)) * 64 + ((lane >> 4) & 1) * 32 + (lane & 3) * 8;
  unsigned hso = lo + HF_OFF + (rb * 4 * 56 + jf * 4 + wt) * 2048;
  {
    const __amdgpu_buffer_rsrc_t rsW = mk_rsrc(P.wqkv, DM * 3 * DM * 2);
    unsigned wsrc[3];
#pragma unroll
    for (int i = 0; i < 3; ++i) {
      const unsigned p = 64 * i + lane, kr = (p * 43691u) >> 19, ch = p - kr * 12;
      wsrc[i] = (16 * wt + kr) * (3 * DM * 2) + j * 168 - (j & 1) * 8 + (ch < 11 ? ch : 10) * 16 - i * 1024;
    }
    auto W = [&](int c) { glds16x3(rsW, lds0 + L_WQ + (c & 3) * 12288 + wt * 3072, wsrc[0], wsrc[1], wsrc[2], kc(c) * 64 * 3 * DM * 2); };
    W(0); X(1); W(1); X(2);
#pragma unroll
    for (int ks = 0; ks < 8; ++ks) {
      xad[ks] = lds + L_X + r * 128 + (((2 * (ks & 3) + h) ^ ((r >> 1) & 7)) << 4) + (ks >> 2) * 65536;
      PINV(xad[ks]);
    }
    PINV(tw); PINV(trd); PINV(hso); PINV(gg_off); PINV(gh_off); PINS(mb); PINS(nb);
    wait_bar<11>();
    W(2); X(3); W(3);
    wait_bar<14>();
    X(4); W(4);
    wait_bar<14>();
    X(5); W(5);
    wait_bar<14>();
    X(6); W(6);
    wait_bar<14>();
    if (wt == 0 && lane < 12) *(LAS unsigned*)(lds + L_CTRL + lane * 4) = 0u;
    L(0, LEA);
    ep = wait_bar_epoch<7 + LEA>(P.ws + FL_OFF);
    L(LEA, LEB);
    tag = (ep + 1) << 16;
    wsb = P.ws + SET_BASE + (((unsigned)ep + 1u) & 3u) * SET_STRIDE;
    PINS(tag); PINS(wsb);
    wait_bar<LEB>();
    L(LEB, LE0);
    bar_lds();
    __builtin_amdgcn_s_setprio(F1PRIO);
  }
  const __amdgpu_buffer_rsrc_t rsWs = mk_rsrc(wsb, WS_END);
  const __amdgpu_buffer_rsrc_t rsW2 = mk_rsrc(P.w2, FF * DM * 2);
  const unsigned w2src = (unsigned)((wt * 448 + (lane >> 3)) * (DM * 2) + nb * 128 + ((((lane >> 2) & 1) ^ ((lane >> 4) & 1)) * 64) + (lane & 3) * 16);
  const unsigned ring0 = lds0 + wt * PAIR;
  constexpr unsigned W2P = 8 * DM * 2 - 1024;
  auto D = [&](int f) { glds16x4(rsW2, ring0 + (f % W2S) * 4096, w2src, w2src + W2P, w2src + 2 * W2P, w2src + 3 * W2P, f * 32 * DM * 2); };
  const unsigned kvsrc = KV_OFF + ((xcd * 2 + (idx & 1)) * 64 + wt * 16) * 4096;
  auto KV3 = [&](int g) {
#pragma unroll
    for (int k = 3 * g; k < (g < 2 ? 3 * g + 3 : 10); ++k) glds_tile_sc1(rsWs, ring0 + k * 4096, lo, kvsrc + (k + NR0) * 4096);
  };
  u64 ggv, ghv;
  auto kv_last = [&]() {
    ggv = ld_granule_raw(wsb, gg_off);
    KV3(2);
    ghv = ld_granule_raw(wsb, gh_off);
    drain();
    PINV(ggv); PINV(ghv);
    lds_arrive(lds + L_CTRL + 20 + wt * 4);
  };
  auto kv_gate = [&]() {
    (void)wait_lds_word(lds + L_CTRL, 4);
    (void)wait_lds_word<2>(lds + L_CTRL + 4 + wt * 4, 1);
  };
  if (ffn) {
#pragma unroll
    for (int i = 0; i < 4; ++i) acc[i] = f32x16{};
    constexpr int LA = F1LA;
    bf16x8 xf[LA + 1][4], wf[LA + 1];
    auto wr = [&](int u) { *(LAS u32x4*)(tw + (u & 7) * 1024) = wl[u]; };
    auto rdw = [&](int u) { wf[u % (LA + 1)] = tr_frag(trd + (u & 7) * 1024, 4 * 64); };
    auto rdx = [&](int u, int t) { xf[u % (LA + 1)][t] = xfrag(u, t); };
    static_assert(LA == 1, "prologue");
    wr(0);
    rdw(0);
    SBAR();
#pragma unroll
    for (int t = 0; t < 4; ++t) rdx(0, t);
    SBAR();
    wr(1); wr(2);
    SBAR();
#pragma unroll
    for (int u = 0; u < 28; ++u) {
#pragma unroll
      for (int t = 0; t < 4; ++t) {
        if (u + LA < 28) {
          if (t == 1) rdw(u + LA);
          rdx(u + LA, t);
          if (t == 3 && u + LA + 2 < 28) wr(u + LA + 2);
        }
        SBAR();
        acc[t] = MFMA(wf[u % (LA + 1)], xf[u % (LA + 1)][t], acc[t]);
        SBAR();
        if (u >= LU0 && LE0 + (u - LU0) * 4 + t < 28) L(LE0 + (u - LU0) * 4 + t, LE0 + (u - LU0) * 4 + t + 1);
      }
    }
    lds_arrive(lds + L_CTRL);
    __builtin_amdgcn_s_setprio(F1PRIO);
    auto silu = [&](int t) {
      f32x16 z = acc[t];
#pragma unroll
      for (int i = 0; i < 16; ++i) z[i] = z[i] * __builtin_amdgcn_rcpf(1.f + __builtin_amdgcn_exp2f(-LOG2E * z[i]));
      st16(rsWs, as_u4(pack8(z, 0)), hso, t * 56 * 2048);
      st16(rsWs, as_u4(pack8(z, 1)), hso, t * 56 * 2048 + 1024);
      SBAR();
    };
    PINV(acc[1]); PINV(acc[2]); PINV(acc[3]);
    silu(0); silu(1); silu(2); silu(3);
    __builtin_amdgcn_s_setprio(F1PRIO);
    kv_gate();
    KV3(0);
    drain();
    if (lane == 0) put_granule(wsb, GH_OFF + (rb * 16 + jf) * 128 + wt * 8, tag, 0);
    lds_arrive(lds + L_CTRL + 20 + wt * 4);
    KV3(1);
    kv_last();
  } else {
    __builtin_amdgcn_s_setprio(F1PRIO);
    if (j == 3) {
      L(LE0, 28);
      acc[0] = f32x16{};
#pragma unroll
      for (int u = 0; u < 28; ++u) {
        acc[0] = MFMA(as_frag(wl[u]), xfrag(u, wt), acc[0]);
      }
      const float g = __builtin_amdgcn_rcpf(1.f + __builtin_amdgcn_exp2f(-LOG2E * acc[0][0]));
      if (h == 0) put_granule(wsb, GG_OFF + ((rb * 4 + wt) * 32 + r) * 8, tag, cvtpk(g, 0.f));
    }
    lds_arrive(lds + L_CTRL);
    kv_gate();
    KV3(0); KV3(1);
    wait_vm<12>();
    lds_arrive(lds + L_CTRL + 20 + wt * 4);
    kv_last();
  }
  if (!has_tile) {
    const __amdgpu_buffer_rsrc_t rsO = mk_rsrc(P.wo, DM * DM * 2);
    const int id0 = ((j == 12) * 16 + rb) * 4 + wt;
    for (int id = id0; id < 224; id += 128) {
      const int nt = id >> 4, hd = id & 15;
      unsigned rp[16];
#pragma unroll
      for (int e = 0; e < 16; ++e)
        rp[e] = __builtin_amdgcn_raw_buffer_load_b16(rsO, (unsigned)(((4 * h + 8 * (e >> 2) + (e & 3)) * DM + r) * 2), (unsigned)((DH * hd * DM + 32 * nt) * 2), 0);
      u32x4 a, c2;
#pragma unroll
      for (int i = 0; i < 4; ++i) {
        a[i] = (rp[2 * i] & 0xffffu) | (rp[2 * i + 1] << 16);
        c2[i] = (rp[8 + 2 * i] & 0xffffu) | (rp[8 + 2 * i + 1] << 16);
      }
      c2[2] = h ? 0u : c2[2];
      c2[3] = h ? 0u : c2[3];
      st16(rsWs, a, lo, WOF_OFF + id * 2048);
      st16(rsWs, c2, lo + 1024, WOF_OFF + id * 2048);
      drain();
      if (lane == 0) put_granule(wsb, GW_OFF + id * 8, tag, 0);
    }
    if (b == 7 && wt == 0) {
#pragma unroll
      for (int i = 0; i < 4; ++i) (void)poll_granule<64>(wsb, GQ_OFF + (i * 64 + lane) * GQW, tag);
      if (lane == 0) __hip_atomic_store((gu64*)(P.ws + FL_OFF), ep + 1, __ATOMIC_RELAXED, __HIP_MEMORY_SCOPE_AGENT);
    }
    return;
  }
  if (!wave_all(tag_in(ggv, tag, 0xffffu))) ggv = poll_granule<8>(wsb, gg_off, tag);
  if (!wave_all(tag_in(ghv, tag, 0xffffu))) (void)poll_granule<8>(wsb, gh_off, tag);
  const float gl = u2f((unsigned)ggv << 16), gs = swap_half(gl);
  const int xm = wt & 1, xn = wt >> 1;
  const float og0 = 1.f - ((h ^ xm) ? gs : gl), og1 = 1.f - ((h ^ xm) ? gl : gs);
  u32x4 hb[HPRE][4], wo[16];
  const unsigned hbase = HF_OFF + (mb * 2 * 56 + wt * 14) * 2048;
  auto H1 = [&](int f, int e) {
    const unsigned so = hbase + (((e >> 1) ^ xm) * 56 + f) * 2048;
    hb[f % HPRE][e] = (e & 1) ? ld16c_raw<1024>(rsWs, lo, so) : ld16c_raw<0>(rsWs, lo, so);
  };
  auto H = [&](int f) {
#pragma unroll
    for (int e = 0; e < 4; ++e) H1(f, e);
  };
  H(0); H(1);
  (void)wait_lds_word<2>(lds + L_CTRL + 4 + wt * 4, 2);
  D(0); D(1);
#pragma unroll
  for (int f = 2; f < HPRE; ++f) H(f);
  (void)wait_lds_word<2>(lds + L_CTRL + 4 + wt * 4, 3);
  D(2); D(3);
  u64 gwv = 0;
  const unsigned gw_off = GW_OFF + ((nb * 2 + ((lane >> 2) & 1)) * 16 + wt * 4 + (lane & 3)) * 8;
  const int q4 = (lane & 15) >> 2;
  unsigned woff[2];
#pragma unroll
  for (int nt = 0; nt < 2; ++nt) woff[nt] = (unsigned)(wt * PAIR + (4 * h + q4) * 128 + ((nt ^ xn ^ (q4 >> 1)) * 64) + ((lane >> 4) & 1) * 32 + (lane & 3) * 8);
#pragma unroll
  for (int i = 0; i < 4; ++i) acc[i] = f32x16{};
  __builtin_amdgcn_s_setprio(0);
  bf16x8 wf[2][2][2];
  auto RW = [&](int f) {
#pragma unroll
    for (int s = 0; s < 2; ++s)
#pragma unroll
      for (int nt = 0; nt < 2; ++nt) wf[f & 1][s][nt] = tr_frag(lds + woff[nt] + (f % W2S) * 4096 + s * 2048, 1024);
  };
  wait_vm<ffn2_younger(0, 0)>();
  RW(0);
  static_for<0, 14>([&](auto fc) {
    constexpr int f = decltype(fc)::value;
    if constexpr (f == 14 - HPRE) {
      PINV(acc[0]); PINV(acc[1]); PINV(acc[2]); PINV(acc[3]);
      wait_vm<ffn2_since(WOCHK + 1, f)>();
      PINV(gwv);
      if (!wave_all(tag_in(gwv, tag, 0xffffu))) (void)poll_granule<8>(wsb, gw_off, tag);
    }
    asm volatile("s_waitcnt lgkmcnt(0)" ::: "memory");
    SBAR();
    {
      constexpr int nh = ffn2_younger_h(f), nw = f + 1 < 14 ? ffn2_younger(f + 1, f) : 63;
      wait_vm<(nh < nw ? nh : nw)>();
      PINV(hb[f % HPRE][0]); PINV(hb[f % HPRE][1]); PINV(hb[f % HPRE][2]); PINV(hb[f % HPRE][3]);
    }
    if constexpr (f + 1 < 14) RW(f + 1);
    SBAR();
#pragma unroll
    for (int u = 0; u < 8; ++u) {
      const int s = u >> 2, nt = (u >> 1) & 1, mt = u & 1;
      acc[nt * 2 + mt] = MFMA(wf[f & 1][s][nt], as_frag(hb[f % HPRE][mt * 2 + s]), acc[nt * 2 + mt]);
      SBAR();
      if (u < 4) {
        if constexpr (f + W2S < 14) { if (u == 0) D(f + W2S); }
      } else {
        const int e = u == 4 ? 0 : u == 5 ? 2 : u == 6 ? 1 : 3;
        if constexpr (f + HPRE < 14) H1(f + HPRE, e);
        else if constexpr (f + HPRE < 18) {
          const int x = (f + HPRE - 14) * 4 + u - 4;
          wo[x] = ld16c_raw<0>(rsWs, lo, WOF_OFF + ((nb * 2 + ((x >> 3) ^ xn)) * 16 + wt * 4) * 2048 + (x & 7) * 1024);
        }
      }
      SBAR();
    }
    if constexpr (f == WOCHK) gwv = ld_granule_raw(wsb, gw_off);
  });
  PINV(acc[0]); PINV(acc[1]); PINV(acc[2]); PINV(acc[3]);
  __builtin_amdgcn_s_setprio(F1PRIO);
#pragma unroll
  for (int i = 0; i < 16; ++i) { acc[0][i] *= og0; acc[1][i] *= og1; acc[2][i] *= og0; acc[3][i] *= og1; }
  PINV(acc[0]); PINV(acc[1]); PINV(acc[2]); PINV(acc[3]);
  const LAS unsigned char *fs1 = lds + (wt ^ 1) * PAIR + 4096 + lo, *fs2 = lds + (wt ^ 2) * PAIR + 2 * 4096 + lo, *fs3 = lds + (wt ^ 3) * PAIR + 3 * 4096 + lo;
  unsigned dst = (unsigned)((((mb * 2 + xm) * 32 + r) * DM + (nb * 2 + xn) * 32 + 8 * h) * 2);
  unsigned oo0 = OF_OFF + ((mb * 2 + xm) * 16 + wt * 4) * 2048, oo1 = OF_OFF + ((mb * 2 + (xm ^ 1)) * 16 + wt * 4) * 2048;
  PINV(fs1); PINV(fs2); PINV(fs3); PINV(dst); PINS(oo0); PINS(oo1);
  (void)wait_lds_word<2>(lds + L_CTRL + 40, 4);
  __builtin_amdgcn_s_sleep(ODELAY);
  (void)poll_granule<POLL_A>(wsb, GO_OFF + ((mb * 2 + ((lane >> 2) & 1)) * 16 + wt * 4 + (lane & 3)) * 128, tag);
#pragma unroll
  for (int e = 0; e < 16; ++e) PINV(wo[e]);
  u32x4 of[16];
#pragma unroll
  for (int e = 0; e < 8; ++e)
#pragma unroll
    for (int mt = 0; mt < 2; ++mt) of[mt * 8 + e] = ld16c(rsWs, lo + (e & 3) * 1024, (mt ? oo1 : oo0) + (e >> 2) * 4096);
  SBAR();
  auto om = [&](int t, int e) { acc[t] = MFMA(as_frag(wo[(t >> 1) * 8 + e]), as_frag(of[(t & 1) * 8 + e]), acc[t]); };
#pragma unroll
  for (int e = 0; e < 8; ++e) {
    if (e < 4) om(0, e);
    om(3, e); om(2, e); om(1, e);
  }
  SBAR();
#pragma unroll
  for (int t = 3; t >= 1; --t) {
    om(0, 7 - t);
    SBAR();
    st_tile(lds + wt * PAIR + t * 4096 + lo, acc[t]);
    SBAR();
  }
  om(0, 7);
  bar_lds();
  f32x16 a = acc[0];
  add_tiles3(a, fs1, fs2, fs3);
#pragma unroll
  for (int k = 0; k < 2; ++k) {
    const auto x = __builtin_amdgcn_permlane32_swap(cvtpk(a[8 * k], a[8 * k + 1]), cvtpk(a[8 * k + 4], a[8 * k + 5]), false, false);
    const auto y = __builtin_amdgcn_permlane32_swap(cvtpk(a[8 * k + 2], a[8 * k + 3]), cvtpk(a[8 * k + 6], a[8 * k + 7]), false, false);
    __builtin_amdgcn_raw_buffer_store_b128(u32x4{x[0], y[0], x[1], y[1]}, mk_rsrc(P.out, SEQ * DM * 2), dst + 32 * k, 0, 0);
  }
}

extern __shared__ __attribute__((aligned(16))) unsigned char g_lds[];

__global__ __launch_bounds__(512, 2) void k_fused(Params P) {
  LAS unsigned char* lds = (LAS unsigned char*)g_lds;
  const int w = __builtin_amdgcn_readfirstlane(TID >> 6);
  unsigned code_word = 0;
  if (w < 4) {
    u64 pc;
    asm volatile("s_getpc_b64 %0" : "=s"(pc));
    code_word = __builtin_amdgcn_raw_buffer_load_b32(mk_rsrc((const void*)(pc & ~63ull), CODE_PF), ((BID >> 3) * 32 + ((TID >> 3) & 24) + (TID & 7)) * 64, 0, 0);
  }
  asm volatile("" ::: "memory");
  if (w < 4) role_a(P, lds, w, code_word);
  else role_f(P, lds, w - 4);
}

#if defined(__HIP_DEVICE_COMPILE__)
#pragma clang attribute pop
#endif

extern "C" void kernel_launch(void* const* d_in, const int* in_sizes, int n_in, void* d_out, int out_size, void* d_ws,
                              size_t ws_size, hipStream_t stream) {
  Params P;
  P.x = (const unsigned short*)d_in[0];
  P.wqkv = (const unsigned short*)d_in[1];
  P.wo = (const unsigned short*)d_in[2];
  P.w1 = (const unsigned short*)d_in[4];
  P.w2 = (const unsigned short*)d_in[5];
  P.gw = (const unsigned short*)d_in[6];
  P.out = (unsigned short*)d_out;
  P.ws = (unsigned char*)d_ws;
  (void)hipFuncSetAttribute((const void*)k_fused, hipFuncAttributeMaxDynamicSharedMemorySize, LDS_BYTES);
  k_fused<<<256, 512, LDS_BYTES, stream>>>(P);
}
```
